```python
import torch

def _softmax(x, axis=-1):
    return torch.softmax(x, dim=axis)
def _gelu(x):
    return torch.nn.functional.gelu(x, approximate="tanh")
def _silu(x):
    return torch.nn.functional.silu(x)
def _sigmoid(x):
    return torch.sigmoid(x)
def _layernorm(x, axis=-1, eps=1e-5):
    m = x.mean(dim=axis, keepdim=True)
    v = x.var(dim=axis, keepdim=True, unbiased=False)
    return (x - m) / torch.sqrt(v + eps)
def _scatter_add(tgt, idx, src, axis=0):
    return tgt.clone().index_add_(axis, idx, src)
def _segment_sum(src, seg, n):
    out = torch.zeros((n,) + src.shape[1:], dtype=src.dtype)
    return out.index_add_(0, seg, src)
def _pad(x, pw):
    flat = tuple(v for pair in reversed(pw) for v in pair)
    return torch.nn.functional.pad(x, flat)

kernel_name = 'sequential_preLN__ssm_conv_s4d__glu_gated_swiglu__prompt'

def setup_inputs():

    _w = (lambda a: (lambda t: t.to(torch.bfloat16) if t.is_floating_point() else t)(torch.as_tensor(a)))
    def _A_setup():
        Bsz, L, D, N = 1, 2048, 448, 256
        return {
            "u": _w((lambda *s: torch.randn(*s, dtype=torch.bfloat16) * 0.05)(Bsz, L, D)),
            "log_A_real": _w((lambda *s: torch.randn(*s, dtype=torch.bfloat16) * 6.4)(D, N) * 0.5),
            "A_imag": _w((lambda *s: torch.randn(*s, dtype=torch.bfloat16) * 0.05)(D, N) * 3.14),
            "C_real": _w((lambda *s: torch.randn(*s, dtype=torch.bfloat16) * 0.05)(D, N) / N ** 0.5),
            "C_imag": _w((lambda *s: torch.randn(*s, dtype=torch.bfloat16) * 0.05)(D, N) / N ** 0.5),
            "log_dt": _w((lambda *s: torch.randn(*s, dtype=torch.bfloat16) * 0.05)(D) * 0.5 - 4.0),
            "D_skip": _w((lambda *s: torch.randn(*s, dtype=torch.bfloat16) * 0.05)(D)),
        }
    _A_in = _A_setup()

    def _B_setup():
        B, S, D, F = 1, 2048, 448, 1792
        return {
            "x": _w((lambda *s: torch.randn(*s, dtype=torch.bfloat16) * 0.05)(B, S, D)),
            "w1": _w((lambda *s: torch.randn(*s, dtype=torch.bfloat16) * 0.4)(D, F) * (2.0 / D) ** 0.5),
            "w3": _w((lambda *s: torch.randn(*s, dtype=torch.bfloat16) * 0.4)(D, F) * (2.0 / D) ** 0.5),
            "w2": _w((lambda *s: torch.randn(*s, dtype=torch.bfloat16) * 0.4)(F, D) * (2.0 / F) ** 0.5),
        }
    _B_in = _B_setup()

    return {
        "A_u": _A_in["u"],
        "A_log_A_real": _A_in["log_A_real"],
        "A_A_imag": _A_in["A_imag"],
        "A_C_real": _A_in["C_real"],
        "A_C_imag": _A_in["C_imag"],
        "A_log_dt": _A_in["log_dt"],
        "A_D_skip": _A_in["D_skip"],
        "B_x": _B_in["x"],
        "B_w1": _B_in["w1"],
        "B_w3": _B_in["w3"],
        "B_w2": _B_in["w2"],
    }

def reference(A_u, A_log_A_real, A_A_imag, A_C_real, A_C_imag, A_log_dt, A_D_skip, B_x, B_w1, B_w3, B_w2):
    def _A_ref(u, log_A_real, A_imag, C_real, C_imag, log_dt, D_skip):
        L = u.shape[1]
        dt = torch.exp(log_dt)
        A = -torch.exp(log_A_real) + 1j * A_imag
        C = (C_real + 1j * C_imag) * dt[:, None]
        pos = (lambda x, dt: x.to(dt))(torch.arange(L), log_dt.dtype)
        P = torch.exp((A * dt[:, None])[:, :, None] * pos[None, None, :])
        K = 2.0 * torch.einsum("dn,dnl->dl", C, P).real
        L2 = 2 * L
        u_t = (lambda x, *d: x.permute(*d))(u, 0, 2, 1)
        Kf = (lambda x, *a, **k: torch.fft.rfft(x.float(), *a, **k))(K, L2)
        Uf = (lambda x, *a, **k: torch.fft.rfft(x.float(), *a, **k))(u_t, L2)
        y = (lambda x, *a, **k: torch.fft.irfft(x, *a, **k).to(torch.bfloat16))(Uf * Kf[None, :, :], L2)[:, :, :L]
        y = (lambda x, *d: x.permute(*d))(y, 0, 2, 1)
        return y + u * D_skip[None, None, :]

    def _B_ref(x, w1, w3, w2):
        gate = torch.einsum("bsd,df->bsf", x, w1)
        up = torch.einsum("bsd,df->bsf", x, w3)
        h = _silu(gate) * up
        out = torch.einsum("bsf,fd->bsd", h, w2)
        return out

    x = A_u
    h = x + _A_ref(_layernorm(x, axis=-1), A_log_A_real, A_A_imag, A_C_real, A_C_imag, A_log_dt, A_D_skip)
    h2 = h + _B_ref(_layernorm(h, axis=-1), B_w1, B_w3, B_w2)
    return h2
```

```cpp
#include <hip/hip_runtime.h>
#include <hip/hip_bf16.h>
#include <stdint.h>

constexpr int L = 2048, D = 448, N = 256, F = 1792;
typedef unsigned short u16;
__device__ __forceinline__ float bf2f(u16 v) { return __uint_as_float(((unsigned)v) << 16); }
__device__ __forceinline__ u16 f2bf(float f) { unsigned u = __float_as_uint(f); return (u16)((u + 0x7FFF + ((u >> 16) & 1)) >> 16); }
__device__ __forceinline__ float rbf(float f) { return bf2f(f2bf(f)); }

__global__ void ln_kernel(const u16* x, u16* u) {
  int t = blockIdx.x, lane = threadIdx.x;
  float v[7]; float s = 0;
  for (int i = 0; i < 7; i++) { v[i] = bf2f(x[t * D + lane + 64 * i]); s += v[i]; }
  for (int o = 32; o; o >>= 1) s += __shfl_xor(s, o);
  float m = s / D; float q = 0;
  for (int i = 0; i < 7; i++) q += (v[i] - m) * (v[i] - m);
  for (int o = 32; o; o >>= 1) q += __shfl_xor(q, o);
  float r = rsqrtf(q / D + 1e-5f);
  for (int i = 0; i < 7; i++) u[t * D + lane + 64 * i] = f2bf((v[i] - m) * r);
}
__global__ void kgen_kernel(const u16* logA, const u16* Aim, const u16* Cre, const u16* Cim, const u16* logdt, float* K) {
  int d = blockIdx.x;
  __shared__ float ar[N], ai[N], cr[N], ci[N];
  float dt = rbf(expf(bf2f(logdt[d])));
  int n = threadIdx.x;
  ar[n] = -rbf(expf(bf2f(logA[d * N + n]))) * dt; ai[n] = bf2f(Aim[d * N + n]) * dt;
  cr[n] = bf2f(Cre[d * N + n]) * dt; ci[n] = bf2f(Cim[d * N + n]) * dt;
  __syncthreads();
  for (int l = threadIdx.x; l < L; l += 256) {
    float p = rbf((float)l); float acc = 0;
    for (int k = 0; k < N; k++) { float e = __expf(ar[k] * p); float sn, cs; __sincosf(ai[k] * p, &sn, &cs); acc += e * (cr[k] * cs - ci[k] * sn); }
    K[d * L + l] = 2.f * acc;
  }
}
__global__ void conv_kernel(const u16* x, const u16* u, const float* K, const u16* Dskip, u16* h) {
  int d = blockIdx.x;
  __shared__ float us[L], ks[L];
  for (int l = threadIdx.x; l < L; l += 256) { us[l] = bf2f(u[l * D + d]); ks[l] = K[d * L + l]; }
  __syncthreads();
  float ds = bf2f(Dskip[d]);
  for (int t = threadIdx.x; t < L; t += 256) {
    float acc = 0;
    for (int l = 0; l <= t; l++) acc += ks[l] * us[t - l];
    h[t * D + d] = f2bf(bf2f(x[t * D + d]) + acc + us[t] * ds);
  }
}
__global__ void g1_kernel(const u16* hn, const u16* w1, const u16* w3, u16* H) {
  int t0 = blockIdx.y * 8; int f = blockIdx.x * 256 + threadIdx.x;
  __shared__ float a[8][D];
  for (int i = threadIdx.x; i < 8 * D; i += 256) a[i / D][i % D] = bf2f(hn[t0 * D + i]);
  __syncthreads();
  float g[8] = {0}, up[8] = {0};
  for (int k = 0; k < D; k++) { float b1 = bf2f(w1[k * F + f]), b3 = bf2f(w3[k * F + f]);
    for (int i = 0; i < 8; i++) { g[i] += a[i][k] * b1; up[i] += a[i][k] * b3; } }
  for (int i = 0; i < 8; i++) H[(t0 + i) * F + f] = f2bf(g[i] / (1.f + __expf(-g[i])) * up[i]);
}
__global__ void g2_kernel(const u16* H, const u16* w2, const u16* h, u16* out) {
  int t0 = blockIdx.x * 4; int d = threadIdx.x;
  __shared__ float a[4][F];
  for (int i = threadIdx.x; i < 4 * F; i += 448) a[i / F][i % F] = bf2f(H[t0 * F + i]);
  __syncthreads();
  float o[4] = {0};
  for (int k = 0; k < F; k++) { float b = bf2f(w2[k * D + d]); for (int i = 0; i < 4; i++) o[i] += a[i][k] * b; }
  for (int i = 0; i < 4; i++) out[(t0 + i) * D + d] = f2bf(o[i] + bf2f(h[(t0 + i) * D + d]));
}

extern "C" void kernel_launch(void* const* d_in, const int* in_sizes, int n_in, void* d_out, int out_size, void* d_ws, size_t ws_size, hipStream_t stream) {
  const u16* x = (const u16*)d_in[0];
  char* ws = (char*)d_ws;
  u16* u = (u16*)ws; float* K = (float*)(ws + (2 << 20)); u16* h = (u16*)(ws + (8 << 20)); u16* hn = (u16*)(ws + (10 << 20)); u16* H = (u16*)(ws + (12 << 20));
  ln_kernel<<<L, 64, 0, stream>>>(x, u);
  kgen_kernel<<<D, 256, 0, stream>>>((const u16*)d_in[1], (const u16*)d_in[2], (const u16*)d_in[3], (const u16*)d_in[4], (const u16*)d_in[5], K);
  conv_kernel<<<D, 256, 0, stream>>>(x, u, K, (const u16*)d_in[6], h);
  ln_kernel<<<L, 64, 0, stream>>>(h, hn);
  g1_kernel<<<dim3(F / 256, L / 8), 256, 0, stream>>>(hn, (const u16*)d_in[8], (const u16*)d_in[9], H);
  g2_kernel<<<L / 4, 448, 0, stream>>>(H, (const u16*)d_in[10], h, (u16*)d_out);
}
```

```cpp
#include <hip/hip_runtime.h>
#include <hip/hip_bf16.h>
#include <stdint.h>

typedef unsigned short u16;
typedef unsigned u32;
typedef __attribute__((ext_vector_type(2))) float f32x2;
typedef __attribute__((ext_vector_type(4))) float f32x4;
typedef __attribute__((ext_vector_type(16))) float f32x16;
typedef __attribute__((ext_vector_type(2))) u32 u32x2;
typedef __attribute__((ext_vector_type(4))) u32 u32x4;
typedef __attribute__((ext_vector_type(2))) _Float16 f16x2;
typedef __attribute__((ext_vector_type(8))) _Float16 f16x8;
typedef __attribute__((ext_vector_type(2))) __bf16 bf16x2;
typedef __attribute__((ext_vector_type(8))) __bf16 bf16x8;
typedef __attribute__((ext_vector_type(4))) short s16x4;
#define LAS __attribute__((address_space(3)))
#define DI __device__ __forceinline__

constexpr int L = 2048, D = 448, NM = 256, F = 1792;
constexpr u32 OFF_UG = 4096, SZ_G = 256 * 14336;
constexpr u32 OFF_XG = OFF_UG + SZ_G;
constexpr u32 OFF_W13 = OFF_XG + SZ_G;
constexpr u32 OFF_W2 = OFF_W13 + 56 * 57344;
constexpr u32 OFF_SG = OFF_W2 + 28 * 57344;
constexpr u32 OFF_HT = OFF_SG + 3584 * 8;
constexpr u32 OFF_HF = OFF_HT + 64 * 28672;
constexpr u32 OFF_DBG = OFF_HF + 64 * 114688;

DI float bflo(u32 v) { return __uint_as_float(v << 16); }
DI float bfhi(u32 v) { return __uint_as_float(v & 0xffff0000u); }
DI u32 pk_bf16(float a, float b) { f32x2 v = {a, b}; return __builtin_bit_cast(u32, __builtin_convertvector(v, bf16x2)); }
DI u32 pk_f16(float a, float b) { f32x2 v = {a, b}; return __builtin_bit_cast(u32, __builtin_convertvector(v, f16x2)); }
DI float rbf(float a) { return bflo(pk_bf16(a, a)); }
DI u32x2 tr_read(const char* p) { return __builtin_bit_cast(u32x2, __builtin_amdgcn_ds_read_tr16_b64_v4i16((LAS s16x4*)p)); }
DI u32 perm(u32 s0, u32 s1, u32 sel) { return __builtin_amdgcn_perm(s0, s1, sel); }

struct P {
  const u16 *x, *logA, *Aim, *Cre, *Cim, *logdt, *Dskip, *w1, *w3, *w2;
  u16* out; char* ws;
};

__global__ void __launch_bounds__(256, 1) k1(P p) {
  __shared__ __attribute__((aligned(16))) char lds[40960];
  const int b = blockIdx.x, tid = threadIdx.x, lane = tid & 63, w = tid >> 6;
  const int xcd = b & 7, slot = b >> 3;
  const u32 tag = 1;
  {
    const int hf = lane >> 5, li = lane & 31, row = 8 * b + 2 * w + hf;
    u32x4 v0 = {0, 0, 0, 0}, v1 = {0, 0, 0, 0};
    if (li < 28) { const u32x4* s = (const u32x4*)(p.x + row * D + 16 * li); v0 = s[0]; v1 = s[1]; }
    float f[16];
    for (int i = 0; i < 4; i++) { f[2 * i] = bflo(v0[i]); f[2 * i + 1] = bfhi(v0[i]); f[8 + 2 * i] = bflo(v1[i]); f[9 + 2 * i] = bfhi(v1[i]); }
    float s = 0;
    for (int i = 0; i < 16; i++) s += f[i];
    for (int o = 16; o; o >>= 1) s += __shfl_xor(s, o);
    const float m = s * (1.f / D);
    float q = 0;
    for (int i = 0; i < 16; i++) { f[i] -= m; q += f[i] * f[i]; }
    if (li >= 28) q = 0;
    for (int o = 16; o; o >>= 1) q += __shfl_xor(q, o);
    const float r = __builtin_amdgcn_rsqf(q * (1.f / D) + 1e-5f);
    u32x4 u0, u1;
    for (int i = 0; i < 4; i++) { u0[i] = pk_f16(f[2 * i] * r, f[2 * i + 1] * r); u1[i] = pk_f16(f[8 + 2 * i] * r, f[9 + 2 * i] * r); }
    if (li < 28) {
      char* d = lds + (2 * w + hf) * 912 + 32 * li;
      *(u32x4*)d = u0; *(u32x4*)(d + 16) = u1;
      *(u32x4*)(d + 7296) = v0; *(u32x4*)(d + 7296 + 16) = v1;
    }
    __syncthreads();
    for (int i = 0; i < 4; i++) {
      const int id = i * 256 + tid;
      if (id < 896) {
        const int c = id >> 2, tp = id & 3;
        const u32* s0 = (const u32*)(lds + 2 * tp * 912 + 4 * c);
        u32x4 g = {s0[0], tag, s0[228], tag};
        *(u32x4*)(p.ws + OFF_UG + b * 14336 + id * 16) = g;
        u32x4 gx = {s0[1824], tag, s0[1824 + 228], tag};
        *(u32x4*)(p.ws + OFF_XG + b * 14336 + id * 16) = gx;
      }
    }
  }
  char* stg = lds + 16384 + w * 5120;
  const int gam = lane >> 4, q4 = (lane & 15) >> 2, p4 = lane & 3;
  if (slot < 28) {
    const int v = 4 * (28 * xcd + slot) + w;
    if (v < 784) {
      const int phi = v / 56, mat = (v / 28) & 1, s = v % 28;
      const u16* W = mat ? p.w3 : p.w1;
      u32x4 r[4];
      for (int i = 0; i < 4; i++) r[i] = *(const u32x4*)(W + (16 * s + 4 * i + (lane >> 4)) * F + 128 * phi + 8 * (lane & 15));
      for (int i = 0; i < 4; i++) *(u32x4*)(stg + (4 * i + (lane >> 4)) * 320 + 16 * (lane & 15)) = r[i];
      __builtin_amdgcn_fence(__ATOMIC_RELEASE, "wavefront"); __builtin_amdgcn_wave_barrier();
      for (int wq = 0; wq < 4; wq++) {
        const char* a = stg + (8 * (gam >> 1) + q4) * 320 + (32 * wq + 16 * (gam & 1) + 4 * p4) * 2;
        u32x2 lo = tr_read(a), hi = tr_read(a + 4 * 320);
        u32x4 fr = {lo[0], lo[1], hi[0], hi[1]};
        *(u32x4*)(p.ws + OFF_W13 + ((((phi * 4 + wq) * 2 + mat) * 28 + s) << 10) + 16 * lane) = fr;
      }
    }
  } else {
    const int sp = 4 * xcd + slot - 28;
    if (sp < 28) {
      const int dl = sp >> 2, wq = sp & 3;
      for (int kk = w; kk < 28; kk += 4) {
        u32x4 r[2];
        for (int i = 0; i < 2; i++) r[i] = *(const u32x4*)(p.w2 + (16 * (28 * wq + kk) + 8 * i + (lane >> 3)) * D + 64 * dl + 8 * (lane & 7));
        for (int i = 0; i < 2; i++) *(u32x4*)(stg + (8 * i + (lane >> 3)) * 192 + 16 * (lane & 7)) = r[i];
        __builtin_amdgcn_fence(__ATOMIC_RELEASE, "wavefront"); __builtin_amdgcn_wave_barrier();
        for (int m = 0; m < 2; m++) {
          const char* a = stg + (4 * (gam >> 1) + q4) * 192 + (32 * m + 16 * (gam & 1) + 4 * p4) * 2;
          u32x2 lo = tr_read(a), hi = tr_read(a + 8 * 192);
          u32x4 fr = {lo[0], lo[1], hi[0], hi[1]};
          *(u32x4*)(p.ws + OFF_W2 + (((sp * 28 + kk) * 2 + m) << 10) + 16 * lane) = fr;
        }
        __builtin_amdgcn_fence(__ATOMIC_RELEASE, "wavefront"); __builtin_amdgcn_wave_barrier();
      }
      const int phi = sp >> 1, mat = sp & 1;
      const u16* W = mat ? p.w3 : p.w1;
      float acc[8] = {0, 0, 0, 0, 0, 0, 0, 0};
      #pragma unroll 7
      for (int i = 0; i < 28; i++) {
        u32x4 r = *(const u32x4*)(W + ((tid >> 4) + 16 * i) * F + 128 * phi + 8 * (tid & 15));
        for (int j = 0; j < 4; j++) { acc[2 * j] += bflo(r[j]); acc[2 * j + 1] += bfhi(r[j]); }
      }
      for (int j = 0; j < 8; j++) { acc[j] += __shfl_xor(acc[j], 16); acc[j] += __shfl_xor(acc[j], 32); }
      __syncthreads();
      float* red = (float*)lds;
      if (lane < 16) for (int j = 0; j < 8; j++) red[w * 128 + 8 * lane + j] = acc[j];
      __syncthreads();
      if (tid < 128) {
        float s = red[tid] + red[128 + tid] + red[256 + tid] + red[384 + tid];
        u32x2 g = {__float_as_uint(s), tag};
        *(u32x2*)(p.ws + OFF_SG + (mat * F + 128 * phi + tid) * 8) = g;
      }
    }
  }
}

DI f32x2 cmul(f32x2 z, f32x2 w) { f32x2 t = z.xx * w; f32x2 w2 = {-w.y, w.x}; return __builtin_elementwise_fma(z.yy, w2, t); }
DI f32x2 cexp_(float arl2, float rev) {
  float e = __builtin_amdgcn_exp2f(arl2); float fr = __builtin_amdgcn_fractf(rev);
  f32x2 r = {e * __builtin_amdgcn_cosf(fr), e * __builtin_amdgcn_sinf(fr)}; return r;
}
constexpr int KROW = 1056, KCH = 53 * KROW;
constexpr int LDS_T2 = 2 * KCH;
constexpr int T2CH = 672 * 4;
constexpr int LDS_U = LDS_T2 + 2 * T2CH;
constexpr int UCH = 4608;
constexpr int LDS_X = LDS_U + 2 * UCH;
constexpr int LDS2_END = LDS_X + 2 * 4096;
DI int eidx(int lam) {
  return (int)(pk_bf16((float)lam, 0.f) & 0xffffu) - 0x4300 + 128;
}
__global__ void __launch_bounds__(256, 1) k2(P p) {
  __shared__ __attribute__((aligned(16))) char lds[LDS2_END];
  const int b = blockIdx.x, tid = threadIdx.x, lane = tid & 63, w = tid >> 6;
  const int xcd = b & 7, slot = b >> 3;
  if (slot >= 28) return;
  const int c2 = 28 * xcd + slot, ch = w >> 1, half = w & 1, d = 2 * c2 + ch;
  const int n0 = d * NM + 128 * half + 2 * lane, n1 = d * NM + 128 * (half ^ 1) + 2 * lane;
  const u32 vla = *(const u32*)(p.logA + n0), vai = *(const u32*)(p.Aim + n0), vcr = *(const u32*)(p.Cre + n0), vci = *(const u32*)(p.Cim + n0);
  const u32 ocr = *(const u32*)(p.Cre + n1), oci = *(const u32*)(p.Cim + n1);
  const float dt = rbf(__expf(bflo(p.logdt[d])));
  const float dsk = bflo(p.Dskip[d]);
  u32 mx = 0;
  { u32 a[4] = {vcr, vci, ocr, oci}; for (int i = 0; i < 4; i++) { mx = max(mx, (a[i] << 1) & 0xff00u); mx = max(mx, (a[i] >> 15) & 0xff00u); } }
  for (int o = 32; o; o >>= 1) mx = max(mx, (u32)__shfl_xor((int)mx, o));
  const u32 ex = min(max(mx >> 8, 1u), 253u);
  const float scale = __uint_as_float((254u - ex) << 23), yscale = 2.f * dt * __uint_as_float(ex << 23);
  f32x2 w1[2], w8[2], w256[2], G[2], E[2];
  for (int i = 0; i < 2; i++) {
    const float la = i ? bfhi(vla) : bflo(vla), ai = (i ? bfhi(vai) : bflo(vai)) * dt;
    const float ar = -rbf(__expf(la)) * dt;
    const float a2 = ar * 1.44269504089f, rv = ai * 0.15915494309f;
    w1[i] = cexp_(a2, rv); w8[i] = cexp_(8.f * a2, 8.f * rv); w256[i] = cexp_(256.f * a2, 256.f * rv);
    w1[i].y = -w1[i].y; w256[i].y = -w256[i].y;
    G[i].x = (i ? bfhi(vcr) : bflo(vcr)) * scale; G[i].y = -(i ? bfhi(vci) : bflo(vci)) * scale;
  }
  char* kb = lds + ch * KCH + 512 * half + 8 * lane;
  auto put = [&](int row, f32x2 a, f32x2 bb) { u32x2 v = {pk_f16(a.x, a.y), pk_f16(bb.x, bb.y)}; *(u32x2*)(kb + row * KROW) = v; };
  {
    f32x2 g[2][8];
    for (int i = 0; i < 2; i++) { g[i][0] = G[i]; for (int e = 1; e < 8; e++) g[i][e] = cmul(g[i][e - 1], w1[i]); }
    for (int e = 0; e < 8; e++) put(e, g[0][e], g[1][e]);
    f32x2 z0 = g[0][0], z1 = g[1][0];
    const int rows0[8] = {8, 12, 14, 16, 17, 18, 19, 20};
    for (int k = 0; k < 8; k++) { z0 = cmul(z0, w256[0]); z1 = cmul(z1, w256[1]); put(rows0[k], z0, z1); }
    put(9, cmul(g[0][2], w256[0]), cmul(g[1][2], w256[1]));
    put(11, cmul(g[0][6], w256[0]), cmul(g[1][6], w256[1]));
    z0 = g[0][4]; z1 = g[1][4];
    const int rows4[3] = {10, 13, 15};
    for (int k = 0; k < 3; k++) { z0 = cmul(z0, w256[0]); z1 = cmul(z1, w256[1]); put(rows4[k], z0, z1); }
    f32x2 one = {1.f, 0.f};
    put(21, one, one);
    E[0] = w8[0]; E[1] = w8[1];
    put(22, E[0], E[1]);
    for (int j = 2; j < 32; j++) { E[0] = cmul(E[0], w8[0]); E[1] = cmul(E[1], w8[1]); put(21 + j, E[0], E[1]); }
  }
  for (int i = 0; i < 4; i++) {
    const u32 off = (i * 64 + (tid >> 2)) * 14336 + c2 * 64 + (tid & 3) * 16;
    u32x4 g = *(const u32x4*)(p.ws + OFF_UG + off), gx = *(const u32x4*)(p.ws + OFF_XG + off);
    u32* us = (u32*)(lds + LDS_U + 512) + 256 * i + tid;
    us[0] = perm(g[2], g[0], 0x05040100); us[UCH / 4] = perm(g[2], g[0], 0x07060302);
    u32* xs = (u32*)(lds + LDS_X) + 256 * i + tid;
    xs[0] = perm(gx[2], gx[0], 0x05040100); xs[1024] = perm(gx[2], gx[0], 0x07060302);
  }
  { u32* z = (u32*)(lds + LDS_U + (tid >> 7) * UCH); z[tid & 127] = 0; }
  if (tid < 32) { u32* z = (u32*)(lds + LDS_T2 + (tid >> 4) * T2CH); z[tid & 15] = 0; }
  __syncthreads();
  const int r = lane & 15, g = lane >> 4;
  {
    f32x4 d0 = {0, 0, 0, 0}, d1 = {0, 0, 0, 0};
    const char* a0 = lds + ch * KCH + r * KROW + 16 * g;
    const char* a1 = lds + ch * KCH + min(16 + r, 20) * KROW + 16 * g;
    const char* bb = lds + ch * KCH + (21 + 16 * half + r) * KROW + 16 * g;
    for (int s = 0; s < 16; s++) {
      f16x8 fa0 = *(const f16x8*)(a0 + 64 * s), fa1 = *(const f16x8*)(a1 + 64 * s), fb = *(const f16x8*)(bb + 64 * s);
      d0 = __builtin_amdgcn_mfma_f32_16x16x32_f16(fa0, fb, d0, 0, 0, 0);
      d1 = __builtin_amdgcn_mfma_f32_16x16x32_f16(fa1, fb, d1, 0, 0, 0);
    }
    const int j = 16 * half + r;
    u16* t2 = (u16*)(lds + LDS_T2 + ch * T2CH + 64);
    for (int rho = 0; rho < 4; rho++) {
      int e0 = g < 2 ? 8 * j + 4 * g + rho : (g == 2 ? 256 + 4 * j + rho : 384 + 64 * (rho >> 1) + 2 * j + (rho & 1));
      u16 v = (u16)pk_f16(d0[rho], 0.f);
      t2[2 * e0 + 1] = v; t2[2 * e0 - 2] = v;
      int e1 = g == 0 ? 512 + 32 * rho + j : ((g == 1 && rho == 0 && j == 0) ? 640 : 648);
      u16 v1 = (u16)pk_f16(d1[rho], 0.f);
      t2[2 * e1 + 1] = v1; t2[2 * e1 - 2] = v1;
    }
  }
  __syncthreads();
  u32x4 Fr[32];
  {
    const u32* t2 = (const u32*)(lds + LDS_T2 + ch * T2CH + 64);
    const int lamL = 16 + r - 8 * g + 32 * half;
    #pragma unroll
    for (int q = 0; q < 4; q++) for (int jj = 0; jj < 4; jj++) Fr[q][jj] = t2[64 * q + lamL - 2 * jj - 1];
    auto generic = [&](int q) { u32x4 f; for (int jj = 0; jj < 4; jj++) { int lam = 64 * q + lamL - 2 * jj; int a = eidx(lam - 1), bq = eidx(lam); u32 v = t2[a]; f[jj] = perm(v, v, a == bq ? 0x03020302u : 0x03020100u); } return f; };
    Fr[4] = generic(4); Fr[8] = generic(8); Fr[16] = generic(16);
    {
      int a[2]; u32 sel[2];
      for (int jj = 0; jj < 2; jj++) { int lam = 64 * 5 + lamL - 2 * jj; a[jj] = eidx(lam - 1); sel[jj] = a[jj] == eidx(lam) ? 0x03020302u : 0x03020100u; }
      #pragma unroll
      for (int q = 5; q < 8; q++) for (int jj = 0; jj < 4; jj++) { u32 v = t2[a[jj & 1] + 32 * (q - 5) - 2 * (jj >> 1)]; Fr[q][jj] = perm(v, v, sel[jj & 1]); }
    }
    {
      const int a3 = eidx(64 * 9 + lamL - 7); u32 sel[4];
      for (int jj = 0; jj < 4; jj++) { int lam = 64 * 9 + lamL - 2 * jj; u32 el = eidx(lam) - a3, eh = eidx(lam - 1) - a3;
        u32 bl = ((el ^ 1) << 1), bh = ((eh ^ 1) << 1); sel[jj] = bl | ((bl + 1) << 8) | (bh << 16) | ((bh + 1) << 24); }
      #pragma unroll
      for (int q = 9; q < 16; q++) { u32 v0 = t2[a3 + 16 * (q - 9)], v1 = t2[a3 + 16 * (q - 9) + 2]; for (int jj = 0; jj < 4; jj++) Fr[q][jj] = perm(v1, v0, sel[jj]); }
    }
    {
      const int a3 = eidx(64 * 17 + lamL - 7); u32 sel[4];
      for (int jj = 0; jj < 4; jj++) { int lam = 64 * 17 + lamL - 2 * jj; u32 el = eidx(lam) - a3, eh = eidx(lam - 1) - a3;
        u32 bl = ((el ^ 1) << 1), bh = ((eh ^ 1) << 1); sel[jj] = bl | ((bl + 1) << 8) | (bh << 16) | ((bh + 1) << 24); }
      #pragma unroll
      for (int q = 17; q < 32; q++) { u32 v0 = t2[a3 + 8 * (q - 17)]; for (int jj = 0; jj < 4; jj++) Fr[q][jj] = perm(v0, v0, sel[jj]); }
    }
  }
  f32x4 acc[8];
  for (int c = 0; c < 8; c++) acc[c] = f32x4{0, 0, 0, 0};
  const char* ub = lds + LDS_U + ch * UCH + 512 + 32 * (r - 2 * half - 1) + 16 * g;
  #pragma unroll
  for (int sg = -3; sg <= 28; sg++) {
    f16x8 fb = *(const f16x8*)(ub + 128 * sg);
    #pragma unroll
    for (int c = 0; c < 8; c++) { const int q = 4 * c - sg; if (q >= 0 && q < 4 * c + 4) acc[c] = __builtin_amdgcn_mfma_f32_16x16x32_f16(__builtin_bit_cast(f16x8, Fr[q]), fb, acc[c], 0, 0, 0); }
  }
  char* pb = lds + (ch * 2 + half) * 8 * 1152;
  for (int c = 0; c < 8; c++) *(f32x4*)(pb + c * 1152 + 16 * lane + 128 * (g >> 1)) = acc[c];
  __syncthreads();
  for (int st = 0; st < 2; st++) {
    const int stage = 2 * st + half, l5 = lane & 31, c = 2 * stage + (lane >> 5), ii = l5 >> 1, g0 = 2 * (l5 & 1);
    const char* p0 = lds + (ch * 2) * 8 * 1152 + c * 1152 + 16 * (ii + 16 * g0) + 128 * (l5 & 1);
    f32x4 a0 = *(const f32x4*)p0, a1 = *(const f32x4*)(p0 + 256), b0 = *(const f32x4*)(p0 + 8 * 1152), b1 = *(const f32x4*)(p0 + 8 * 1152 + 256);
    const int t0 = 512 * stage + 8 * lane;
    f16x8 uu = *(const f16x8*)(lds + LDS_U + ch * UCH + 512 + 2 * t0);
    u32x4 xx = *(const u32x4*)(lds + LDS_X + ch * 4096 + 2 * t0);
    float y[8];
    for (int i = 0; i < 4; i++) { y[i] = a0[i] + b0[i]; y[4 + i] = a1[i] + b1[i]; }
    u32x4 o;
    for (int i = 0; i < 4; i++) {
      float h0 = y[2 * i] * yscale + ((float)uu[2 * i] * dsk + bflo(xx[i])), h1 = y[2 * i + 1] * yscale + ((float)uu[2 * i + 1] * dsk + bfhi(xx[i]));
      o[i] = pk_bf16(h0, h1);
    }
    *(u32x4*)(p.ws + OFF_HT + (16 * stage + (lane >> 2)) * 28672 + d * 64 + (lane & 3) * 16) = o;
  }
}

__global__ void __launch_bounds__(256, 1) k3(P p) {
  __shared__ __attribute__((aligned(16))) char lds[28672 + 2048 + 1024];
  const int b = blockIdx.x, tid = threadIdx.x, lane = tid & 63, w = tid >> 6;
  const int xcd = b & 7, slot = b >> 3;
  if (slot >= 28) return;
  const int mb = 4 * (xcd >> 1) + slot / 7, phi = 7 * (xcd & 1) + slot % 7;
  u32x4 Wf[56];
  #pragma unroll
  for (int i = 0; i < 56; i++) Wf[i] = *(const u32x4*)(p.ws + OFF_W13 + (((phi * 4 + w) * 56 + i) << 10) + 16 * lane);
  float* sl = (float*)(lds + 28672 + 2048);
  sl[tid] = *(const float*)(p.ws + OFF_SG + ((tid >> 7) * F + 128 * phi + (tid & 127)) * 8);
  __syncthreads();
  const int h = lane >> 5, gam = lane >> 4, q4 = (lane & 15) >> 2, p4 = lane & 3;
  f32x4 Sg[4], Su[4];
  for (int a = 0; a < 4; a++) { Sg[a] = *(const f32x4*)(sl + 32 * w + 8 * a + 4 * h); Su[a] = *(const f32x4*)(sl + 128 + 32 * w + 8 * a + 4 * h); }
  const char* bb = lds + (8 * (gam >> 1) + q4) * 64 + (16 * (gam & 1) + 4 * p4) * 2;
  #pragma unroll 1
  for (int j = 0; j < 4; j++) {
    const int tau = mb + 16 * j;
    __syncthreads();
    for (int i = 0; i < 7; i++) *(u32x4*)(lds + (i * 256 + tid) * 16) = *(const u32x4*)(p.ws + OFF_HT + tau * 28672 + (i * 256 + tid) * 16);
    __syncthreads();
    f32x16 ag, au;
    for (int i = 0; i < 16; i++) { ag[i] = 0; au[i] = 0; }
    float s1 = 0, s2 = 0;
    const bf16x2 ones = __builtin_bit_cast(bf16x2, 0x3f803f80u);
    #pragma unroll
    for (int s = 0; s < 28; s++) {
      u32x2 lo = tr_read(bb + 1024 * s), hi = tr_read(bb + 1024 * s + 256);
      u32x4 fb = {lo[0], lo[1], hi[0], hi[1]};
      ag = __builtin_amdgcn_mfma_f32_32x32x16_bf16(__builtin_bit_cast(bf16x8, Wf[s]), __builtin_bit_cast(bf16x8, fb), ag, 0, 0, 0);
      au = __builtin_amdgcn_mfma_f32_32x32x16_bf16(__builtin_bit_cast(bf16x8, Wf[28 + s]), __builtin_bit_cast(bf16x8, fb), au, 0, 0, 0);
      if ((s & 3) == w) for (int i = 0; i < 4; i++) { const u32 tv = fb[i]; bf16x2 v = __builtin_bit_cast(bf16x2, tv); s1 = __builtin_amdgcn_fdot2_f32_bf16(v, ones, s1, false); s2 = __builtin_amdgcn_fdot2_f32_bf16(v, v, s2, false); }
    }
    f32x2* st = (f32x2*)(lds + 28672);
    st[w * 64 + lane] = f32x2{s1, s2};
    __syncthreads();
    f32x2 tot = {0, 0};
    for (int i = 0; i < 8; i++) tot += st[32 * i + (lane & 31)];
    const float m = tot.x * (1.f / D), var = fmaxf(tot.y * (1.f / D) - m * m, 0.f) + 1e-5f;
    const float rs = __builtin_amdgcn_rsqf(var), nl = -rs * 1.44269504089f;
    u32 ho[8];
    for (int i = 0; i < 16; i += 2) {
      float o[2];
      for (int e = 0; e < 2; e++) {
        const int rho = i + e;
        const float t1 = ag[rho] - m * Sg[rho >> 2][rho & 3], t2 = au[rho] - m * Su[rho >> 2][rho & 3];
        const float ex = __builtin_amdgcn_exp2f(t1 * nl);
        o[e] = t1 * t2 * __builtin_amdgcn_rcpf(ex * var + var);
      }
      ho[i >> 1] = pk_bf16(o[0], o[1]);
    }
    for (int s = 0; s < 2; s++) *(u32x4*)(p.ws + OFF_HF + tau * 114688 + ((8 * phi + 2 * w + s) << 10) + 16 * lane) = u32x4{ho[4 * s], ho[4 * s + 1], ho[4 * s + 2], ho[4 * s + 3]};
  }
}

__global__ void __launch_bounds__(256, 1) k4(P p) {
  __shared__ __attribute__((aligned(16))) char lds[2 * 4 * 8192];
  const int b = blockIdx.x, tid = threadIdx.x, lane = tid & 63, w = tid >> 6;
  const int xcd = b & 7, slot = b >> 3;
  if (slot >= 28) return;
  const int tb = 4 * xcd + slot / 7, dl = slot % 7, mb = tb >> 1, e = tb & 1;
  u32x4 Wf[56];
  #pragma unroll
  for (int i = 0; i < 56; i++) Wf[i] = *(const u32x4*)(p.ws + OFF_W2 + (((dl * 4 + w) * 56 + i) << 10) + 16 * lane);
  #pragma unroll
  for (int el = 0; el < 2; el++) {
    const int tau = el == 0 ? mb + 16 * e : mb + 48 - 16 * e;
    u32x4 Hf[28];
    #pragma unroll
    for (int k = 0; k < 28; k++) Hf[k] = *(const u32x4*)(p.ws + OFF_HF + tau * 114688 + ((28 * w + k) << 10) + 16 * lane);
    f32x16 a0, a1;
    for (int i = 0; i < 16; i++) { a0[i] = 0; a1[i] = 0; }
    #pragma unroll
    for (int k = 0; k < 28; k++) {
      a0 = __builtin_amdgcn_mfma_f32_32x32x16_bf16(__builtin_bit_cast(bf16x8, Wf[2 * k]), __builtin_bit_cast(bf16x8, Hf[k]), a0, 0, 0, 0);
      a1 = __builtin_amdgcn_mfma_f32_32x32x16_bf16(__builtin_bit_cast(bf16x8, Wf[2 * k + 1]), __builtin_bit_cast(bf16x8, Hf[k]), a1, 0, 0, 0);
    }
    char* pb = lds + el * 32768 + w * 8192 + 16 * lane;
    for (int a = 0; a < 4; a++) {
      *(f32x4*)(pb + a * 1024) = f32x4{a0[4 * a], a0[4 * a + 1], a0[4 * a + 2], a0[4 * a + 3]};
      *(f32x4*)(pb + 4096 + a * 1024) = f32x4{a1[4 * a], a1[4 * a + 1], a1[4 * a + 2], a1[4 * a + 3]};
    }
    __syncthreads();
    const int t = tid >> 3, dg = tid & 7;
    f32x4 lo = {0, 0, 0, 0}, hi = {0, 0, 0, 0};
    for (int ww = 0; ww < 4; ww++) { const char* q = lds + el * 32768 + ww * 8192 + dg * 1024 + 16 * t; lo += *(const f32x4*)q; hi += *(const f32x4*)(q + 512); }
    const u16* hr = (const u16*)(p.ws + OFF_HT + tau * 28672 + (64 * dl + 8 * dg) * 64 + 2 * t);
    float o[8];
    for (int i = 0; i < 4; i++) { o[i] = lo[i] + bflo(hr[32 * i]); o[4 + i] = hi[i] + bflo(hr[32 * (4 + i)]); }
    u32x4 ov = {pk_bf16(o[0], o[1]), pk_bf16(o[2], o[3]), pk_bf16(o[4], o[5]), pk_bf16(o[6], o[7])};
    *(u32x4*)(p.out + (32 * tau + t) * D + 64 * dl + 8 * dg) = ov;
  }
}

extern "C" void kernel_launch(void* const* d_in, const int* in_sizes, int n_in, void* d_out, int out_size, void* d_ws, size_t ws_size, hipStream_t stream) {
  P p;
  p.x = (const u16*)d_in[0]; p.logA = (const u16*)d_in[1]; p.Aim = (const u16*)d_in[2]; p.Cre = (const u16*)d_in[3]; p.Cim = (const u16*)d_in[4];
  p.logdt = (const u16*)d_in[5]; p.Dskip = (const u16*)d_in[6]; p.w1 = (const u16*)d_in[8]; p.w3 = (const u16*)d_in[9]; p.w2 = (const u16*)d_in[10];
  p.out = (u16*)d_out; p.ws = (char*)d_ws;
  k1<<<256, 256, 0, stream>>>(p);
  k2<<<256, 256, 0, stream>>>(p);
  k3<<<256, 256, 0, stream>>>(p);
  k4<<<256, 256, 0, stream>>>(p);
}
```

```cpp
#include <hip/hip_runtime.h>
#include <stdint.h>

struct Srd { const void* p; uint32_t bytes; uint32_t flags; };
struct Args { Srd s[12]; };

__global__ void __launch_bounds__(256, 1) fused_block(Args a) {
  __shared__ __attribute__((aligned(16))) char lds[161216];
  asm volatile(
    "s_load_dwordx16 s[8:23], %0, 0\n"
    "s_load_dwordx16 s[40:55], %0, 128\n"
    "s_load_dwordx16 s[24:39], %0, 64\n"
    "s_getpc_b64 s[62:63]\n"
    "s_mov_b32 s56, %1\n"
    "s_lshr_b32 s59, s56, 3\n"
    "s_and_b32 s64, s59, 1\n"
    "s_lshl_b32 s64, s64, 15\n"
    "v_lshl_add_u32 v224, v0, 7, s64\n"
    "v_lshrrev_b32_e64 v225, 6, v0\n"
    "v_min_u32 v224, 38016, v224\n"
    "v_readfirstlane_b32 s57, v225\n"
    "global_load_dword v223, v224, s[62:63]\n"
    "v_and_b32 v226, 31, v0\n"
    "v_bfe_u32 v227, v0, 5, 1\n"
    "v_mul_u32_u24 v227, 896, v227\n"
    "v_cmp_gt_u32_e64 vcc, 28, v226\n"
    "v_lshl_add_u32 v227, v226, 5, v227\n"
    "v_mov_b32 v228, 1073741824\n"
    "v_cndmask_b32 v227, v228, v227, vcc\n"
    "s_lshl_b32 s64, s56, 2\n"
    "s_add_u32 s64, s64, s57\n"
    "s_mul_i32 s64, s64, 1792\n"
    "s_waitcnt lgkmcnt(0)\n"
    "buffer_load_dwordx4 v[2:5], v227, s[8:11], s64 offen\n"
    "buffer_load_dwordx4 v[6:9], v227, s[8:11], s64 offen offset:16\n"
    "buffer_load_dword v1, off, s[52:55], 0 sc1\n"
    "v_and_b32 v222, 63, v0\n"
    "v_lshlrev_b32_e64 v230, 4, v0\n"
    "v_cmp_gt_u32 vcc, 192, v0\n"
    "s_and_b32 s58, s56, 7\n"
    "s_mov_b32 s61, 20000\n"
    "v_cndmask_b32 v14, v228, v230, vcc\n"
    "v_mov_b32 v10, -1\n"
    "v_mov_b32 v11, -1\n"
    "v_mov_b32 v12, -1\n"
    "v_mov_b32 v13, -1\n"
    "s_cmp_ge_u32 s59, 28\n"
    "s_cbranch_scc1 Lspare_%=\n"
    "s_mul_i32 s68, s58, 28\n"
    "s_add_u32 s68, s68, s59\n"
    "s_lshr_b32 s69, s57, 1\n"
    "s_and_b32 s70, s57, 1\n"
    "s_lshl1_add_u32 s71, s68, s69\n"
    "v_lshlrev_b32_e64 v38, 3, v222\n"
    "s_lshl_b32 s73, s71, 9\n"
    "s_lshl2_add_u32 s65, s70, s73\n"
    "s_lshl_b32 s66, s68, 2\n"
    "buffer_load_dword v16, v38, s[12:15], s65 offen\n"
    "buffer_load_dword v17, v38, s[16:19], s65 offen\n"
    "s_load_dword s81, s[28:29], s66\n"
    "s_load_dword s82, s[32:33], s66\n"
    "s_add_u32 s64, s66, s57\n"
    "s_mul_i32 s65, s64, 2341\n"
    "s_lshr_b32 s65, s65, 16\n"
    "s_mul_i32 s66, s65, 28\n"
    "s_sub_u32 s64, s64, s66\n"
    "s_bitcmp1_b32 s65, 0\n"
    "s_cselect_b32 s76, s40, s36\n"
    "s_cselect_b32 s77, s41, s37\n"
    "s_cselect_b32 s67, 28, 0\n"
    "buffer_load_dwordx2 v[18:19], v38, s[20:23], s73 offen\n"
    "s_cmp_lt_u32 s65, 28\n"
    "s_cselect_b32 s78, s38, 0\n"
    "s_mov_b32 s79, 0x20000\n"
    "s_cselect_b32 s66, 0, 1073741824\n"
    "v_lshlrev_b32 v233, 4, v222\n"
    "v_add_u32 v15, s66, v233\n"
    "s_lshr_b32 s65, s65, 1\n"
    "s_add_u32 s67, s67, s64\n"
    "s_lshl_b32 s80, s65, 6\n"
    "s_add_u32 s80, s80, s67\n"
    "s_lshl_b32 s80, s80, 2\n"
    "s_add_u32 s80, s80, 20480\n"
    "s_mul_i32 s72, s65, 224\n"
    "s_add_u32 s72, s72, s67\n"
    "s_lshl_b32 s72, s72, 10\n"
    "s_add_u32 s72, s72, 3768320\n"
    "s_mul_i32 s64, s64, 57344\n"
    "s_lshl_b32 s65, s65, 8\n"
    "s_add_u32 s74, s64, s65\n"
    "buffer_load_dwordx2 v[20:21], v38, s[24:27], s73 offen\n"
    "v_lshrrev_b32 v234, 4, v222\n"
    "v_and_b32 v235, 15, v222\n"
    "v_mul_u32_u24 v226, 3584, v234\n"
    "v_lshl_add_u32 v39, v235, 4, v226\n"
    "s_and_b32 s64, s58, 1\n"
    "s_lshl_b32 s64, s64, 5\n"
    "s_add_u32 s64, s64, s59\n"
    "s_and_b32 s65, s64, 48\n"
    "s_bfe_u32 s66, s64, 0x20002\n"
    "s_add_u32 s65, s65, s66\n"
    "s_and_b32 s66, s58, 6\n"
    "s_lshl1_add_u32 s65, s66, s65\n"
    "s_and_b32 s64, s64, 3\n"
    "s_lshl2_add_u32 s65, s65, s64\n"
    "s_mul_i32 s65, s65, 7168\n"
    "s_add_u32 s75, s65, 8613888\n"
    "s_mul_i32 s13, s59, 37\n"
    "s_lshr_b32 s13, s13, 8\n"
    "s_mul_i32 s65, s13, 7\n"
    "s_sub_u32 s10, s59, s65\n"
    "s_and_b32 s65, s58, 1\n"
    "s_mul_i32 s65, s65, 7\n"
    "s_add_u32 s8, s65, s10\n"
    "s_lshr_b32 s65, s58, 1\n"
    "buffer_store_dwordx4 v[10:13], v230, s[52:55], s75 offen sc1\n"
    "s_add_u32 s75, s75, 4096\n"
    "s_lshl2_add_u32 s9, s65, s13\n"
    "s_lshl_b32 s14, s8, 8\n"
    "s_add_u32 s14, s14, 20480\n"
    "s_lshl2_add_u32 s65, s58, s13\n"
    "s_lshr_b32 s66, s65, 1\n"
    "s_and_b32 s65, s65, 1\n"
    "s_lshl_b32 s65, s65, 4\n"
    "s_add_u32 s11, s66, s65\n"
    "s_sub_u32 s12, s66, s65\n"
    "s_add_u32 s12, s12, 48\n"
    "v_lshlrev_b32_e64 v232, 2, v222\n"
    "v_bfe_u32 v237, v222, 2, 2\n"
    "v_lshrrev_b32 v238, 5, v222\n"
    "v_and_b32 v226, 3, v222\n"
    "v_and_b32 v227, 1, v234\n"
    "v_lshlrev_b32 v227, 5, v227\n"
    "buffer_store_dwordx4 v[10:13], v14, s[52:55], s75 offen sc1\n"
    "v_lshl_add_u32 v236, v226, 3, v227\n"
    "s_mul_i32 s64, s57, 5120\n"
    "s_add_u32 s64, s64, 111936\n"
    "v_mul_u32_u24 v226, 320, v234\n"
    "v_lshl_add_u32 v226, v235, 4, v226\n"
    "v_add_u32_e64 v40, s64, v226\n"
    "v_lshl_add_u32 v226, v238, 3, v237\n"
    "v_mul_u32_u24 v226, 320, v226\n"
    "v_add3_u32 v41, v226, v236, s64\n"
    "v_and_b32 v44, 31, v0\n"
    "v_lshrrev_b32 v45, 5, v0\n"
    "v_lshlrev_b32_e64 v91, 4, v45\n"
    "v_mul_u32_u24 v45, 912, v45\n"
    "v_cmp_gt_u32_e64 vcc, 28, v44\n"
    "v_lshl_add_u32 v72, v44, 5, v45\n"
    "v_add_u32 v72, 143680, v72\n"
    "buffer_load_dwordx4 v[22:25], v39, s[76:79], s74 offen\n"
    "s_add_u32 s74, s74, 14336\n"
    "v_mov_b32 v46, 150976\n"
    "v_cndmask_b32 v72, v46, v72, vcc\n"
    "v_and_b32 v44, 3, v0\n"
    "v_mul_u32_u24 v44, 1824, v44\n"
    "v_and_b32 v45, 0xfc, v0\n"
    "v_cmp_gt_u32 vcc, 128, v0\n"
    "v_add_u32 v44, 143680, v44\n"
    "v_add_u32 v73, v44, v45\n"
    "v_mov_b32 v46, 1073741824\n"
    "v_cndmask_b32 v90, v46, v230, vcc\n"
    "v_mov_b32 v42, 0\n"
    "v_mov_b32_e64 v43, 0\n"
    "s_mul_i32 s15, s69, 3072\n"
    "s_add_u32 s15, s15, 112000\n"
    "s_lshl_b32 s64, s70, 5\n"
    "s_add_u32 s64, s64, 16\n"
    "buffer_load_dwordx4 v[26:29], v39, s[76:79], s74 offen\n"
    "s_add_u32 s74, s74, 14336\n"
    "v_lshlrev_b32 v226, 3, v234\n"
    "v_sub_u32 v226, v235, v226\n"
    "v_add_u32 v226, s64, v226\n"
    "v_cvt_f32_i32_e64 v96, v226\n"
    "s_sub_u32 s65, s15, 28\n"
    "v_lshl_add_u32 v130, v226, 2, s65\n"
    "v_mov_b32 v97, 0x03020302\n"
    "v_mov_b32 v98, 0x01000100\n"
    "s_add_u32 s65, s15, 4294899200\n"
    "s_movk_i32 s16, 0xfdfe\n"
    "s_nop 0\n"
    "v_add_f32 v100, 0x43800000, v96\n"
    "v_add_f32 v101, 0x437f0000, v96\n"
    "v_add_f32 v102, 0x437e0000, v96\n"
    "v_add_f32 v103, 0x437d0000, v96\n"
    "v_add_f32 v104, 0x437c0000, v96\n"
    "v_add_f32 v105, 0x437b0000, v96\n"
    "buffer_load_dwordx4 v[30:33], v39, s[76:79], s74 offen\n"
    "s_add_u32 s74, s74, 14336\n"
    "v_add_f32 v106, 0x437a0000, v96\n"
    "v_add_f32 v107, 0x43790000, v96\n"
    "v_cvt_pk_bf16_f32 v108, v100, v101\n"
    "v_cvt_pk_bf16_f32 v109, v102, v103\n"
    "v_cvt_pk_bf16_f32 v110, v104, v105\n"
    "v_cvt_pk_bf16_f32 v111, v106, v107\n"
    "v_lshrrev_b32_e64 v100, 16, v108\n"
    "v_sub_u32_sdwa v104, v108, v108 dst_sel:DWORD dst_unused:UNUSED_PAD src0_sel:WORD_0 src1_sel:WORD_1\n"
    "v_lshrrev_b32_e64 v101, 16, v109\n"
    "v_sub_u32_sdwa v105, v109, v109 dst_sel:DWORD dst_unused:UNUSED_PAD src0_sel:WORD_0 src1_sel:WORD_1\n"
    "v_lshrrev_b32_e64 v102, 16, v110\n"
    "v_sub_u32_sdwa v106, v110, v110 dst_sel:DWORD dst_unused:UNUSED_PAD src0_sel:WORD_0 src1_sel:WORD_1\n"
    "v_lshrrev_b32_e64 v103, 16, v111\n"
    "v_sub_u32_sdwa v107, v111, v111 dst_sel:DWORD dst_unused:UNUSED_PAD src0_sel:WORD_0 src1_sel:WORD_1\n"
    "v_lshl_add_u32 v132, v100, 2, s65\n"
    "v_mad_i32_i24 v144, v104, s16, v97\n"
    "buffer_load_dwordx4 v[34:37], v39, s[76:79], s74 offen\n"
    "v_lshl_add_u32 v133, v101, 2, s65\n"
    "v_mad_i32_i24 v145, v105, s16, v97\n"
    "v_lshl_add_u32 v134, v102, 2, s65\n"
    "v_mad_i32_i24 v146, v106, s16, v97\n"
    "v_lshl_add_u32 v135, v103, 2, s65\n"
    "v_mad_i32_i24 v147, v107, s16, v97\n"
    "v_add_f32 v100, 0x44000000, v96\n"
    "v_add_f32 v101, 0x43ff8000, v96\n"
    "v_add_f32 v102, 0x43ff0000, v96\n"
    "v_add_f32 v103, 0x43fe8000, v96\n"
    "v_add_f32 v104, 0x43fe0000, v96\n"
    "v_add_f32 v105, 0x43fd8000, v96\n"
    "v_add_f32 v106, 0x43fd0000, v96\n"
    "v_add_f32 v107, 0x43fc8000, v96\n"
    "v_cvt_pk_bf16_f32 v108, v100, v101\n"
    "v_cvt_pk_bf16_f32 v109, v102, v103\n"
    "v_cvt_pk_bf16_f32 v110, v104, v105\n"
    "v_cvt_pk_bf16_f32 v111, v106, v107\n"
    "v_lshrrev_b32_e64 v100, 16, v108\n"
    "v_sub_u32_sdwa v104, v108, v108 dst_sel:DWORD dst_unused:UNUSED_PAD src0_sel:WORD_0 src1_sel:WORD_1\n"
    "v_lshrrev_b32_e64 v101, 16, v109\n"
    "v_sub_u32_sdwa v105, v109, v109 dst_sel:DWORD dst_unused:UNUSED_PAD src0_sel:WORD_0 src1_sel:WORD_1\n"
    "v_lshrrev_b32_e64 v102, 16, v110\n"
    "v_sub_u32_sdwa v106, v110, v110 dst_sel:DWORD dst_unused:UNUSED_PAD src0_sel:WORD_0 src1_sel:WORD_1\n"
    "v_lshrrev_b32_e64 v103, 16, v111\n"
    "v_sub_u32_sdwa v107, v111, v111 dst_sel:DWORD dst_unused:UNUSED_PAD src0_sel:WORD_0 src1_sel:WORD_1\n"
    "v_lshl_add_u32 v136, v100, 2, s65\n"
    "v_mad_i32_i24 v148, v104, s16, v97\n"
    "v_lshl_add_u32 v137, v101, 2, s65\n"
    "v_mad_i32_i24 v149, v105, s16, v97\n"
    "v_lshl_add_u32 v138, v102, 2, s65\n"
    "v_mad_i32_i24 v150, v106, s16, v97\n"
    "v_lshl_add_u32 v139, v103, 2, s65\n"
    "v_mad_i32_i24 v151, v107, s16, v97\n"
    "v_add_f32 v100, 0x44800000, v96\n"
    "v_add_f32 v101, 0x447fc000, v96\n"
    "v_add_f32 v102, 0x447f8000, v96\n"
    "v_add_f32 v103, 0x447f4000, v96\n"
    "v_add_f32 v104, 0x447f0000, v96\n"
    "v_add_f32 v105, 0x447ec000, v96\n"
    "v_add_f32 v106, 0x447e8000, v96\n"
    "v_add_f32 v107, 0x447e4000, v96\n"
    "v_cvt_pk_bf16_f32 v108, v100, v101\n"
    "v_cvt_pk_bf16_f32 v109, v102, v103\n"
    "v_cvt_pk_bf16_f32 v110, v104, v105\n"
    "v_cvt_pk_bf16_f32 v111, v106, v107\n"
    "v_lshrrev_b32_e64 v100, 16, v108\n"
    "v_sub_u32_sdwa v104, v108, v108 dst_sel:DWORD dst_unused:UNUSED_PAD src0_sel:WORD_0 src1_sel:WORD_1\n"
    "v_lshrrev_b32_e64 v101, 16, v109\n"
    "v_sub_u32_sdwa v105, v109, v109 dst_sel:DWORD dst_unused:UNUSED_PAD src0_sel:WORD_0 src1_sel:WORD_1\n"
    "v_lshrrev_b32_e64 v102, 16, v110\n"
    "v_sub_u32_sdwa v106, v110, v110 dst_sel:DWORD dst_unused:UNUSED_PAD src0_sel:WORD_0 src1_sel:WORD_1\n"
    "v_lshrrev_b32_e64 v103, 16, v111\n"
    "v_sub_u32_sdwa v107, v111, v111 dst_sel:DWORD dst_unused:UNUSED_PAD src0_sel:WORD_0 src1_sel:WORD_1\n"
    "v_lshl_add_u32 v140, v100, 2, s65\n"
    "v_mad_i32_i24 v152, v104, s16, v97\n"
    "v_lshl_add_u32 v141, v101, 2, s65\n"
    "v_mad_i32_i24 v153, v105, s16, v97\n"
    "v_lshl_add_u32 v142, v102, 2, s65\n"
    "v_mad_i32_i24 v154, v106, s16, v97\n"
    "v_lshl_add_u32 v143, v103, 2, s65\n"
    "v_mad_i32_i24 v155, v107, s16, v97\n"
    "v_add_f32 v100, 0x43a00000, v96\n"
    "v_add_f32 v101, 0x439f8000, v96\n"
    "v_add_f32 v102, 0x439f0000, v96\n"
    "v_add_f32 v103, 0x439e8000, v96\n"
    "v_cvt_pk_bf16_f32 v108, v100, v101\n"
    "v_cvt_pk_bf16_f32 v109, v102, v103\n"
    "v_lshrrev_b32_e64 v100, 16, v108\n"
    "v_sub_u32_sdwa v104, v108, v108 dst_sel:DWORD dst_unused:UNUSED_PAD src0_sel:WORD_0 src1_sel:WORD_1\n"
    "v_lshrrev_b32_e64 v101, 16, v109\n"
    "v_sub_u32_sdwa v105, v109, v109 dst_sel:DWORD dst_unused:UNUSED_PAD src0_sel:WORD_0 src1_sel:WORD_1\n"
    "v_lshl_add_u32 v156, v100, 2, s65\n"
    "v_add_u32_e64 v156, -8, v156\n"
    "v_mad_i32_i24 v158, v104, s16, v97\n"
    "v_lshl_add_u32 v157, v101, 2, s65\n"
    "v_add_u32_e64 v157, -8, v157\n"
    "v_mad_i32_i24 v159, v105, s16, v97\n"
    "s_movk_i32 s16, 0x202\n"
    "s_nop 0\n"
    "v_add_f32 v100, 0x44100000, v96\n"
    "v_add_f32 v101, 0x440fc000, v96\n"
    "v_add_f32 v102, 0x440f8000, v96\n"
    "v_add_f32 v103, 0x440f4000, v96\n"
    "v_add_f32 v104, 0x440f0000, v96\n"
    "v_add_f32 v105, 0x440ec000, v96\n"
    "v_add_f32 v106, 0x440e8000, v96\n"
    "v_add_f32 v107, 0x440e4000, v96\n"
    "v_cvt_pk_bf16_f32 v108, v100, v101\n"
    "v_cvt_pk_bf16_f32 v109, v102, v103\n"
    "v_cvt_pk_bf16_f32 v110, v104, v105\n"
    "v_cvt_pk_bf16_f32 v111, v106, v107\n"
    "v_lshrrev_b32_e64 v100, 16, v111\n"
    "v_lshl_add_u32 v131, v100, 2, s65\n"
    "v_lshl_or_b32 v101, v100, 16, v100\n"
    "v_sub_u32 v104, v108, v101\n"
    "v_sub_u32 v105, v109, v101\n"
    "v_sub_u32 v106, v110, v101\n"
    "v_sub_u32 v107, v111, v101\n"
    "v_xor_b32 v104, 0x10001, v104\n"
    "v_xor_b32 v105, 0x10001, v105\n"
    "v_xor_b32 v106, 0x10001, v106\n"
    "v_xor_b32 v107, 0x10001, v107\n"
    "v_mad_u32_u24 v160, v104, s16, v98\n"
    "v_mad_u32_u24 v161, v105, s16, v98\n"
    "v_mad_u32_u24 v162, v106, s16, v98\n"
    "v_mad_u32_u24 v163, v107, s16, v98\n"
    "v_add_f32 v100, 0x44880000, v96\n"
    "v_add_f32 v101, 0x4487e000, v96\n"
    "v_add_f32 v102, 0x4487c000, v96\n"
    "v_add_f32 v103, 0x4487a000, v96\n"
    "v_add_f32 v104, 0x44878000, v96\n"
    "v_add_f32 v105, 0x44876000, v96\n"
    "v_add_f32 v106, 0x44874000, v96\n"
    "v_add_f32 v107, 0x44872000, v96\n"
    "v_cvt_pk_bf16_f32 v108, v100, v101\n"
    "v_cvt_pk_bf16_f32 v109, v102, v103\n"
    "v_cvt_pk_bf16_f32 v110, v104, v105\n"
    "v_cvt_pk_bf16_f32 v111, v106, v107\n"
    "v_lshrrev_b32_e64 v100, 16, v111\n"
    "v_lshl_add_u32 v164, v100, 2, s65\n"
    "v_lshl_or_b32 v101, v100, 16, v100\n"
    "v_sub_u32 v104, v108, v101\n"
    "v_sub_u32 v105, v109, v101\n"
    "v_sub_u32 v106, v110, v101\n"
    "v_sub_u32 v107, v111, v101\n"
    "v_xor_b32 v104, 0x10001, v104\n"
    "v_xor_b32 v105, 0x10001, v105\n"
    "v_xor_b32 v106, 0x10001, v106\n"
    "v_xor_b32 v107, 0x10001, v107\n"
    "v_mad_u32_u24 v166, v104, s16, v98\n"
    "v_mad_u32_u24 v167, v105, s16, v98\n"
    "v_mad_u32_u24 v168, v106, s16, v98\n"
    "v_mad_u32_u24 v169, v107, s16, v98\n"
    "s_lshl_b32 s64, s70, 4\n"
    "v_add_u32 v226, s64, v235\n"
    "v_lshlrev_b32_e64 v227, 3, v226\n"
    "v_lshl_add_u32 v227, v234, 2, v227\n"
    "v_cmp_eq_u32 vcc, 2, v234\n"
    "v_lshlrev_b32 v228, 2, v226\n"
    "v_add_u32 v228, 256, v228\n"
    "v_cndmask_b32 v227, v227, v228, vcc\n"
    "v_cmp_eq_u32 vcc, 3, v234\n"
    "v_lshlrev_b32_e64 v228, 1, v226\n"
    "v_add_u32 v228, 384, v228\n"
    "v_mov_b32_e64 v229, 8\n"
    "v_mov_b32 v170, 256\n"
    "v_cndmask_b32 v227, v227, v228, vcc\n"
    "v_cndmask_b32_e64 v229, v229, v170, vcc\n"
    "s_sub_u32 s65, s15, 4\n"
    "v_lshl_add_u32 v165, v227, 2, s65\n"
    "v_add_u32_e64 v170, v165, v229\n"
    "v_lshl_add_u32 v228, v234, 8, v226\n"
    "v_cmp_eq_u32 vcc, 256, v228\n"
    "v_add_u32 v227, 512, v226\n"
    "v_mov_b32 v229, 648\n"
    "v_mov_b32 v171, 640\n"
    "v_cndmask_b32 v229, v229, v171, vcc\n"
    "v_cmp_eq_u32 vcc, 0, v234\n"
    "s_mul_i32 s64, s69, 55968\n"
    "s_lshl_b32 s66, s70, 9\n"
    "s_add_u32 s66, s66, s64\n"
    "v_cndmask_b32_e64 v227, v229, v227, vcc\n"
    "v_lshl_add_u32 v171, v227, 2, s65\n"
    "v_add_u32 v172, s66, v38\n"
    "s_mov_b32 m0, s66\n"
    "v_lshl_add_u32 v227, v234, 4, s64\n"
    "s_movk_i32 s67, 1056\n"
    "v_mad_u32_u24 v173, v235, s67, v227\n"
    "v_add_u32 v228, 21, v226\n"
    "v_mad_u32_u24 v175, v228, s67, v227\n"
    "v_min_u32 v228, 4, v235\n"
    "v_add_u32 v228, 16, v228\n"
    "v_mad_u32_u24 v174, v228, s67, v227\n"
    "s_cmp_eq_u32 s69, 0\n"
    "s_cselect_b32 s64, 16, 0\n"
    "s_waitcnt lgkmcnt(0)\n"
    "s_lshl_b32 s81, s81, s64\n"
    "s_and_b32 s81, s81, 0xffff0000\n"
    "s_lshl_b32 s82, s82, s64\n"
    "s_and_b32 s82, s82, 0xffff0000\n"
    "s_waitcnt vmcnt(12)\n"
    "v_dot2c_f32_bf16 v42, 0x3f803f80, v2\n"
    "v_dot2c_f32_bf16 v43, v2, v2\n"
    "v_dot2c_f32_bf16 v42, 0x3f803f80, v3\n"
    "v_dot2c_f32_bf16 v43, v3, v3\n"
    "v_dot2c_f32_bf16 v42, 0x3f803f80, v4\n"
    "v_dot2c_f32_bf16 v43, v4, v4\n"
    "v_dot2c_f32_bf16 v42, 0x3f803f80, v5\n"
    "v_dot2c_f32_bf16 v43, v5, v5\n"
    "s_waitcnt vmcnt(11)\n"
    "v_dot2c_f32_bf16 v42, 0x3f803f80, v6\n"
    "v_dot2c_f32_bf16 v43, v6, v6\n"
    "v_dot2c_f32_bf16 v42, 0x3f803f80, v7\n"
    "v_dot2c_f32_bf16 v43, v7, v7\n"
    "v_dot2c_f32_bf16 v42, 0x3f803f80, v8\n"
    "v_dot2c_f32_bf16 v43, v8, v8\n"
    "v_dot2c_f32_bf16 v42, 0x3f803f80, v9\n"
    "v_dot2c_f32_bf16 v43, v9, v9\n"
    "s_waitcnt vmcnt(10)\n"
    "v_add_u32 v231, 1, v1\n"
    "v_lshlrev_b32 v56, 16, v2\n"
    "v_and_b32 v57, 0xffff0000, v2\n"
    "v_add_f32_dpp v42, v42, v42 quad_perm:[1,0,3,2] row_mask:0xf bank_mask:0xf\n"
    "v_add_f32_dpp v43, v43, v43 quad_perm:[1,0,3,2] row_mask:0xf bank_mask:0xf\n"
    "v_lshlrev_b32_e64 v58, 16, v3\n"
    "v_and_b32 v59, 0xffff0000, v3\n"
    "v_add_f32_dpp v42, v42, v42 quad_perm:[2,3,0,1] row_mask:0xf bank_mask:0xf\n"
    "v_add_f32_dpp v43, v43, v43 quad_perm:[2,3,0,1] row_mask:0xf bank_mask:0xf\n"
    "v_lshlrev_b32_e64 v60, 16, v4\n"
    "v_and_b32 v61, 0xffff0000, v4\n"
    "v_add_f32_dpp v42, v42, v42 row_half_mirror row_mask:0xf bank_mask:0xf\n"
    "v_add_f32_dpp v43, v43, v43 row_half_mirror row_mask:0xf bank_mask:0xf\n"
    "v_lshlrev_b32_e64 v62, 16, v5\n"
    "v_and_b32 v63, 0xffff0000, v5\n"
    "v_add_f32_dpp v42, v42, v42 row_mirror row_mask:0xf bank_mask:0xf\n"
    "v_add_f32_dpp v43, v43, v43 row_mirror row_mask:0xf bank_mask:0xf\n"
    "v_lshlrev_b32_e64 v64, 16, v6\n"
    "v_and_b32 v65, 0xffff0000, v6\n"
    "v_mov_b32 v44, v42\n"
    "v_mov_b32 v45, v43\n"
    "v_readfirstlane_b32 s60, v231\n"
    "v_lshlrev_b32 v66, 16, v7\n"
    "v_permlane16_swap_b32 v42, v44\n"
    "v_permlane16_swap_b32 v43, v45\n"
    "v_add_f32 v42, v42, v44\n"
    "v_add_f32 v43, v43, v45\n"
    "v_mul_f32 v92, 0x3b124925, v42\n"
    "v_mov_b32 v46, 0x3727c5ac\n"
    "v_fmac_f32 v46, 0x3b124925, v43\n"
    "v_fma_f32 v46, -v92, v92, v46\n"
    "v_max_f32 v94, 0x3727c5ac, v46\n"
    "v_rsq_f32_e64 v46, v94\n"
    "v_and_b32 v67, 0xffff0000, v7\n"
    "v_lshlrev_b32_e64 v68, 16, v8\n"
    "v_and_b32 v69, 0xffff0000, v8\n"
    "v_lshlrev_b32_e64 v70, 16, v9\n"
    "v_and_b32 v71, 0xffff0000, v9\n"
    "v_mul_f32_e64 v42, -v92, v46\n"
    "v_mul_f32_e64 v94, v94, v46\n"
    "v_fma_f32 v56, v56, v46, v42\n"
    "v_fma_f32 v57, v57, v46, v42\n"
    "v_fma_f32 v58, v58, v46, v42\n"
    "v_fma_f32 v59, v59, v46, v42\n"
    "v_fma_f32 v60, v60, v46, v42\n"
    "v_fma_f32 v61, v61, v46, v42\n"
    "v_fma_f32 v62, v62, v46, v42\n"
    "v_fma_f32 v63, v63, v46, v42\n"
    "v_fma_f32 v64, v64, v46, v42\n"
    "v_fma_f32 v65, v65, v46, v42\n"
    "v_fma_f32 v66, v66, v46, v42\n"
    "v_fma_f32 v67, v67, v46, v42\n"
    "v_fma_f32 v68, v68, v46, v42\n"
    "v_fma_f32 v69, v69, v46, v42\n"
    "v_fma_f32 v70, v70, v46, v42\n"
    "v_fma_f32 v71, v71, v46, v42\n"
    "v_cvt_pk_f16_f32 v48, v56, v57\n"
    "v_cvt_pk_f16_f32 v49, v58, v59\n"
    "v_cvt_pk_f16_f32 v50, v60, v61\n"
    "v_cvt_pk_f16_f32 v51, v62, v63\n"
    "v_cvt_pk_f16_f32 v52, v64, v65\n"
    "v_cvt_pk_f16_f32 v53, v66, v67\n"
    "v_cvt_pk_f16_f32 v54, v68, v69\n"
    "v_cvt_pk_f16_f32 v55, v70, v71\n"
    "ds_write_b128 v72, v[48:51]\n"
    "ds_write_b128 v72, v[52:55] offset:16\n"
    "v_mov_b32 v75, v231\n"
    "v_mov_b32 v77, v231\n"
    "v_mov_b32 v79, v231\n"
    "v_mov_b32 v81, v231\n"
    "v_mov_b32 v83, v231\n"
    "v_mov_b32 v85, v231\n"
    "v_mov_b32 v87, v231\n"
    "v_mov_b32 v89, v231\n"
    "v_mov_b32 v93, v231\n"
    "v_mov_b32_e64 v95, v231\n"
    "s_waitcnt vmcnt(7)\n"
    "v_and_b32 v2, 0x7f807f80, v18\n"
    "v_and_b32 v3, 0x7f807f80, v19\n"
    "s_waitcnt vmcnt(6)\n"
    "v_and_b32 v4, 0x7f807f80, v20\n"
    "v_and_b32 v5, 0x7f807f80, v21\n"
    "v_mov_b32 v1, 0x3fb8aa3b\n"
    "v_pk_max_u16 v2, v2, v3\n"
    "v_pk_max_u16 v4, v4, v5\n"
    "v_mul_f32 v1, s81, v1\n"
    "v_pk_max_u16 v2, v2, v4\n"
    "v_exp_f32 v1, v1\n"
    "v_lshlrev_b32 v3, 16, v2\n"
    "v_max_u32 v2, v2, v3\n"
    "s_cmp_eq_u32 s70, 1\n"
    "s_cselect_b64 vcc, -1, 0\n"
    "v_cndmask_b32 v18, v18, v19, vcc\n"
    "v_cndmask_b32_e64 v19, v20, v21, vcc\n"
    "v_cvt_pk_bf16_f32 v1, v1, v1\n"
    "v_lshlrev_b32_e64 v6, 16, v16\n"
    "v_max_u32_dpp v2, v2, v2 quad_perm:[1,0,3,2] row_mask:0xf bank_mask:0xf\n"
    "v_and_b32 v7, 0xffff0000, v16\n"
    "v_lshlrev_b32_e64 v1, 16, v1\n"
    "v_max_u32_dpp v2, v2, v2 quad_perm:[2,3,0,1] row_mask:0xf bank_mask:0xf\n"
    "v_mul_f32 v6, 0x3fb8aa3b, v6\n"
    "v_mul_f32 v7, 0x3fb8aa3b, v7\n"
    "v_max_u32_dpp v2, v2, v2 row_half_mirror row_mask:0xf bank_mask:0xf\n"
    "v_exp_f32 v6, v6\n"
    "v_exp_f32 v7, v7\n"
    "v_max_u32_dpp v2, v2, v2 row_mirror row_mask:0xf bank_mask:0xf\n"
    "v_lshlrev_b32_e64 v8, 16, v17\n"
    "v_and_b32 v9, 0xffff0000, v17\n"
    "v_mov_b32_e64 v3, v2\n"
    "v_cvt_pk_bf16_f32 v7, v6, v7\n"
    "v_mul_f32 v54, 0xbfb8aa3b, v1\n"
    "v_permlane16_swap_b32 v2, v3\n"
    "v_mul_f32 v55, 0x3e22f983, v1\n"
    "v_max_u32 v2, v2, v3\n"
    "v_lshlrev_b32 v6, 16, v7\n"
    "v_mov_b32_e64 v3, v2\n"
    "v_and_b32 v7, 0xffff0000, v7\n"
    "v_mul_f32 v6, v6, v54\n"
    "v_permlane32_swap_b32 v2, v3\n"
    "v_mul_f32 v7, v7, v54\n"
    "v_max_u32 v2, v2, v3\n"
    "v_mul_f32 v8, v8, v55\n"
    "v_mul_f32_e64 v9, v9, v55\n"
    "v_readfirstlane_b32 s16, v2\n"
    "s_lshr_b32 s16, s16, 23\n"
    "s_max_u32 s16, s16, 1\n"
    "s_min_u32 s16, s16, 253\n"
    "s_add_u32 s17, s16, 1\n"
    "s_lshl_b32 s17, s17, 23\n"
    "s_sub_u32 s16, 254, s16\n"
    "s_lshl_b32 s16, s16, 23\n"
    "s_waitcnt lgkmcnt(0)\n"
    "s_barrier\n"
    "s_nop 0\n"
    "ds_read_b32 v74, v73 offset:0\n"
    "ds_read_b32 v76, v73 offset:912\n"
    "ds_read_b32 v78, v73 offset:256\n"
    "ds_read_b32 v80, v73 offset:1168\n"
    "ds_read_b32 v82, v73 offset:512\n"
    "ds_read_b32 v84, v73 offset:1424\n"
    "ds_read_b32 v86, v73 offset:768\n"
    "ds_read_b32 v88, v73 offset:1680\n"
    "s_mul_i32 s18, s56, 14336\n"
    "s_add_u32 s18, s18, 98304\n"
    "s_waitcnt lgkmcnt(6)\n"
    "buffer_store_dwordx4 v[74:77], v230, s[52:55], s18 offen sc1\n"
    "s_add_u32 s18, s18, 4096\n"
    "s_waitcnt lgkmcnt(4)\n"
    "buffer_store_dwordx4 v[78:81], v230, s[52:55], s18 offen sc1\n"
    "s_add_u32 s18, s18, 4096\n"
    "v_fract_f32 v56, v8\n"
    "v_fract_f32 v57, v9\n"
    "v_exp_f32 v54, v6\n"
    "v_exp_f32 v55, v7\n"
    "v_cos_f32 v58, v56\n"
    "v_sin_f32 v60, v56\n"
    "v_cos_f32 v59, v57\n"
    "v_sin_f32_e64 v61, v57\n"
    "s_waitcnt lgkmcnt(2)\n"
    "buffer_store_dwordx4 v[82:85], v230, s[52:55], s18 offen sc1\n"
    "s_add_u32 s18, s18, 4096\n"
    "v_mul_f32 v42, v54, v58\n"
    "v_mul_f32 v43, v54, v60\n"
    "v_mul_f32 v44, v55, v59\n"
    "v_mul_f32 v45, v55, v61\n"
    "v_mul_f32 v54, 0x41000000, v6\n"
    "v_mul_f32 v56, 0x41000000, v8\n"
    "v_mul_f32 v55, 0x41000000, v7\n"
    "v_mul_f32 v57, 0x41000000, v9\n"
    "s_waitcnt lgkmcnt(0)\n"
    "buffer_store_dwordx4 v[86:89], v90, s[52:55], s18 offen sc1\n"
    "v_fract_f32 v56, v56\n"
    "v_fract_f32 v57, v57\n"
    "v_exp_f32 v54, v54\n"
    "v_exp_f32 v55, v55\n"
    "v_cos_f32 v58, v56\n"
    "v_sin_f32 v60, v56\n"
    "v_cos_f32 v59, v57\n"
    "v_sin_f32 v61, v57\n"
    "v_mul_f32 v46, v54, v58\n"
    "v_mul_f32 v47, v54, v60\n"
    "v_mul_f32 v48, v55, v59\n"
    "v_mul_f32_e64 v49, v55, v61\n"
    "v_mul_f32 v54, 0x43800000, v6\n"
    "v_mul_f32 v56, 0x43800000, v8\n"
    "v_mul_f32 v55, 0x43800000, v7\n"
    "v_mul_f32 v57, 0x43800000, v9\n"
    "v_fract_f32 v56, v56\n"
    "v_fract_f32 v57, v57\n"
    "v_exp_f32 v54, v54\n"
    "v_exp_f32 v55, v55\n"
    "v_cos_f32 v58, v56\n"
    "v_sin_f32 v60, v56\n"
    "v_cos_f32 v59, v57\n"
    "v_sin_f32 v61, v57\n"
    "v_mul_f32 v50, v54, v58\n"
    "v_mul_f32 v51, v54, v60\n"
    "v_mul_f32 v52, v55, v59\n"
    "v_mul_f32 v53, v55, v61\n"
    "v_mul_f32 v239, s17, v1\n"
    "v_lshlrev_b32 v54, 16, v18\n"
    "v_lshlrev_b32_e64 v55, 16, v19\n"
    "v_and_b32 v56, 0xffff0000, v18\n"
    "v_and_b32 v57, 0xffff0000, v19\n"
    "v_mul_f32_e64 v10, s16, v54\n"
    "v_mul_f32_e64 v11, s16, -v55\n"
    "v_mul_f32_e64 v12, s16, v56\n"
    "v_mul_f32_e64 v13, s16, -v57\n"
    "v_cvt_pk_f16_f32 v112, v10, v11\n"
    "v_cvt_pk_f16_f32 v113, v12, v13\n"
    "v_cvt_pk_f16_f32 v114, v46, v47\n"
    "v_cvt_pk_f16_f32 v115, v48, v49\n"
    "v_mov_b32 v116, 0x3c00\n"
    "v_mov_b32 v117, 0x3c00\n"
    "v_pk_mul_f32 v[176:177], v[46:47], v[46:47] op_sel:[0,0] op_sel_hi:[0,1]\n"
    "v_pk_mul_f32 v[178:179], v[48:49], v[48:49] op_sel:[0,0] op_sel_hi:[0,1]\n"
    "v_pk_mul_f32 v[180:181], v[10:11], v[42:43] op_sel:[0,0] op_sel_hi:[0,1] neg_hi:[0,1]\n"
    "v_pk_mul_f32 v[182:183], v[12:13], v[44:45] op_sel:[0,0] op_sel_hi:[0,1] neg_hi:[0,1]\n"
    "v_pk_fma_f32 v[82:83], v[46:47], v[46:47], v[176:177] op_sel:[1,1,0] op_sel_hi:[1,0,1] neg_lo:[0,1,0]\n"
    "v_pk_fma_f32 v[84:85], v[48:49], v[48:49], v[178:179] op_sel:[1,1,0] op_sel_hi:[1,0,1] neg_lo:[0,1,0]\n"
    "ds_write_addtid_b32 v112 offset:0\n"
    "ds_write_addtid_b32 v113 offset:256\n"
    "ds_write_addtid_b32 v114 offset:23232\n"
    "ds_write_addtid_b32 v115 offset:23488\n"
    "ds_write_addtid_b32 v116 offset:22176\n"
    "ds_write_addtid_b32 v117 offset:22432\n"
    "v_pk_fma_f32 v[74:75], v[10:11], v[42:43], v[180:181] op_sel:[1,1,0] op_sel_hi:[1,0,1]\n"
    "v_pk_fma_f32 v[76:77], v[12:13], v[44:45], v[182:183] op_sel:[1,1,0] op_sel_hi:[1,0,1]\n"
    "v_pk_mul_f32 v[176:177], v[82:83], v[46:47] op_sel:[0,0] op_sel_hi:[0,1]\n"
    "v_pk_mul_f32 v[178:179], v[84:85], v[48:49] op_sel:[0,0] op_sel_hi:[0,1]\n"
    "v_pk_mul_f32 v[180:181], v[74:75], v[42:43] op_sel:[0,0] op_sel_hi:[0,1] neg_hi:[0,1]\n"
    "v_pk_mul_f32 v[182:183], v[76:77], v[44:45] op_sel:[0,0] op_sel_hi:[0,1] neg_hi:[0,1]\n"
    "v_cvt_pk_f16_f32 v118, v82, v83\n"
    "v_cvt_pk_f16_f32 v119, v84, v85\n"
    "v_pk_fma_f32 v[82:83], v[82:83], v[46:47], v[176:177] op_sel:[1,1,0] op_sel_hi:[1,0,1] neg_lo:[0,1,0]\n"
    "v_pk_fma_f32 v[84:85], v[84:85], v[48:49], v[178:179] op_sel:[1,1,0] op_sel_hi:[1,0,1] neg_lo:[0,1,0]\n"
    "ds_write_addtid_b32 v118 offset:24288\n"
    "ds_write_addtid_b32 v119 offset:24544\n"
    "v_cvt_pk_f16_f32 v120, v74, v75\n"
    "v_cvt_pk_f16_f32 v121, v76, v77\n"
    "v_pk_fma_f32 v[62:63], v[74:75], v[42:43], v[180:181] op_sel:[1,1,0] op_sel_hi:[1,0,1]\n"
    "v_pk_fma_f32 v[64:65], v[76:77], v[44:45], v[182:183] op_sel:[1,1,0] op_sel_hi:[1,0,1]\n"
    "ds_write_addtid_b32 v120 offset:1056\n"
    "ds_write_addtid_b32 v121 offset:1312\n"
    "v_pk_mul_f32 v[176:177], v[82:83], v[46:47] op_sel:[0,0] op_sel_hi:[0,1]\n"
    "v_pk_mul_f32 v[178:179], v[84:85], v[48:49] op_sel:[0,0] op_sel_hi:[0,1]\n"
    "v_pk_mul_f32 v[180:181], v[62:63], v[42:43] op_sel:[0,0] op_sel_hi:[0,1] neg_hi:[0,1]\n"
    "v_pk_mul_f32 v[182:183], v[64:65], v[44:45] op_sel:[0,0] op_sel_hi:[0,1] neg_hi:[0,1]\n"
    "v_cvt_pk_f16_f32 v122, v82, v83\n"
    "v_cvt_pk_f16_f32 v123, v84, v85\n"
    "v_pk_fma_f32 v[82:83], v[82:83], v[46:47], v[176:177] op_sel:[1,1,0] op_sel_hi:[1,0,1] neg_lo:[0,1,0]\n"
    "v_pk_fma_f32 v[84:85], v[84:85], v[48:49], v[178:179] op_sel:[1,1,0] op_sel_hi:[1,0,1] neg_lo:[0,1,0]\n"
    "ds_write_addtid_b32 v122 offset:25344\n"
    "ds_write_addtid_b32 v123 offset:25600\n"
    "v_cvt_pk_f16_f32 v124, v62, v63\n"
    "v_cvt_pk_f16_f32 v125, v64, v65\n"
    "v_pk_fma_f32 v[74:75], v[62:63], v[42:43], v[180:181] op_sel:[1,1,0] op_sel_hi:[1,0,1]\n"
    "v_pk_fma_f32 v[76:77], v[64:65], v[44:45], v[182:183] op_sel:[1,1,0] op_sel_hi:[1,0,1]\n"
    "ds_write_addtid_b32 v124 offset:2112\n"
    "ds_write_addtid_b32 v125 offset:2368\n"
    "s_waitcnt vmcnt(8)\n"
    "s_lshl_b32 s64, s56, 7\n"
    "s_add_u32 s64, s64, 32768\n"
    "s_mov_b32 exec_lo, 1\n"
    "s_mov_b32 exec_hi, 1\n"
    "buffer_store_dwordx4 v[92:95], v91, s[52:55], s64 offen sc1\n"
    "s_mov_b64 exec, -1\n"
    "s_nop 0\n"
    "v_pk_mul_f32 v[176:177], v[82:83], v[46:47] op_sel:[0,0] op_sel_hi:[0,1]\n"
    "v_pk_mul_f32 v[178:179], v[84:85], v[48:49] op_sel:[0,0] op_sel_hi:[0,1]\n"
    "v_pk_mul_f32 v[180:181], v[74:75], v[42:43] op_sel:[0,0] op_sel_hi:[0,1] neg_hi:[0,1]\n"
    "v_pk_mul_f32 v[182:183], v[76:77], v[44:45] op_sel:[0,0] op_sel_hi:[0,1] neg_hi:[0,1]\n"
    "v_cvt_pk_f16_f32 v126, v82, v83\n"
    "v_cvt_pk_f16_f32 v127, v84, v85\n"
    "v_pk_fma_f32 v[82:83], v[82:83], v[46:47], v[176:177] op_sel:[1,1,0] op_sel_hi:[1,0,1] neg_lo:[0,1,0]\n"
    "v_pk_fma_f32 v[84:85], v[84:85], v[48:49], v[178:179] op_sel:[1,1,0] op_sel_hi:[1,0,1] neg_lo:[0,1,0]\n"
    "ds_write_addtid_b32 v126 offset:26400\n"
    "ds_write_addtid_b32 v127 offset:26656\n"
    "v_cvt_pk_f16_f32 v112, v74, v75\n"
    "v_cvt_pk_f16_f32 v113, v76, v77\n"
    "v_pk_fma_f32 v[66:67], v[74:75], v[42:43], v[180:181] op_sel:[1,1,0] op_sel_hi:[1,0,1]\n"
    "v_pk_fma_f32 v[68:69], v[76:77], v[44:45], v[182:183] op_sel:[1,1,0] op_sel_hi:[1,0,1]\n"
    "ds_write_addtid_b32 v112 offset:3168\n"
    "ds_write_addtid_b32 v113 offset:3424\n"
    "v_lshrrev_b32_e64 v226, 7, v0\n"
    "v_and_b32 v227, 127, v0\n"
    "v_mul_u32_u24 v226, 4608, v226\n"
    "v_lshl_add_u32 v226, v227, 2, v226\n"
    "v_add_u32 v226, 118080, v226\n"
    "v_mov_b32 v228, 0\n"
    "s_sub_u32 s65, s15, 64\n"
    "v_lshl_add_u32 v227, v235, 2, s65\n"
    "v_lshrrev_b32 v224, 2, v0\n"
    "v_and_b32 v225, 3, v0\n"
    "v_mul_u32_u24 v224, 14336, v224\n"
    "v_lshl_add_u32 v184, v225, 4, v224\n"
    "v_min_u32 v185, 55, v222\n"
    "v_lshlrev_b32 v185, 2, v185\n"
    "v_pk_mul_f32 v[176:177], v[82:83], v[46:47] op_sel:[0,0] op_sel_hi:[0,1]\n"
    "v_pk_mul_f32 v[178:179], v[84:85], v[48:49] op_sel:[0,0] op_sel_hi:[0,1]\n"
    "v_pk_mul_f32 v[180:181], v[66:67], v[42:43] op_sel:[0,0] op_sel_hi:[0,1] neg_hi:[0,1]\n"
    "v_pk_mul_f32 v[182:183], v[68:69], v[44:45] op_sel:[0,0] op_sel_hi:[0,1] neg_hi:[0,1]\n"
    "v_cvt_pk_f16_f32 v114, v82, v83\n"
    "v_cvt_pk_f16_f32 v115, v84, v85\n"
    "v_pk_fma_f32 v[82:83], v[82:83], v[46:47], v[176:177] op_sel:[1,1,0] op_sel_hi:[1,0,1] neg_lo:[0,1,0]\n"
    "v_pk_fma_f32 v[84:85], v[84:85], v[48:49], v[178:179] op_sel:[1,1,0] op_sel_hi:[1,0,1] neg_lo:[0,1,0]\n"
    "ds_write_addtid_b32 v114 offset:27456\n"
    "ds_write_addtid_b32 v115 offset:27712\n"
    "v_cvt_pk_f16_f32 v116, v66, v67\n"
    "v_cvt_pk_f16_f32 v117, v68, v69\n"
    "v_pk_fma_f32 v[74:75], v[66:67], v[42:43], v[180:181] op_sel:[1,1,0] op_sel_hi:[1,0,1]\n"
    "v_pk_fma_f32 v[76:77], v[68:69], v[44:45], v[182:183] op_sel:[1,1,0] op_sel_hi:[1,0,1]\n"
    "ds_write_addtid_b32 v116 offset:4224\n"
    "ds_write_addtid_b32 v117 offset:4480\n"
    "v_pk_mul_f32 v[176:177], v[82:83], v[46:47] op_sel:[0,0] op_sel_hi:[0,1]\n"
    "v_pk_mul_f32 v[178:179], v[84:85], v[48:49] op_sel:[0,0] op_sel_hi:[0,1]\n"
    "v_pk_mul_f32 v[180:181], v[74:75], v[42:43] op_sel:[0,0] op_sel_hi:[0,1] neg_hi:[0,1]\n"
    "v_pk_mul_f32 v[182:183], v[76:77], v[44:45] op_sel:[0,0] op_sel_hi:[0,1] neg_hi:[0,1]\n"
    "v_cvt_pk_f16_f32 v118, v82, v83\n"
    "v_cvt_pk_f16_f32 v119, v84, v85\n"
    "v_pk_fma_f32 v[82:83], v[82:83], v[46:47], v[176:177] op_sel:[1,1,0] op_sel_hi:[1,0,1] neg_lo:[0,1,0]\n"
    "v_pk_fma_f32 v[84:85], v[84:85], v[48:49], v[178:179] op_sel:[1,1,0] op_sel_hi:[1,0,1] neg_lo:[0,1,0]\n"
    "ds_write_addtid_b32 v118 offset:28512\n"
    "ds_write_addtid_b32 v119 offset:28768\n"
    "v_cvt_pk_f16_f32 v120, v74, v75\n"
    "v_cvt_pk_f16_f32 v121, v76, v77\n"
    "v_pk_fma_f32 v[70:71], v[74:75], v[42:43], v[180:181] op_sel:[1,1,0] op_sel_hi:[1,0,1]\n"
    "v_pk_fma_f32 v[72:73], v[76:77], v[44:45], v[182:183] op_sel:[1,1,0] op_sel_hi:[1,0,1]\n"
    "ds_write_addtid_b32 v120 offset:5280\n"
    "ds_write_addtid_b32 v121 offset:5536\n"
    "s_waitcnt vmcnt(8)\n"
    "ds_write_b128 v40, v[22:25] offset:0\n"
    "s_waitcnt vmcnt(7)\n"
    "ds_write_b128 v40, v[26:29] offset:1280\n"
    "s_waitcnt vmcnt(6)\n"
    "ds_write_b128 v40, v[30:33] offset:2560\n"
    "s_waitcnt vmcnt(5)\n"
    "ds_write_b128 v40, v[34:37] offset:3840\n"
    "ds_read_b64_tr_b16 v[96:97], v41 offset:0\n"
    "ds_read_b64_tr_b16 v[98:99], v41 offset:1280\n"
    "ds_read_b64_tr_b16 v[100:101], v41 offset:64\n"
    "ds_read_b64_tr_b16 v[102:103], v41 offset:1344\n"
    "ds_read_b64_tr_b16 v[104:105], v41 offset:128\n"
    "ds_read_b64_tr_b16 v[106:107], v41 offset:1408\n"
    "ds_read_b64_tr_b16 v[108:109], v41 offset:192\n"
    "ds_read_b64_tr_b16 v[110:111], v41 offset:1472\n"
    "v_pk_mul_f32 v[176:177], v[82:83], v[46:47] op_sel:[0,0] op_sel_hi:[0,1]\n"
    "v_pk_mul_f32 v[178:179], v[84:85], v[48:49] op_sel:[0,0] op_sel_hi:[0,1]\n"
    "v_pk_mul_f32 v[180:181], v[70:71], v[42:43] op_sel:[0,0] op_sel_hi:[0,1] neg_hi:[0,1]\n"
    "v_pk_mul_f32 v[182:183], v[72:73], v[44:45] op_sel:[0,0] op_sel_hi:[0,1] neg_hi:[0,1]\n"
    "v_cvt_pk_f16_f32 v122, v82, v83\n"
    "v_cvt_pk_f16_f32 v123, v84, v85\n"
    "v_pk_fma_f32 v[82:83], v[82:83], v[46:47], v[176:177] op_sel:[1,1,0] op_sel_hi:[1,0,1] neg_lo:[0,1,0]\n"
    "v_pk_fma_f32 v[84:85], v[84:85], v[48:49], v[178:179] op_sel:[1,1,0] op_sel_hi:[1,0,1] neg_lo:[0,1,0]\n"
    "ds_write_addtid_b32 v122 offset:29568\n"
    "ds_write_addtid_b32 v123 offset:29824\n"
    "v_cvt_pk_f16_f32 v124, v70, v71\n"
    "v_cvt_pk_f16_f32 v125, v72, v73\n"
    "v_pk_fma_f32 v[74:75], v[70:71], v[42:43], v[180:181] op_sel:[1,1,0] op_sel_hi:[1,0,1]\n"
    "v_pk_fma_f32 v[76:77], v[72:73], v[44:45], v[182:183] op_sel:[1,1,0] op_sel_hi:[1,0,1]\n"
    "ds_write_addtid_b32 v124 offset:6336\n"
    "ds_write_addtid_b32 v125 offset:6592\n"
    "s_waitcnt lgkmcnt(10)\n"
    "s_nop 0\n"
    "buffer_store_dwordx4 v[96:99], v15, s[52:55], s72 offen sc1\n"
    "s_add_u32 s72, s72, 57344\n"
    "v_pk_mul_f32 v[176:177], v[82:83], v[46:47] op_sel:[0,0] op_sel_hi:[0,1]\n"
    "v_pk_mul_f32 v[178:179], v[84:85], v[48:49] op_sel:[0,0] op_sel_hi:[0,1]\n"
    "v_pk_mul_f32 v[180:181], v[10:11], v[50:51] op_sel:[0,0] op_sel_hi:[0,1] neg_hi:[0,1]\n"
    "v_pk_mul_f32 v[182:183], v[12:13], v[52:53] op_sel:[0,0] op_sel_hi:[0,1] neg_hi:[0,1]\n"
    "v_cvt_pk_f16_f32 v126, v82, v83\n"
    "v_cvt_pk_f16_f32 v127, v84, v85\n"
    "v_pk_fma_f32 v[82:83], v[82:83], v[46:47], v[176:177] op_sel:[1,1,0] op_sel_hi:[1,0,1] neg_lo:[0,1,0]\n"
    "v_pk_fma_f32 v[84:85], v[84:85], v[48:49], v[178:179] op_sel:[1,1,0] op_sel_hi:[1,0,1] neg_lo:[0,1,0]\n"
    "ds_write_addtid_b32 v126 offset:30624\n"
    "ds_write_addtid_b32 v127 offset:30880\n"
    "v_cvt_pk_f16_f32 v112, v74, v75\n"
    "v_cvt_pk_f16_f32 v113, v76, v77\n"
    "v_pk_fma_f32 v[74:75], v[10:11], v[50:51], v[180:181] op_sel:[1,1,0] op_sel_hi:[1,0,1]\n"
    "v_pk_fma_f32 v[76:77], v[12:13], v[52:53], v[182:183] op_sel:[1,1,0] op_sel_hi:[1,0,1]\n"
    "ds_write_addtid_b32 v112 offset:7392\n"
    "ds_write_addtid_b32 v113 offset:7648\n"
    "s_waitcnt lgkmcnt(12)\n"
    "s_nop 0\n"
    "buffer_store_dwordx4 v[100:103], v15, s[52:55], s72 offen sc1\n"
    "s_add_u32 s72, s72, 57344\n"
    "v_pk_mul_f32 v[176:177], v[82:83], v[46:47] op_sel:[0,0] op_sel_hi:[0,1]\n"
    "v_pk_mul_f32 v[178:179], v[84:85], v[48:49] op_sel:[0,0] op_sel_hi:[0,1]\n"
    "v_pk_mul_f32 v[180:181], v[74:75], v[50:51] op_sel:[0,0] op_sel_hi:[0,1] neg_hi:[0,1]\n"
    "v_pk_mul_f32 v[182:183], v[76:77], v[52:53] op_sel:[0,0] op_sel_hi:[0,1] neg_hi:[0,1]\n"
    "v_cvt_pk_f16_f32 v114, v82, v83\n"
    "v_cvt_pk_f16_f32 v115, v84, v85\n"
    "v_pk_fma_f32 v[82:83], v[82:83], v[46:47], v[176:177] op_sel:[1,1,0] op_sel_hi:[1,0,1] neg_lo:[0,1,0]\n"
    "v_pk_fma_f32 v[84:85], v[84:85], v[48:49], v[178:179] op_sel:[1,1,0] op_sel_hi:[1,0,1] neg_lo:[0,1,0]\n"
    "ds_write_addtid_b32 v114 offset:31680\n"
    "ds_write_addtid_b32 v115 offset:31936\n"
    "v_cvt_pk_f16_f32 v116, v74, v75\n"
    "v_cvt_pk_f16_f32 v117, v76, v77\n"
    "v_pk_fma_f32 v[74:75], v[74:75], v[50:51], v[180:181] op_sel:[1,1,0] op_sel_hi:[1,0,1]\n"
    "v_pk_fma_f32 v[76:77], v[76:77], v[52:53], v[182:183] op_sel:[1,1,0] op_sel_hi:[1,0,1]\n"
    "ds_write_addtid_b32 v116 offset:8448\n"
    "ds_write_addtid_b32 v117 offset:8704\n"
    "s_waitcnt lgkmcnt(14)\n"
    "s_nop 0\n"
    "buffer_store_dwordx4 v[104:107], v15, s[52:55], s72 offen sc1\n"
    "s_add_u32 s72, s72, 57344\n"
    "v_pk_mul_f32 v[176:177], v[82:83], v[46:47] op_sel:[0,0] op_sel_hi:[0,1]\n"
    "v_pk_mul_f32 v[178:179], v[84:85], v[48:49] op_sel:[0,0] op_sel_hi:[0,1]\n"
    "v_pk_mul_f32 v[180:181], v[74:75], v[50:51] op_sel:[0,0] op_sel_hi:[0,1] neg_hi:[0,1]\n"
    "v_pk_mul_f32 v[182:183], v[76:77], v[52:53] op_sel:[0,0] op_sel_hi:[0,1] neg_hi:[0,1]\n"
    "v_cvt_pk_f16_f32 v118, v82, v83\n"
    "v_cvt_pk_f16_f32 v119, v84, v85\n"
    "v_pk_fma_f32 v[82:83], v[82:83], v[46:47], v[176:177] op_sel:[1,1,0] op_sel_hi:[1,0,1] neg_lo:[0,1,0]\n"
    "v_pk_fma_f32 v[84:85], v[84:85], v[48:49], v[178:179] op_sel:[1,1,0] op_sel_hi:[1,0,1] neg_lo:[0,1,0]\n"
    "ds_write_addtid_b32 v118 offset:32736\n"
    "ds_write_addtid_b32 v119 offset:32992\n"
    "v_cvt_pk_f16_f32 v120, v74, v75\n"
    "v_cvt_pk_f16_f32 v121, v76, v77\n"
    "v_pk_fma_f32 v[74:75], v[74:75], v[50:51], v[180:181] op_sel:[1,1,0] op_sel_hi:[1,0,1]\n"
    "v_pk_fma_f32 v[76:77], v[76:77], v[52:53], v[182:183] op_sel:[1,1,0] op_sel_hi:[1,0,1]\n"
    "ds_write_addtid_b32 v120 offset:12672\n"
    "ds_write_addtid_b32 v121 offset:12928\n"
    "s_waitcnt lgkmcnt(15)\n"
    "s_nop 0\n"
    "buffer_store_dwordx4 v[108:111], v15, s[52:55], s72 offen sc1\n"
    "v_pk_mul_f32 v[176:177], v[82:83], v[46:47] op_sel:[0,0] op_sel_hi:[0,1]\n"
    "v_pk_mul_f32 v[178:179], v[84:85], v[48:49] op_sel:[0,0] op_sel_hi:[0,1]\n"
    "v_pk_mul_f32 v[180:181], v[74:75], v[50:51] op_sel:[0,0] op_sel_hi:[0,1] neg_hi:[0,1]\n"
    "v_pk_mul_f32 v[182:183], v[76:77], v[52:53] op_sel:[0,0] op_sel_hi:[0,1] neg_hi:[0,1]\n"
    "v_cvt_pk_f16_f32 v122, v82, v83\n"
    "v_cvt_pk_f16_f32 v123, v84, v85\n"
    "v_pk_fma_f32 v[82:83], v[82:83], v[46:47], v[176:177] op_sel:[1,1,0] op_sel_hi:[1,0,1] neg_lo:[0,1,0]\n"
    "v_pk_fma_f32 v[84:85], v[84:85], v[48:49], v[178:179] op_sel:[1,1,0] op_sel_hi:[1,0,1] neg_lo:[0,1,0]\n"
    "ds_write_addtid_b32 v122 offset:33792\n"
    "ds_write_addtid_b32 v123 offset:34048\n"
    "v_cvt_pk_f16_f32 v124, v74, v75\n"
    "v_cvt_pk_f16_f32 v125, v76, v77\n"
    "v_pk_fma_f32 v[74:75], v[74:75], v[50:51], v[180:181] op_sel:[1,1,0] op_sel_hi:[1,0,1]\n"
    "v_pk_fma_f32 v[76:77], v[76:77], v[52:53], v[182:183] op_sel:[1,1,0] op_sel_hi:[1,0,1]\n"
    "ds_write_addtid_b32 v124 offset:14784\n"
    "ds_write_addtid_b32 v125 offset:15040\n"
    "v_pk_mul_f32 v[176:177], v[82:83], v[46:47] op_sel:[0,0] op_sel_hi:[0,1]\n"
    "v_pk_mul_f32 v[178:179], v[84:85], v[48:49] op_sel:[0,0] op_sel_hi:[0,1]\n"
    "v_pk_mul_f32 v[180:181], v[74:75], v[50:51] op_sel:[0,0] op_sel_hi:[0,1] neg_hi:[0,1]\n"
    "v_pk_mul_f32 v[182:183], v[76:77], v[52:53] op_sel:[0,0] op_sel_hi:[0,1] neg_hi:[0,1]\n"
    "v_cvt_pk_f16_f32 v126, v82, v83\n"
    "v_cvt_pk_f16_f32 v127, v84, v85\n"
    "v_pk_fma_f32 v[82:83], v[82:83], v[46:47], v[176:177] op_sel:[1,1,0] op_sel_hi:[1,0,1] neg_lo:[0,1,0]\n"
    "v_pk_fma_f32 v[84:85], v[84:85], v[48:49], v[178:179] op_sel:[1,1,0] op_sel_hi:[1,0,1] neg_lo:[0,1,0]\n"
    "ds_write_addtid_b32 v126 offset:34848\n"
    "ds_write_addtid_b32 v127 offset:35104\n"
    "v_cvt_pk_f16_f32 v112, v74, v75\n"
    "v_cvt_pk_f16_f32 v113, v76, v77\n"
    "v_pk_fma_f32 v[74:75], v[74:75], v[50:51], v[180:181] op_sel:[1,1,0] op_sel_hi:[1,0,1]\n"
    "v_pk_fma_f32 v[76:77], v[76:77], v[52:53], v[182:183] op_sel:[1,1,0] op_sel_hi:[1,0,1]\n"
    "ds_write_addtid_b32 v112 offset:16896\n"
    "ds_write_addtid_b32 v113 offset:17152\n"
    "v_pk_mul_f32 v[176:177], v[82:83], v[46:47] op_sel:[0,0] op_sel_hi:[0,1]\n"
    "v_pk_mul_f32 v[178:179], v[84:85], v[48:49] op_sel:[0,0] op_sel_hi:[0,1]\n"
    "v_pk_mul_f32 v[180:181], v[74:75], v[50:51] op_sel:[0,0] op_sel_hi:[0,1] neg_hi:[0,1]\n"
    "v_pk_mul_f32 v[182:183], v[76:77], v[52:53] op_sel:[0,0] op_sel_hi:[0,1] neg_hi:[0,1]\n"
    "v_cvt_pk_f16_f32 v114, v82, v83\n"
    "v_cvt_pk_f16_f32 v115, v84, v85\n"
    "v_pk_fma_f32 v[82:83], v[82:83], v[46:47], v[176:177] op_sel:[1,1,0] op_sel_hi:[1,0,1] neg_lo:[0,1,0]\n"
    "v_pk_fma_f32 v[84:85], v[84:85], v[48:49], v[178:179] op_sel:[1,1,0] op_sel_hi:[1,0,1] neg_lo:[0,1,0]\n"
    "ds_write_addtid_b32 v114 offset:35904\n"
    "ds_write_addtid_b32 v115 offset:36160\n"
    "v_cvt_pk_f16_f32 v116, v74, v75\n"
    "v_cvt_pk_f16_f32 v117, v76, v77\n"
    "v_pk_fma_f32 v[74:75], v[74:75], v[50:51], v[180:181] op_sel:[1,1,0] op_sel_hi:[1,0,1]\n"
    "v_pk_fma_f32 v[76:77], v[76:77], v[52:53], v[182:183] op_sel:[1,1,0] op_sel_hi:[1,0,1]\n"
    "ds_write_addtid_b32 v116 offset:17952\n"
    "ds_write_addtid_b32 v117 offset:18208\n"
    "v_pk_mul_f32 v[176:177], v[82:83], v[46:47] op_sel:[0,0] op_sel_hi:[0,1]\n"
    "v_pk_mul_f32 v[178:179], v[84:85], v[48:49] op_sel:[0,0] op_sel_hi:[0,1]\n"
    "v_pk_mul_f32 v[180:181], v[74:75], v[50:51] op_sel:[0,0] op_sel_hi:[0,1] neg_hi:[0,1]\n"
    "v_pk_mul_f32 v[182:183], v[76:77], v[52:53] op_sel:[0,0] op_sel_hi:[0,1] neg_hi:[0,1]\n"
    "v_cvt_pk_f16_f32 v118, v82, v83\n"
    "v_cvt_pk_f16_f32 v119, v84, v85\n"
    "v_pk_fma_f32 v[82:83], v[82:83], v[46:47], v[176:177] op_sel:[1,1,0] op_sel_hi:[1,0,1] neg_lo:[0,1,0]\n"
    "v_pk_fma_f32 v[84:85], v[84:85], v[48:49], v[178:179] op_sel:[1,1,0] op_sel_hi:[1,0,1] neg_lo:[0,1,0]\n"
    "ds_write_addtid_b32 v118 offset:36960\n"
    "ds_write_addtid_b32 v119 offset:37216\n"
    "v_cvt_pk_f16_f32 v120, v74, v75\n"
    "v_cvt_pk_f16_f32 v121, v76, v77\n"
    "v_pk_fma_f32 v[74:75], v[74:75], v[50:51], v[180:181] op_sel:[1,1,0] op_sel_hi:[1,0,1]\n"
    "v_pk_fma_f32 v[76:77], v[76:77], v[52:53], v[182:183] op_sel:[1,1,0] op_sel_hi:[1,0,1]\n"
    "ds_write_addtid_b32 v120 offset:19008\n"
    "ds_write_addtid_b32 v121 offset:19264\n"
    "v_pk_mul_f32 v[176:177], v[82:83], v[46:47] op_sel:[0,0] op_sel_hi:[0,1]\n"
    "v_pk_mul_f32 v[178:179], v[84:85], v[48:49] op_sel:[0,0] op_sel_hi:[0,1]\n"
    "v_pk_mul_f32 v[180:181], v[74:75], v[50:51] op_sel:[0,0] op_sel_hi:[0,1] neg_hi:[0,1]\n"
    "v_pk_mul_f32 v[182:183], v[76:77], v[52:53] op_sel:[0,0] op_sel_hi:[0,1] neg_hi:[0,1]\n"
    "v_cvt_pk_f16_f32 v122, v82, v83\n"
    "v_cvt_pk_f16_f32 v123, v84, v85\n"
    "v_pk_fma_f32 v[82:83], v[82:83], v[46:47], v[176:177] op_sel:[1,1,0] op_sel_hi:[1,0,1] neg_lo:[0,1,0]\n"
    "v_pk_fma_f32 v[84:85], v[84:85], v[48:49], v[178:179] op_sel:[1,1,0] op_sel_hi:[1,0,1] neg_lo:[0,1,0]\n"
    "ds_write_addtid_b32 v122 offset:38016\n"
    "ds_write_addtid_b32 v123 offset:38272\n"
    "v_cvt_pk_f16_f32 v124, v74, v75\n"
    "v_cvt_pk_f16_f32 v125, v76, v77\n"
    "v_pk_fma_f32 v[74:75], v[74:75], v[50:51], v[180:181] op_sel:[1,1,0] op_sel_hi:[1,0,1]\n"
    "v_pk_fma_f32 v[76:77], v[76:77], v[52:53], v[182:183] op_sel:[1,1,0] op_sel_hi:[1,0,1]\n"
    "ds_write_addtid_b32 v124 offset:20064\n"
    "ds_write_addtid_b32 v125 offset:20320\n"
    "v_pk_mul_f32 v[176:177], v[82:83], v[46:47] op_sel:[0,0] op_sel_hi:[0,1]\n"
    "v_pk_mul_f32 v[178:179], v[84:85], v[48:49] op_sel:[0,0] op_sel_hi:[0,1]\n"
    "v_pk_mul_f32 v[180:181], v[62:63], v[50:51] op_sel:[0,0] op_sel_hi:[0,1] neg_hi:[0,1]\n"
    "v_pk_mul_f32 v[182:183], v[64:65], v[52:53] op_sel:[0,0] op_sel_hi:[0,1] neg_hi:[0,1]\n"
    "v_cvt_pk_f16_f32 v126, v82, v83\n"
    "v_cvt_pk_f16_f32 v127, v84, v85\n"
    "v_pk_fma_f32 v[82:83], v[82:83], v[46:47], v[176:177] op_sel:[1,1,0] op_sel_hi:[1,0,1] neg_lo:[0,1,0]\n"
    "v_pk_fma_f32 v[84:85], v[84:85], v[48:49], v[178:179] op_sel:[1,1,0] op_sel_hi:[1,0,1] neg_lo:[0,1,0]\n"
    "ds_write_addtid_b32 v126 offset:39072\n"
    "ds_write_addtid_b32 v127 offset:39328\n"
    "v_cvt_pk_f16_f32 v112, v74, v75\n"
    "v_cvt_pk_f16_f32 v113, v76, v77\n"
    "v_pk_fma_f32 v[78:79], v[62:63], v[50:51], v[180:181] op_sel:[1,1,0] op_sel_hi:[1,0,1]\n"
    "v_pk_fma_f32 v[80:81], v[64:65], v[52:53], v[182:183] op_sel:[1,1,0] op_sel_hi:[1,0,1]\n"
    "ds_write_addtid_b32 v112 offset:21120\n"
    "ds_write_addtid_b32 v113 offset:21376\n"
    "v_pk_mul_f32 v[176:177], v[82:83], v[46:47] op_sel:[0,0] op_sel_hi:[0,1]\n"
    "v_pk_mul_f32 v[178:179], v[84:85], v[48:49] op_sel:[0,0] op_sel_hi:[0,1]\n"
    "v_pk_mul_f32 v[180:181], v[70:71], v[50:51] op_sel:[0,0] op_sel_hi:[0,1] neg_hi:[0,1]\n"
    "v_pk_mul_f32 v[182:183], v[72:73], v[52:53] op_sel:[0,0] op_sel_hi:[0,1] neg_hi:[0,1]\n"
    "v_cvt_pk_f16_f32 v114, v82, v83\n"
    "v_cvt_pk_f16_f32 v115, v84, v85\n"
    "v_pk_fma_f32 v[82:83], v[82:83], v[46:47], v[176:177] op_sel:[1,1,0] op_sel_hi:[1,0,1] neg_lo:[0,1,0]\n"
    "v_pk_fma_f32 v[84:85], v[84:85], v[48:49], v[178:179] op_sel:[1,1,0] op_sel_hi:[1,0,1] neg_lo:[0,1,0]\n"
    "ds_write_addtid_b32 v114 offset:40128\n"
    "ds_write_addtid_b32 v115 offset:40384\n"
    "v_cvt_pk_f16_f32 v116, v78, v79\n"
    "v_cvt_pk_f16_f32 v117, v80, v81\n"
    "v_pk_fma_f32 v[74:75], v[70:71], v[50:51], v[180:181] op_sel:[1,1,0] op_sel_hi:[1,0,1]\n"
    "v_pk_fma_f32 v[76:77], v[72:73], v[52:53], v[182:183] op_sel:[1,1,0] op_sel_hi:[1,0,1]\n"
    "ds_write_addtid_b32 v116 offset:9504\n"
    "ds_write_addtid_b32 v117 offset:9760\n"
    "v_pk_mul_f32 v[176:177], v[82:83], v[46:47] op_sel:[0,0] op_sel_hi:[0,1]\n"
    "v_pk_mul_f32 v[178:179], v[84:85], v[48:49] op_sel:[0,0] op_sel_hi:[0,1]\n"
    "v_pk_mul_f32 v[180:181], v[66:67], v[50:51] op_sel:[0,0] op_sel_hi:[0,1] neg_hi:[0,1]\n"
    "v_pk_mul_f32 v[182:183], v[68:69], v[52:53] op_sel:[0,0] op_sel_hi:[0,1] neg_hi:[0,1]\n"
    "v_cvt_pk_f16_f32 v118, v82, v83\n"
    "v_cvt_pk_f16_f32 v119, v84, v85\n"
    "v_pk_fma_f32 v[82:83], v[82:83], v[46:47], v[176:177] op_sel:[1,1,0] op_sel_hi:[1,0,1] neg_lo:[0,1,0]\n"
    "v_pk_fma_f32 v[84:85], v[84:85], v[48:49], v[178:179] op_sel:[1,1,0] op_sel_hi:[1,0,1] neg_lo:[0,1,0]\n"
    "ds_write_addtid_b32 v118 offset:41184\n"
    "ds_write_addtid_b32 v119 offset:41440\n"
    "v_cvt_pk_f16_f32 v120, v74, v75\n"
    "v_cvt_pk_f16_f32 v121, v76, v77\n"
    "v_pk_fma_f32 v[78:79], v[66:67], v[50:51], v[180:181] op_sel:[1,1,0] op_sel_hi:[1,0,1]\n"
    "v_pk_fma_f32 v[80:81], v[68:69], v[52:53], v[182:183] op_sel:[1,1,0] op_sel_hi:[1,0,1]\n"
    "ds_write_addtid_b32 v120 offset:11616\n"
    "ds_write_addtid_b32 v121 offset:11872\n"
    "v_pk_mul_f32 v[176:177], v[82:83], v[46:47] op_sel:[0,0] op_sel_hi:[0,1]\n"
    "v_pk_mul_f32 v[178:179], v[84:85], v[48:49] op_sel:[0,0] op_sel_hi:[0,1]\n"
    "v_pk_mul_f32 v[180:181], v[78:79], v[50:51] op_sel:[0,0] op_sel_hi:[0,1] neg_hi:[0,1]\n"
    "v_pk_mul_f32 v[182:183], v[80:81], v[52:53] op_sel:[0,0] op_sel_hi:[0,1] neg_hi:[0,1]\n"
    "v_cvt_pk_f16_f32 v122, v82, v83\n"
    "v_cvt_pk_f16_f32 v123, v84, v85\n"
    "v_pk_fma_f32 v[82:83], v[82:83], v[46:47], v[176:177] op_sel:[1,1,0] op_sel_hi:[1,0,1] neg_lo:[0,1,0]\n"
    "v_pk_fma_f32 v[84:85], v[84:85], v[48:49], v[178:179] op_sel:[1,1,0] op_sel_hi:[1,0,1] neg_lo:[0,1,0]\n"
    "ds_write_addtid_b32 v122 offset:42240\n"
    "ds_write_addtid_b32 v123 offset:42496\n"
    "v_cvt_pk_f16_f32 v124, v78, v79\n"
    "v_cvt_pk_f16_f32 v125, v80, v81\n"
    "v_pk_fma_f32 v[78:79], v[78:79], v[50:51], v[180:181] op_sel:[1,1,0] op_sel_hi:[1,0,1]\n"
    "v_pk_fma_f32 v[80:81], v[80:81], v[52:53], v[182:183] op_sel:[1,1,0] op_sel_hi:[1,0,1]\n"
    "ds_write_addtid_b32 v124 offset:10560\n"
    "ds_write_addtid_b32 v125 offset:10816\n"
    "v_pk_mul_f32 v[176:177], v[82:83], v[46:47] op_sel:[0,0] op_sel_hi:[0,1]\n"
    "v_pk_mul_f32 v[178:179], v[84:85], v[48:49] op_sel:[0,0] op_sel_hi:[0,1]\n"
    "v_pk_mul_f32 v[180:181], v[78:79], v[50:51] op_sel:[0,0] op_sel_hi:[0,1] neg_hi:[0,1]\n"
    "v_pk_mul_f32 v[182:183], v[80:81], v[52:53] op_sel:[0,0] op_sel_hi:[0,1] neg_hi:[0,1]\n"
    "v_cvt_pk_f16_f32 v126, v82, v83\n"
    "v_cvt_pk_f16_f32 v127, v84, v85\n"
    "v_pk_fma_f32 v[82:83], v[82:83], v[46:47], v[176:177] op_sel:[1,1,0] op_sel_hi:[1,0,1] neg_lo:[0,1,0]\n"
    "v_pk_fma_f32 v[84:85], v[84:85], v[48:49], v[178:179] op_sel:[1,1,0] op_sel_hi:[1,0,1] neg_lo:[0,1,0]\n"
    "ds_write_addtid_b32 v126 offset:43296\n"
    "ds_write_addtid_b32 v127 offset:43552\n"
    "v_cvt_pk_f16_f32 v112, v78, v79\n"
    "v_cvt_pk_f16_f32 v113, v80, v81\n"
    "v_pk_fma_f32 v[78:79], v[78:79], v[50:51], v[180:181] op_sel:[1,1,0] op_sel_hi:[1,0,1]\n"
    "v_pk_fma_f32 v[80:81], v[80:81], v[52:53], v[182:183] op_sel:[1,1,0] op_sel_hi:[1,0,1]\n"
    "ds_write_addtid_b32 v112 offset:13728\n"
    "ds_write_addtid_b32 v113 offset:13984\n"
    "v_pk_mul_f32 v[176:177], v[82:83], v[46:47] op_sel:[0,0] op_sel_hi:[0,1]\n"
    "v_pk_mul_f32 v[178:179], v[84:85], v[48:49] op_sel:[0,0] op_sel_hi:[0,1]\n"
    "v_cvt_pk_f16_f32 v114, v82, v83\n"
    "v_cvt_pk_f16_f32 v115, v84, v85\n"
    "v_pk_fma_f32 v[82:83], v[82:83], v[46:47], v[176:177] op_sel:[1,1,0] op_sel_hi:[1,0,1] neg_lo:[0,1,0]\n"
    "v_pk_fma_f32 v[84:85], v[84:85], v[48:49], v[178:179] op_sel:[1,1,0] op_sel_hi:[1,0,1] neg_lo:[0,1,0]\n"
    "ds_write_addtid_b32 v114 offset:44352\n"
    "ds_write_addtid_b32 v115 offset:44608\n"
    "v_cvt_pk_f16_f32 v116, v78, v79\n"
    "v_cvt_pk_f16_f32 v117, v80, v81\n"
    "ds_write_addtid_b32 v116 offset:15840\n"
    "ds_write_addtid_b32 v117 offset:16096\n"
    "s_waitcnt vmcnt(0)\n"
    "s_mov_b64 exec, 1\n"
    "buffer_store_dword v231, off, s[52:55], s80 offset:0 sc1\n"
    "s_mov_b64 exec, -1\n"
    "s_nop 0\n"
    "v_pk_mul_f32 v[180:181], v[82:83], v[46:47] op_sel:[0,0] op_sel_hi:[0,1]\n"
    "v_pk_mul_f32 v[182:183], v[84:85], v[48:49] op_sel:[0,0] op_sel_hi:[0,1]\n"
    "v_cvt_pk_f16_f32 v118, v82, v83\n"
    "v_cvt_pk_f16_f32 v119, v84, v85\n"
    "v_pk_fma_f32 v[82:83], v[82:83], v[46:47], v[180:181] op_sel:[1,1,0] op_sel_hi:[1,0,1] neg_lo:[0,1,0]\n"
    "v_pk_fma_f32 v[84:85], v[84:85], v[48:49], v[182:183] op_sel:[1,1,0] op_sel_hi:[1,0,1] neg_lo:[0,1,0]\n"
    "ds_write_addtid_b32 v118 offset:45408\n"
    "ds_write_addtid_b32 v119 offset:45664\n"
    "v_pk_mul_f32 v[176:177], v[82:83], v[46:47] op_sel:[0,0] op_sel_hi:[0,1]\n"
    "v_pk_mul_f32 v[178:179], v[84:85], v[48:49] op_sel:[0,0] op_sel_hi:[0,1]\n"
    "v_cvt_pk_f16_f32 v120, v82, v83\n"
    "v_cvt_pk_f16_f32 v121, v84, v85\n"
    "v_pk_fma_f32 v[82:83], v[82:83], v[46:47], v[176:177] op_sel:[1,1,0] op_sel_hi:[1,0,1] neg_lo:[0,1,0]\n"
    "v_pk_fma_f32 v[84:85], v[84:85], v[48:49], v[178:179] op_sel:[1,1,0] op_sel_hi:[1,0,1] neg_lo:[0,1,0]\n"
    "ds_write_addtid_b32 v120 offset:46464\n"
    "ds_write_addtid_b32 v121 offset:46720\n"
    "v_pk_mul_f32 v[180:181], v[82:83], v[46:47] op_sel:[0,0] op_sel_hi:[0,1]\n"
    "v_pk_mul_f32 v[182:183], v[84:85], v[48:49] op_sel:[0,0] op_sel_hi:[0,1]\n"
    "v_cvt_pk_f16_f32 v122, v82, v83\n"
    "v_cvt_pk_f16_f32 v123, v84, v85\n"
    "v_pk_fma_f32 v[82:83], v[82:83], v[46:47], v[180:181] op_sel:[1,1,0] op_sel_hi:[1,0,1] neg_lo:[0,1,0]\n"
    "v_pk_fma_f32 v[84:85], v[84:85], v[48:49], v[182:183] op_sel:[1,1,0] op_sel_hi:[1,0,1] neg_lo:[0,1,0]\n"
    "ds_write_addtid_b32 v122 offset:47520\n"
    "ds_write_addtid_b32 v123 offset:47776\n"
    "v_pk_mul_f32 v[176:177], v[82:83], v[46:47] op_sel:[0,0] op_sel_hi:[0,1]\n"
    "v_pk_mul_f32 v[178:179], v[84:85], v[48:49] op_sel:[0,0] op_sel_hi:[0,1]\n"
    "v_cvt_pk_f16_f32 v124, v82, v83\n"
    "v_cvt_pk_f16_f32 v125, v84, v85\n"
    "v_pk_fma_f32 v[82:83], v[82:83], v[46:47], v[176:177] op_sel:[1,1,0] op_sel_hi:[1,0,1] neg_lo:[0,1,0]\n"
    "v_pk_fma_f32 v[84:85], v[84:85], v[48:49], v[178:179] op_sel:[1,1,0] op_sel_hi:[1,0,1] neg_lo:[0,1,0]\n"
    "ds_write_addtid_b32 v124 offset:48576\n"
    "ds_write_addtid_b32 v125 offset:48832\n"
    "s_lshl_b32 s16, s68, 6\n"
    "s_nop 0\n"
    "s_add_u32 s16, s16, 98304\n"
    "v_pk_mul_f32 v[180:181], v[82:83], v[46:47] op_sel:[0,0] op_sel_hi:[0,1]\n"
    "v_pk_mul_f32 v[182:183], v[84:85], v[48:49] op_sel:[0,0] op_sel_hi:[0,1]\n"
    "v_cvt_pk_f16_f32 v126, v82, v83\n"
    "v_cvt_pk_f16_f32 v127, v84, v85\n"
    "v_pk_fma_f32 v[82:83], v[82:83], v[46:47], v[180:181] op_sel:[1,1,0] op_sel_hi:[1,0,1] neg_lo:[0,1,0]\n"
    "v_pk_fma_f32 v[84:85], v[84:85], v[48:49], v[182:183] op_sel:[1,1,0] op_sel_hi:[1,0,1] neg_lo:[0,1,0]\n"
    "ds_write_addtid_b32 v126 offset:49632\n"
    "ds_write_addtid_b32 v127 offset:49888\n"
    "buffer_load_dwordx4 v[206:209], v184, s[52:55], s16 offen sc1\n"
    "s_add_u32 s16, s16, 917504\n"
    "v_pk_mul_f32 v[176:177], v[82:83], v[46:47] op_sel:[0,0] op_sel_hi:[0,1]\n"
    "v_pk_mul_f32 v[178:179], v[84:85], v[48:49] op_sel:[0,0] op_sel_hi:[0,1]\n"
    "v_cvt_pk_f16_f32 v112, v82, v83\n"
    "v_cvt_pk_f16_f32 v113, v84, v85\n"
    "v_pk_fma_f32 v[82:83], v[82:83], v[46:47], v[176:177] op_sel:[1,1,0] op_sel_hi:[1,0,1] neg_lo:[0,1,0]\n"
    "v_pk_fma_f32 v[84:85], v[84:85], v[48:49], v[178:179] op_sel:[1,1,0] op_sel_hi:[1,0,1] neg_lo:[0,1,0]\n"
    "ds_write_addtid_b32 v112 offset:50688\n"
    "ds_write_addtid_b32 v113 offset:50944\n"
    "buffer_load_dwordx4 v[210:213], v184, s[52:55], s16 offen sc1\n"
    "s_add_u32 s16, s16, 917504\n"
    "v_pk_mul_f32 v[180:181], v[82:83], v[46:47] op_sel:[0,0] op_sel_hi:[0,1]\n"
    "v_pk_mul_f32 v[182:183], v[84:85], v[48:49] op_sel:[0,0] op_sel_hi:[0,1]\n"
    "v_cvt_pk_f16_f32 v114, v82, v83\n"
    "v_cvt_pk_f16_f32 v115, v84, v85\n"
    "v_pk_fma_f32 v[82:83], v[82:83], v[46:47], v[180:181] op_sel:[1,1,0] op_sel_hi:[1,0,1] neg_lo:[0,1,0]\n"
    "v_pk_fma_f32 v[84:85], v[84:85], v[48:49], v[182:183] op_sel:[1,1,0] op_sel_hi:[1,0,1] neg_lo:[0,1,0]\n"
    "ds_write_addtid_b32 v114 offset:51744\n"
    "ds_write_addtid_b32 v115 offset:52000\n"
    "buffer_load_dwordx4 v[214:217], v184, s[52:55], s16 offen sc1\n"
    "s_add_u32 s16, s16, 917504\n"
    "v_pk_mul_f32 v[176:177], v[82:83], v[46:47] op_sel:[0,0] op_sel_hi:[0,1]\n"
    "v_pk_mul_f32 v[178:179], v[84:85], v[48:49] op_sel:[0,0] op_sel_hi:[0,1]\n"
    "v_cvt_pk_f16_f32 v116, v82, v83\n"
    "v_cvt_pk_f16_f32 v117, v84, v85\n"
    "v_pk_fma_f32 v[82:83], v[82:83], v[46:47], v[176:177] op_sel:[1,1,0] op_sel_hi:[1,0,1] neg_lo:[0,1,0]\n"
    "v_pk_fma_f32 v[84:85], v[84:85], v[48:49], v[178:179] op_sel:[1,1,0] op_sel_hi:[1,0,1] neg_lo:[0,1,0]\n"
    "ds_write_addtid_b32 v116 offset:52800\n"
    "ds_write_addtid_b32 v117 offset:53056\n"
    "buffer_load_dwordx4 v[218:221], v184, s[52:55], s16 offen sc1\n"
    "v_pk_mul_f32 v[180:181], v[82:83], v[46:47] op_sel:[0,0] op_sel_hi:[0,1]\n"
    "v_pk_mul_f32 v[182:183], v[84:85], v[48:49] op_sel:[0,0] op_sel_hi:[0,1]\n"
    "v_cvt_pk_f16_f32 v118, v82, v83\n"
    "v_cvt_pk_f16_f32 v119, v84, v85\n"
    "v_pk_fma_f32 v[82:83], v[82:83], v[46:47], v[180:181] op_sel:[1,1,0] op_sel_hi:[1,0,1] neg_lo:[0,1,0]\n"
    "v_pk_fma_f32 v[84:85], v[84:85], v[48:49], v[182:183] op_sel:[1,1,0] op_sel_hi:[1,0,1] neg_lo:[0,1,0]\n"
    "ds_write_addtid_b32 v118 offset:53856\n"
    "ds_write_addtid_b32 v119 offset:54112\n"
    "v_cvt_pk_f16_f32 v120, v82, v83\n"
    "v_cvt_pk_f16_f32 v121, v84, v85\n"
    "ds_write_addtid_b32 v120 offset:54912\n"
    "ds_write_addtid_b32 v121 offset:55168\n"
    "s_waitcnt lgkmcnt(0)\n"
    "s_barrier\n"
    "ds_read_b128 v[2:5], v173 offset:0\n"
    "ds_read_b128 v[10:13], v175 offset:0\n"
    "ds_read_b128 v[6:9], v174 offset:0\n"
    "ds_read_b128 v[14:17], v173 offset:64\n"
    "ds_read_b128 v[22:25], v175 offset:64\n"
    "ds_read_b128 v[18:21], v174 offset:64\n"
    "ds_read_b128 v[26:29], v173 offset:128\n"
    "ds_read_b128 v[34:37], v175 offset:128\n"
    "ds_read_b128 v[30:33], v174 offset:128\n"
    "ds_write_b32 v226, v228\n"
    "ds_write_b32 v227, v228\n"
    "s_waitcnt lgkmcnt(9)\n"
    "v_mfma_f32_16x16x32_f16 v[50:53], v[2:5], v[10:13], 0\n"
    "ds_read_b128 v[38:41], v173 offset:192\n"
    "ds_read_b128 v[46:49], v175 offset:192\n"
    "s_waitcnt lgkmcnt(10)\n"
    "v_mfma_f32_16x16x32_f16 v[54:57], v[6:9], v[10:13], 0\n"
    "ds_read_b128 v[42:45], v174 offset:192\n"
    "s_mov_b32 s16, 32768\n"
    "buffer_load_dwordx4 v[98:101], v230, s[52:55], s16 offen sc1\n"
    "s_waitcnt lgkmcnt(9)\n"
    "v_mfma_f32_16x16x32_f16 v[50:53], v[14:17], v[22:25], v[50:53]\n"
    "ds_read_b128 v[2:5], v173 offset:256\n"
    "ds_read_b128 v[10:13], v175 offset:256\n"
    "s_waitcnt lgkmcnt(10)\n"
    "v_mfma_f32_16x16x32_f16 v[54:57], v[18:21], v[22:25], v[54:57]\n"
    "ds_read_b128 v[6:9], v174 offset:256\n"
    "s_waitcnt lgkmcnt(9)\n"
    "v_mfma_f32_16x16x32_f16 v[50:53], v[26:29], v[34:37], v[50:53]\n"
    "ds_read_b128 v[14:17], v173 offset:320\n"
    "ds_read_b128 v[22:25], v175 offset:320\n"
    "s_waitcnt lgkmcnt(10)\n"
    "v_mfma_f32_16x16x32_f16 v[54:57], v[30:33], v[34:37], v[54:57]\n"
    "ds_read_b128 v[18:21], v174 offset:320\n"
    "s_mov_b32 s16, 36864\n"
    "buffer_load_dwordx4 v[102:105], v230, s[52:55], s16 offen sc1\n"
    "s_waitcnt lgkmcnt(7)\n"
    "v_mfma_f32_16x16x32_f16 v[50:53], v[38:41], v[46:49], v[50:53]\n"
    "ds_read_b128 v[26:29], v173 offset:384\n"
    "ds_read_b128 v[34:37], v175 offset:384\n"
    "s_waitcnt lgkmcnt(8)\n"
    "v_mfma_f32_16x16x32_f16 v[54:57], v[42:45], v[46:49], v[54:57]\n"
    "ds_read_b128 v[30:33], v174 offset:384\n"
    "s_waitcnt lgkmcnt(7)\n"
    "v_mfma_f32_16x16x32_f16 v[50:53], v[2:5], v[10:13], v[50:53]\n"
    "ds_read_b128 v[38:41], v173 offset:448\n"
    "ds_read_b128 v[46:49], v175 offset:448\n"
    "s_waitcnt lgkmcnt(8)\n"
    "v_mfma_f32_16x16x32_f16 v[54:57], v[6:9], v[10:13], v[54:57]\n"
    "ds_read_b128 v[42:45], v174 offset:448\n"
    "s_mov_b32 s16, 40960\n"
    "buffer_load_dwordx4 v[106:109], v230, s[52:55], s16 offen sc1\n"
    "s_waitcnt lgkmcnt(7)\n"
    "v_mfma_f32_16x16x32_f16 v[50:53], v[14:17], v[22:25], v[50:53]\n"
    "ds_read_b128 v[2:5], v173 offset:512\n"
    "ds_read_b128 v[10:13], v175 offset:512\n"
    "s_waitcnt lgkmcnt(8)\n"
    "v_mfma_f32_16x16x32_f16 v[54:57], v[18:21], v[22:25], v[54:57]\n"
    "ds_read_b128 v[6:9], v174 offset:512\n"
    "s_waitcnt lgkmcnt(7)\n"
    "v_mfma_f32_16x16x32_f16 v[50:53], v[26:29], v[34:37], v[50:53]\n"
    "ds_read_b128 v[14:17], v173 offset:576\n"
    "ds_read_b128 v[22:25], v175 offset:576\n"
    "s_waitcnt lgkmcnt(8)\n"
    "v_mfma_f32_16x16x32_f16 v[54:57], v[30:33], v[34:37], v[54:57]\n"
    "ds_read_b128 v[18:21], v174 offset:576\n"
    "s_mov_b32 s16, 45056\n"
    "buffer_load_dwordx4 v[110:113], v230, s[52:55], s16 offen sc1\n"
    "s_waitcnt lgkmcnt(7)\n"
    "v_mfma_f32_16x16x32_f16 v[50:53], v[38:41], v[46:49], v[50:53]\n"
    "ds_read_b128 v[26:29], v173 offset:640\n"
    "ds_read_b128 v[34:37], v175 offset:640\n"
    "s_waitcnt lgkmcnt(8)\n"
    "v_mfma_f32_16x16x32_f16 v[54:57], v[42:45], v[46:49], v[54:57]\n"
    "ds_read_b128 v[30:33], v174 offset:640\n"
    "s_waitcnt lgkmcnt(7)\n"
    "v_mfma_f32_16x16x32_f16 v[50:53], v[2:5], v[10:13], v[50:53]\n"
    "ds_read_b128 v[38:41], v173 offset:704\n"
    "ds_read_b128 v[46:49], v175 offset:704\n"
    "s_waitcnt lgkmcnt(8)\n"
    "v_mfma_f32_16x16x32_f16 v[54:57], v[6:9], v[10:13], v[54:57]\n"
    "ds_read_b128 v[42:45], v174 offset:704\n"
    "s_mov_b32 s16, 49152\n"
    "buffer_load_dwordx4 v[114:117], v230, s[52:55], s16 offen sc1\n"
    "s_waitcnt lgkmcnt(7)\n"
    "v_mfma_f32_16x16x32_f16 v[50:53], v[14:17], v[22:25], v[50:53]\n"
    "ds_read_b128 v[2:5], v173 offset:768\n"
    "ds_read_b128 v[10:13], v175 offset:768\n"
    "s_waitcnt lgkmcnt(8)\n"
    "v_mfma_f32_16x16x32_f16 v[54:57], v[18:21], v[22:25], v[54:57]\n"
    "ds_read_b128 v[6:9], v174 offset:768\n"
    "s_waitcnt lgkmcnt(7)\n"
    "v_mfma_f32_16x16x32_f16 v[50:53], v[26:29], v[34:37], v[50:53]\n"
    "ds_read_b128 v[14:17], v173 offset:832\n"
    "ds_read_b128 v[22:25], v175 offset:832\n"
    "s_waitcnt lgkmcnt(8)\n"
    "v_mfma_f32_16x16x32_f16 v[54:57], v[30:33], v[34:37], v[54:57]\n"
    "ds_read_b128 v[18:21], v174 offset:832\n"
    "s_mov_b32 s16, 53248\n"
    "buffer_load_dwordx4 v[118:121], v230, s[52:55], s16 offen sc1\n"
    "s_waitcnt lgkmcnt(7)\n"
    "v_mfma_f32_16x16x32_f16 v[50:53], v[38:41], v[46:49], v[50:53]\n"
    "ds_read_b128 v[26:29], v173 offset:896\n"
    "ds_read_b128 v[34:37], v175 offset:896\n"
    "s_waitcnt lgkmcnt(8)\n"
    "v_mfma_f32_16x16x32_f16 v[54:57], v[42:45], v[46:49], v[54:57]\n"
    "ds_read_b128 v[30:33], v174 offset:896\n"
    "s_waitcnt lgkmcnt(7)\n"
    "v_mfma_f32_16x16x32_f16 v[50:53], v[2:5], v[10:13], v[50:53]\n"
    "ds_read_b128 v[38:41], v173 offset:960\n"
    "ds_read_b128 v[46:49], v175 offset:960\n"
    "s_waitcnt lgkmcnt(8)\n"
    "v_mfma_f32_16x16x32_f16 v[54:57], v[6:9], v[10:13], v[54:57]\n"
    "ds_read_b128 v[42:45], v174 offset:960\n"
    "s_mov_b32 s16, 57344\n"
    "buffer_load_dwordx4 v[122:125], v230, s[52:55], s16 offen sc1\n"
    "s_waitcnt lgkmcnt(7)\n"
    "v_mfma_f32_16x16x32_f16 v[50:53], v[14:17], v[22:25], v[50:53]\n"
    "s_waitcnt lgkmcnt(6)\n"
    "v_mfma_f32_16x16x32_f16 v[54:57], v[18:21], v[22:25], v[54:57]\n"
    "s_waitcnt lgkmcnt(4)\n"
    "v_mfma_f32_16x16x32_f16 v[50:53], v[26:29], v[34:37], v[50:53]\n"
    "s_waitcnt lgkmcnt(3)\n"
    "v_mfma_f32_16x16x32_f16 v[54:57], v[30:33], v[34:37], v[54:57]\n"
    "s_mov_b32 s16, 61440\n"
    "buffer_load_dwordx4 v[126:129], v230, s[52:55], s16 offen sc1\n"
    "s_waitcnt lgkmcnt(1)\n"
    "v_mfma_f32_16x16x32_f16 v[50:53], v[38:41], v[46:49], v[50:53]\n"
    "s_waitcnt lgkmcnt(0)\n"
    "v_mfma_f32_16x16x32_f16 v[54:57], v[42:45], v[46:49], v[54:57]\n"
    "buffer_load_dword v240, v185, s[52:55], s14 offen sc1\n"
    "s_nop 4\n"
    "s_nop 0\n"
    "v_cvt_pk_f16_f32 v2, v50, v51\n"
    "v_cvt_pk_f16_f32 v3, v52, v53\n"
    "v_cvt_pk_f16_f32 v4, v54, v55\n"
    "v_cvt_pk_f16_f32 v5, v56, v57\n"
    "ds_write_b16 v165, v2 offset:0\n"
    "ds_write_b16 v165, v2 offset:6\n"
    "ds_write_b16_d16_hi v165, v2 offset:4\n"
    "ds_write_b16_d16_hi v165, v2 offset:10\n"
    "ds_write_b16 v170, v3 offset:0\n"
    "ds_write_b16 v170, v3 offset:6\n"
    "ds_write_b16_d16_hi v170, v3 offset:4\n"
    "ds_write_b16_d16_hi v170, v3 offset:10\n"
    "ds_write_b16 v171, v4 offset:0\n"
    "ds_write_b16 v171, v4 offset:6\n"
    "ds_write_b16_d16_hi v171, v4 offset:128\n"
    "ds_write_b16_d16_hi v171, v4 offset:134\n"
    "ds_write_b16 v171, v5 offset:256\n"
    "ds_write_b16 v171, v5 offset:262\n"
    "ds_write_b16_d16_hi v171, v5 offset:384\n"
    "ds_write_b16_d16_hi v171, v5 offset:390\n"
    "v_lshlrev_b32_e64 v165, 2, v0\n"
    "v_add_u32 v165, 118592, v165\n"
    "s_mov_b32 s17, 0x05040100\n"
    "s_mov_b32 s18, 0x07060302\n"
    "s_waitcnt vmcnt(11)\n"
    "v_min3_u32 v170, v207, v209, v211\n"
    "v_max3_u32 v171, v207, v209, v211\n"
    "s_waitcnt vmcnt(10)\n"
    "v_min3_u32 v170, v170, v213, v215\n"
    "v_max3_u32 v171, v171, v213, v215\n"
    "s_waitcnt vmcnt(9)\n"
    "v_min3_u32 v170, v170, v217, v219\n"
    "v_max3_u32 v171, v171, v217, v219\n"
    "v_min_u32 v170, v170, v221\n"
    "v_max_u32 v171, v171, v221\n"
    "v_xor_b32 v170, s60, v170\n"
    "v_xor_b32 v171, s60, v171\n"
    "v_or_b32 v170, v170, v171\n"
    "v_cmp_ne_u32 vcc, 0, v170\n"
    "s_cbranch_vccnz Lslow_ux0_%=\n"
    "Lback_ux0_%=:\n"
    "v_perm_b32 v2, v208, v206, s17\n"
    "v_perm_b32 v3, v208, v206, s18\n"
    "v_perm_b32 v4, v212, v210, s17\n"
    "v_perm_b32 v5, v212, v210, s18\n"
    "v_perm_b32 v6, v216, v214, s17\n"
    "v_perm_b32 v7, v216, v214, s18\n"
    "v_perm_b32 v8, v220, v218, s17\n"
    "v_perm_b32 v9, v220, v218, s18\n"
    "ds_write2st64_b32 v165, v2, v3 offset0:0 offset1:18\n"
    "ds_write2st64_b32 v165, v4, v5 offset0:4 offset1:22\n"
    "ds_write2st64_b32 v165, v6, v7 offset0:8 offset1:26\n"
    "ds_write2st64_b32 v165, v8, v9 offset0:12 offset1:30\n"
    "s_waitcnt vmcnt(7)\n"
    "v_min3_u32 v170, v99, v101, v103\n"
    "v_max3_u32 v171, v99, v101, v103\n"
    "s_waitcnt vmcnt(6)\n"
    "v_min3_u32 v170, v170, v105, v107\n"
    "v_max3_u32 v171, v171, v105, v107\n"
    "s_waitcnt vmcnt(5)\n"
    "v_min3_u32 v170, v170, v109, v111\n"
    "v_max3_u32 v171, v171, v109, v111\n"
    "s_waitcnt vmcnt(4)\n"
    "v_min3_u32 v170, v170, v113, v115\n"
    "v_max3_u32 v171, v171, v113, v115\n"
    "s_waitcnt vmcnt(3)\n"
    "v_min3_u32 v170, v170, v117, v119\n"
    "v_max3_u32 v171, v171, v117, v119\n"
    "s_waitcnt vmcnt(2)\n"
    "v_min3_u32 v170, v170, v121, v123\n"
    "v_max3_u32 v171, v171, v121, v123\n"
    "s_waitcnt vmcnt(1)\n"
    "v_min3_u32 v170, v170, v125, v127\n"
    "v_max3_u32 v171, v171, v125, v127\n"
    "v_min_u32 v170, v170, v129\n"
    "v_max_u32 v171, v171, v129\n"
    "v_xor_b32 v170, s60, v170\n"
    "v_xor_b32 v171, s60, v171\n"
    "v_or_b32 v170, v170, v171\n"
    "v_cmp_ne_u32 vcc, 0, v170\n"
    "s_cbranch_vccnz Lslow_ux1_%=\n"
    "Lback_ux1_%=:\n"
    "ds_write2st64_b32 v165, v98, v100 offset0:34 offset1:66\n"
    "ds_write2st64_b32 v165, v102, v104 offset0:38 offset1:70\n"
    "ds_write2st64_b32 v165, v106, v108 offset0:42 offset1:74\n"
    "ds_write2st64_b32 v165, v110, v112 offset0:46 offset1:78\n"
    "ds_write2st64_b32 v165, v114, v116 offset0:50 offset1:82\n"
    "ds_write2st64_b32 v165, v118, v120 offset0:54 offset1:86\n"
    "ds_write2st64_b32 v165, v122, v124 offset0:58 offset1:90\n"
    "ds_write2st64_b32 v165, v126, v128 offset0:62 offset1:94\n"
    "v_add_u32 v241, 4096, v233\n"
    "s_lshl2_add_u32 s18, s8, s57\n"
    "s_mul_i32 s18, s18, 57344\n"
    "s_add_u32 s18, s18, 3768320\n"
    "s_add_u32 s65, s57, 0\n"
    "s_add_u32 s64, s65, 0\n"
    "s_and_b32 s64, s64, 3\n"
    "s_mul_i32 s20, s64, 7168\n"
    "s_add_u32 s64, s65, 1\n"
    "s_and_b32 s64, s64, 3\n"
    "s_mul_i32 s21, s64, 7168\n"
    "s_add_u32 s64, s65, 2\n"
    "s_and_b32 s64, s64, 3\n"
    "s_mul_i32 s22, s64, 7168\n"
    "s_add_u32 s64, s65, 3\n"
    "s_and_b32 s64, s64, 3\n"
    "s_mul_i32 s23, s64, 7168\n"
    "s_waitcnt lgkmcnt(0)\n"
    "s_barrier\n"
    "s_cmp_lg_u32 s56, 0\n"
    "s_cbranch_scc1 Lnoadv_%=\n"
    "s_mov_b64 exec, 1\n"
    "buffer_store_dword v231, off, s[52:55], 0 offset:0 sc1\n"
    "s_mov_b64 exec, -1\n"
    "Lnoadv_%=:\n"
    "s_waitcnt vmcnt(0)\n"
    "v_cmp_ne_u32_e64 vcc, s60, v240\n"
    "s_cbranch_vccnz Lslow_f13_%=\n"
    "Lback_f13_%=:\n"
    "ds_read2_b32 v[2:3], v130 offset0:6 offset1:4\n"
    "ds_read2_b32 v[4:5], v130 offset0:2 offset1:0\n"
    "ds_read2_b32 v[6:7], v130 offset0:70 offset1:68\n"
    "ds_read2_b32 v[8:9], v130 offset0:66 offset1:64\n"
    "ds_read2_b32 v[10:11], v130 offset0:134 offset1:132\n"
    "ds_read2_b32 v[12:13], v130 offset0:130 offset1:128\n"
    "ds_read2_b32 v[14:15], v130 offset0:198 offset1:196\n"
    "ds_read2_b32 v[16:17], v130 offset0:194 offset1:192\n"
    "ds_read_b32 v18, v132\n"
    "ds_read_b32 v19, v133\n"
    "ds_read_b32 v20, v134\n"
    "ds_read_b32 v21, v135\n"
    "s_add_u32 s16, s18, s20\n"
    "s_nop 0\n"
    "s_add_u32 s17, s16, 28672\n"
    "buffer_load_dwordx4 a[0:3], v233, s[52:55], s16 offen offset:0 sc1\n"
    "ds_read2_b32 v[170:171], v156 offset0:2 offset1:0\n"
    "ds_read2_b32 v[172:173], v157 offset0:2 offset1:0\n"
    "ds_read2_b32 v[174:175], v156 offset0:34 offset1:32\n"
    "ds_read2_b32 v[176:177], v157 offset0:34 offset1:32\n"
    "ds_read2_b32 v[178:179], v156 offset0:66 offset1:64\n"
    "ds_read2_b32 v[180:181], v157 offset0:66 offset1:64\n"
    "ds_read_b32 v34, v136\n"
    "ds_read_b32 v35, v137\n"
    "ds_read_b32 v36, v138\n"
    "ds_read_b32 v37, v139\n"
    "buffer_load_dwordx4 a[4:7], v233, s[52:55], s17 offen offset:0 sc1\n"
    "s_waitcnt lgkmcnt(13)\n"
    "v_perm_b32 v18, v18, v18, v144\n"
    "s_waitcnt lgkmcnt(12)\n"
    "v_perm_b32 v19, v19, v19, v145\n"
    "s_waitcnt lgkmcnt(11)\n"
    "v_perm_b32 v20, v20, v20, v146\n"
    "s_waitcnt lgkmcnt(10)\n"
    "v_perm_b32 v21, v21, v21, v147\n"
    "s_waitcnt lgkmcnt(9)\n"
    "v_perm_b32 v22, v170, v170, v158\n"
    "s_waitcnt lgkmcnt(8)\n"
    "v_perm_b32 v23, v172, v172, v159\n"
    "v_perm_b32 v24, v171, v171, v158\n"
    "v_perm_b32 v25, v173, v173, v159\n"
    "s_waitcnt lgkmcnt(7)\n"
    "v_perm_b32 v26, v174, v174, v158\n"
    "s_waitcnt lgkmcnt(6)\n"
    "v_perm_b32 v27, v176, v176, v159\n"
    "v_perm_b32 v28, v175, v175, v158\n"
    "v_perm_b32 v29, v177, v177, v159\n"
    "s_waitcnt lgkmcnt(5)\n"
    "v_perm_b32 v30, v178, v178, v158\n"
    "s_waitcnt lgkmcnt(4)\n"
    "v_perm_b32 v31, v180, v180, v159\n"
    "v_perm_b32 v32, v179, v179, v158\n"
    "v_perm_b32 v33, v181, v181, v159\n"
    "buffer_load_dwordx4 a[8:11], v233, s[52:55], s16 offen offset:1024 sc1\n"
    "ds_read2_b32 v[170:171], v131 offset0:0 offset1:2\n"
    "ds_read2_b32 v[172:173], v131 offset0:16 offset1:18\n"
    "ds_read2_b32 v[174:175], v131 offset0:32 offset1:34\n"
    "ds_read2_b32 v[176:177], v131 offset0:48 offset1:50\n"
    "ds_read2_b32 v[178:179], v131 offset0:64 offset1:66\n"
    "ds_read2_b32 v[180:181], v131 offset0:80 offset1:82\n"
    "ds_read2_b32 v[182:183], v131 offset0:96 offset1:98\n"
    "ds_read_b32 v66, v140\n"
    "ds_read_b32 v67, v141\n"
    "ds_read_b32 v68, v142\n"
    "ds_read_b32 v69, v143\n"
    "ds_read_b32 v73, v164 offset:0\n"
    "ds_read_b32 v77, v164 offset:32\n"
    "ds_read_b32 v81, v164 offset:64\n"
    "ds_read_b32 v85, v164 offset:96\n"
    "ds_read_b32 v89, v164 offset:128\n"
    "ds_read_b32 v93, v164 offset:160\n"
    "ds_read_b32 v97, v164 offset:192\n"
    "ds_read_b32 v101, v164 offset:224\n"
    "ds_read_b32 v105, v164 offset:256\n"
    "ds_read_b32 v109, v164 offset:288\n"
    "ds_read_b32 v113, v164 offset:320\n"
    "ds_read_b32 v117, v164 offset:352\n"
    "ds_read_b32 v121, v164 offset:384\n"
    "ds_read_b32 v125, v164 offset:416\n"
    "ds_read_b32 v129, v164 offset:448\n"
    "buffer_load_dwordx4 a[12:15], v233, s[52:55], s17 offen offset:1024 sc1\n"
    "s_waitcnt lgkmcnt(15)\n"
    "v_perm_b32 v34, v34, v34, v148\n"
    "s_waitcnt lgkmcnt(15)\n"
    "v_perm_b32 v35, v35, v35, v149\n"
    "s_waitcnt lgkmcnt(15)\n"
    "v_perm_b32 v36, v36, v36, v150\n"
    "s_waitcnt lgkmcnt(15)\n"
    "v_perm_b32 v37, v37, v37, v151\n"
    "s_waitcnt lgkmcnt(15)\n"
    "v_perm_b32 v38, v171, v170, v160\n"
    "v_perm_b32 v39, v171, v170, v161\n"
    "v_perm_b32 v40, v171, v170, v162\n"
    "v_perm_b32 v41, v171, v170, v163\n"
    "s_waitcnt lgkmcnt(15)\n"
    "v_perm_b32 v42, v173, v172, v160\n"
    "v_perm_b32 v43, v173, v172, v161\n"
    "v_perm_b32 v44, v173, v172, v162\n"
    "v_perm_b32 v45, v173, v172, v163\n"
    "buffer_load_dwordx4 a[16:19], v233, s[52:55], s16 offen offset:2048 sc1\n"
    "s_waitcnt lgkmcnt(15)\n"
    "v_perm_b32 v46, v175, v174, v160\n"
    "v_perm_b32 v47, v175, v174, v161\n"
    "v_perm_b32 v48, v175, v174, v162\n"
    "v_perm_b32 v49, v175, v174, v163\n"
    "s_waitcnt lgkmcnt(15)\n"
    "v_perm_b32 v50, v177, v176, v160\n"
    "v_perm_b32 v51, v177, v176, v161\n"
    "v_perm_b32 v52, v177, v176, v162\n"
    "v_perm_b32 v53, v177, v176, v163\n"
    "buffer_load_dwordx4 a[20:23], v233, s[52:55], s17 offen offset:2048 sc1\n"
    "s_waitcnt lgkmcnt(15)\n"
    "v_perm_b32 v54, v179, v178, v160\n"
    "v_perm_b32 v55, v179, v178, v161\n"
    "v_perm_b32 v56, v179, v178, v162\n"
    "v_perm_b32 v57, v179, v178, v163\n"
    "s_waitcnt lgkmcnt(15)\n"
    "v_perm_b32 v58, v181, v180, v160\n"
    "v_perm_b32 v59, v181, v180, v161\n"
    "v_perm_b32 v60, v181, v180, v162\n"
    "v_perm_b32 v61, v181, v180, v163\n"
    "buffer_load_dwordx4 a[24:27], v233, s[52:55], s16 offen offset:3072 sc1\n"
    "s_waitcnt lgkmcnt(15)\n"
    "v_perm_b32 v62, v183, v182, v160\n"
    "v_perm_b32 v63, v183, v182, v161\n"
    "v_perm_b32 v64, v183, v182, v162\n"
    "v_perm_b32 v65, v183, v182, v163\n"
    "s_waitcnt lgkmcnt(15)\n"
    "v_perm_b32 v66, v66, v66, v152\n"
    "s_waitcnt lgkmcnt(15)\n"
    "v_perm_b32 v67, v67, v67, v153\n"
    "s_waitcnt lgkmcnt(15)\n"
    "v_perm_b32 v68, v68, v68, v154\n"
    "s_waitcnt lgkmcnt(15)\n"
    "v_perm_b32 v69, v69, v69, v155\n"
    "s_waitcnt lgkmcnt(14)\n"
    "v_perm_b32 v70, v73, v73, v166\n"
    "v_perm_b32 v71, v73, v73, v167\n"
    "v_perm_b32 v72, v73, v73, v168\n"
    "v_perm_b32 v73, v73, v73, v169\n"
    "s_waitcnt lgkmcnt(13)\n"
    "v_perm_b32 v74, v77, v77, v166\n"
    "v_perm_b32 v75, v77, v77, v167\n"
    "v_perm_b32 v76, v77, v77, v168\n"
    "v_perm_b32 v77, v77, v77, v169\n"
    "s_waitcnt lgkmcnt(12)\n"
    "v_perm_b32 v78, v81, v81, v166\n"
    "v_perm_b32 v79, v81, v81, v167\n"
    "v_perm_b32 v80, v81, v81, v168\n"
    "v_perm_b32 v81, v81, v81, v169\n"
    "buffer_load_dwordx4 a[28:31], v233, s[52:55], s17 offen offset:3072 sc1\n"
    "s_waitcnt lgkmcnt(11)\n"
    "v_perm_b32 v82, v85, v85, v166\n"
    "v_perm_b32 v83, v85, v85, v167\n"
    "v_perm_b32 v84, v85, v85, v168\n"
    "v_perm_b32 v85, v85, v85, v169\n"
    "s_waitcnt lgkmcnt(10)\n"
    "v_perm_b32 v86, v89, v89, v166\n"
    "v_perm_b32 v87, v89, v89, v167\n"
    "v_perm_b32 v88, v89, v89, v168\n"
    "v_perm_b32 v89, v89, v89, v169\n"
    "s_waitcnt lgkmcnt(9)\n"
    "v_perm_b32 v90, v93, v93, v166\n"
    "v_perm_b32 v91, v93, v93, v167\n"
    "v_perm_b32 v92, v93, v93, v168\n"
    "v_perm_b32 v93, v93, v93, v169\n"
    "buffer_load_dwordx4 a[32:35], v241, s[52:55], s16 offen offset:0 sc1\n"
    "s_waitcnt lgkmcnt(8)\n"
    "v_perm_b32 v94, v97, v97, v166\n"
    "v_perm_b32 v95, v97, v97, v167\n"
    "v_perm_b32 v96, v97, v97, v168\n"
    "v_perm_b32 v97, v97, v97, v169\n"
    "s_waitcnt lgkmcnt(7)\n"
    "v_perm_b32 v98, v101, v101, v166\n"
    "v_perm_b32 v99, v101, v101, v167\n"
    "v_perm_b32 v100, v101, v101, v168\n"
    "v_perm_b32 v101, v101, v101, v169\n"
    "s_waitcnt lgkmcnt(6)\n"
    "v_perm_b32 v102, v105, v105, v166\n"
    "v_perm_b32 v103, v105, v105, v167\n"
    "v_perm_b32 v104, v105, v105, v168\n"
    "v_perm_b32 v105, v105, v105, v169\n"
    "buffer_load_dwordx4 a[36:39], v241, s[52:55], s17 offen offset:0 sc1\n"
    "s_waitcnt lgkmcnt(5)\n"
    "v_perm_b32 v106, v109, v109, v166\n"
    "v_perm_b32 v107, v109, v109, v167\n"
    "v_perm_b32 v108, v109, v109, v168\n"
    "v_perm_b32 v109, v109, v109, v169\n"
    "s_waitcnt lgkmcnt(4)\n"
    "v_perm_b32 v110, v113, v113, v166\n"
    "v_perm_b32 v111, v113, v113, v167\n"
    "v_perm_b32 v112, v113, v113, v168\n"
    "v_perm_b32 v113, v113, v113, v169\n"
    "s_waitcnt lgkmcnt(3)\n"
    "v_perm_b32 v114, v117, v117, v166\n"
    "v_perm_b32 v115, v117, v117, v167\n"
    "v_perm_b32 v116, v117, v117, v168\n"
    "v_perm_b32 v117, v117, v117, v169\n"
    "buffer_load_dwordx4 a[40:43], v241, s[52:55], s16 offen offset:1024 sc1\n"
    "s_waitcnt lgkmcnt(2)\n"
    "v_perm_b32 v118, v121, v121, v166\n"
    "v_perm_b32 v119, v121, v121, v167\n"
    "v_perm_b32 v120, v121, v121, v168\n"
    "v_perm_b32 v121, v121, v121, v169\n"
    "s_waitcnt lgkmcnt(1)\n"
    "v_perm_b32 v122, v125, v125, v166\n"
    "v_perm_b32 v123, v125, v125, v167\n"
    "v_perm_b32 v124, v125, v125, v168\n"
    "v_perm_b32 v125, v125, v125, v169\n"
    "s_waitcnt lgkmcnt(0)\n"
    "v_perm_b32 v126, v129, v129, v166\n"
    "v_perm_b32 v127, v129, v129, v167\n"
    "v_perm_b32 v128, v129, v129, v168\n"
    "v_perm_b32 v129, v129, v129, v169\n"
    "buffer_load_dwordx4 a[44:47], v241, s[52:55], s17 offen offset:1024 sc1\n"
    "s_mul_i32 s64, s69, 4608\n"
    "s_lshl_b32 s65, s70, 6\n"
    "s_sub_u32 s66, s64, s65\n"
    "s_add_u32 s66, s66, 118176\n"
    "v_lshlrev_b32_e64 v224, 4, v234\n"
    "v_lshl_add_u32 v224, v235, 5, v224\n"
    "v_add_u32_e64 v243, s66, v224\n"
    "s_lshl_b32 s65, s70, 10\n"
    "s_add_u32 s64, s64, s65\n"
    "s_add_u32 s64, s64, 118592\n"
    "v_add_u32 v246, s64, v233\n"
    "s_lshl_b32 s64, s70, 11\n"
    "s_add_u32 s64, s64, 127296\n"
    "v_lshl_add_u32 v242, v222, 5, s64\n"
    "s_mul_i32 s64, s57, 9216\n"
    "v_lshl_add_u32 v224, v238, 7, v233\n"
    "v_add_u32_e64 v244, s64, v224\n"
    "s_mul_i32 s64, s69, 18432\n"
    "s_mul_i32 s65, s70, 2304\n"
    "s_add_u32 s64, s64, s65\n"
    "buffer_load_dwordx4 a[48:51], v241, s[52:55], s16 offen offset:2048 sc1\n"
    "v_and_b32 v224, 1, v222\n"
    "v_mul_u32_u24 v224, 640, v224\n"
    "v_bfe_u32 v225, v222, 1, 4\n"
    "v_lshl_add_u32 v224, v225, 4, v224\n"
    "v_mul_u32_u24 v225, 1152, v238\n"
    "v_add3_u32 v245, v224, v225, s64\n"
    "v_lshrrev_b32 v224, 2, v222\n"
    "v_and_b32 v225, 3, v222\n"
    "v_mul_u32_u24 v224, 28672, v224\n"
    "v_lshl_add_u32 v247, v225, 4, v224\n"
    "s_mul_i32 s14, s70, 458752\n"
    "s_lshl_b32 s64, s71, 6\n"
    "s_add_u32 s14, s14, s64\n"
    "s_add_u32 s14, s14, 8613888\n"
    "buffer_load_dwordx4 a[52:55], v241, s[52:55], s17 offen offset:2048 sc1\n"
    "v_lshl_add_u32 v224, v238, 3, v237\n"
    "v_lshl_add_u32 v224, v224, 6, v236\n"
    "v_add_u32 v224, 36864, v224\n"
    "v_add_u32 v248, s20, v224\n"
    "v_add_u32 v249, s21, v224\n"
    "v_add_u32 v250, s22, v224\n"
    "v_add_u32 v251, s23, v224\n"
    "s_mul_i32 s64, s57, 7168\n"
    "s_add_u32 s64, s64, 36864\n"
    "v_add_u32 v223, s64, v233\n"
    "s_add_u32 s16, s18, s21\n"
    "s_add_u32 s17, s16, 28672\n"
    "buffer_load_dwordx4 a[56:59], v233, s[52:55], s16 offen offset:0 sc1\n"
    "buffer_load_dwordx4 a[60:63], v233, s[52:55], s17 offen offset:0 sc1\n"
    "buffer_load_dwordx4 a[64:67], v233, s[52:55], s16 offen offset:1024 sc1\n"
    "buffer_load_dwordx4 a[68:71], v233, s[52:55], s17 offen offset:1024 sc1\n"
    "ds_read_b128 v[162:165], v243 offset:0\n"
    "ds_read_b128 v[166:169], v243 offset:128\n"
    "ds_read_b128 v[170:173], v243 offset:256\n"
    "ds_read_b128 v[174:177], v243 offset:384\n"
    "s_cmp_eq_u32 s70, 1\n"
    "s_cbranch_scc1 Lconv1_%=\n"
    "s_waitcnt lgkmcnt(3)\n"
    "v_mfma_f32_16x16x32_f16 v[130:133], v[14:17], v[162:165], 0\n"
    "ds_read_b128 v[178:181], v243 offset:512\n"
    "ds_read_b128 v[218:221], v242 offset:8192\n"
    "v_mfma_f32_16x16x32_f16 v[134:137], v[30:33], v[162:165], 0\n"
    "ds_read_b128 v[182:185], v243 offset:640\n"
    "ds_read_b128 v[214:217], v242 offset:0\n"
    "s_waitcnt lgkmcnt(6)\n"
    "v_mfma_f32_16x16x32_f16 v[130:133], v[10:13], v[166:169], v[130:133]\n"
    "ds_read_b128 v[186:189], v243 offset:768\n"
    "ds_read_b128 v[210:213], v246 offset:0\n"
    "buffer_load_dwordx4 a[72:75], v233, s[52:55], s16 offen offset:2048 sc1\n"
    "v_mfma_f32_16x16x32_f16 v[134:137], v[26:29], v[166:169], v[134:137]\n"
    "ds_read_b128 v[190:193], v243 offset:896\n"
    "ds_read_b128 v[252:255], v242 offset:8208\n"
    "s_waitcnt lgkmcnt(9)\n"
    "v_mfma_f32_16x16x32_f16 v[130:133], v[6:9], v[170:173], v[130:133]\n"
    "ds_read_b128 v[162:165], v243 offset:0\n"
    "ds_read_b128 v[224:227], v242 offset:16\n"
    "v_mfma_f32_16x16x32_f16 v[134:137], v[22:25], v[170:173], v[134:137]\n"
    "s_waitcnt lgkmcnt(10)\n"
    "v_mfma_f32_16x16x32_f16 v[130:133], v[2:5], v[174:177], v[130:133]\n"
    "ds_read_b128 v[166:169], v243 offset:128\n"
    "buffer_load_dwordx4 a[76:79], v233, s[52:55], s17 offen offset:2048 sc1\n"
    "v_mfma_f32_16x16x32_f16 v[134:137], v[18:21], v[174:177], v[134:137]\n"
    "s_waitcnt lgkmcnt(9)\n"
    "v_add_f32 v218, s82, v218\n"
    "v_add_f32 v219, s82, v219\n"
    "v_add_f32 v220, s82, v220\n"
    "v_mfma_f32_16x16x32_f16 v[134:137], v[14:17], v[178:181], v[134:137]\n"
    "ds_read_b128 v[170:173], v243 offset:256\n"
    "v_add_f32 v221, s82, v221\n"
    "s_waitcnt lgkmcnt(9)\n"
    "v_mfma_f32_16x16x32_f16 v[134:137], v[10:13], v[182:185], v[134:137]\n"
    "s_waitcnt lgkmcnt(6)\n"
    "v_fma_mix_f32 v214, v210, v218, v214 op_sel:[0,0,0] op_sel_hi:[1,0,0]\n"
    "v_mfma_f32_16x16x32_f16 v[134:137], v[6:9], v[186:189], v[134:137]\n"
    "ds_read_b128 v[174:177], v243 offset:384\n"
    "buffer_load_dwordx4 a[80:83], v233, s[52:55], s16 offen offset:3072 sc1\n"
    "v_fma_mix_f32 v215, v210, v219, v215 op_sel:[1,0,0] op_sel_hi:[1,0,0]\n"
    "s_waitcnt lgkmcnt(6)\n"
    "v_mfma_f32_16x16x32_f16 v[134:137], v[2:5], v[190:193], v[134:137]\n"
    "v_fma_mix_f32 v216, v211, v220, v216 op_sel:[0,0,0] op_sel_hi:[1,0,0]\n"
    "s_waitcnt lgkmcnt(4)\n"
    "v_mfma_f32_16x16x32_f16 v[138:141], v[46:49], v[162:165], 0\n"
    "ds_read_b128 v[178:181], v243 offset:512\n"
    "v_fma_mix_f32 v217, v211, v221, v217 op_sel:[1,0,0] op_sel_hi:[1,0,0]\n"
    "v_mfma_f32_16x16x32_f16 v[142:145], v[62:65], v[162:165], 0\n"
    "v_add_f32 v252, s82, v252\n"
    "v_add_f32 v253, s82, v253\n"
    "v_add_f32_e64 v254, s82, v254\n"
    "s_waitcnt lgkmcnt(3)\n"
    "v_mfma_f32_16x16x32_f16 v[138:141], v[42:45], v[166:169], v[138:141]\n"
    "ds_read_b128 v[182:185], v243 offset:640\n"
    "buffer_load_dwordx4 a[84:87], v233, s[52:55], s17 offen offset:3072 sc1\n"
    "v_add_f32_e64 v255, s82, v255\n"
    "v_mfma_f32_16x16x32_f16 v[142:145], v[58:61], v[166:169], v[142:145]\n"
    "v_fma_mix_f32 v224, v212, v252, v224 op_sel:[0,0,0] op_sel_hi:[1,0,0]\n"
    "s_waitcnt lgkmcnt(3)\n"
    "v_mfma_f32_16x16x32_f16 v[138:141], v[38:41], v[170:173], v[138:141]\n"
    "ds_read_b128 v[186:189], v243 offset:768\n"
    "ds_write_b128 v244, v[130:133] offset:0\n"
    "ds_write_b128 v244, v[134:137] offset:1152\n"
    "v_mfma_f32_16x16x32_f16 v[142:145], v[54:57], v[170:173], v[142:145]\n"
    "v_fma_mix_f32 v225, v212, v253, v225 op_sel:[1,0,0] op_sel_hi:[1,0,0]\n"
    "s_waitcnt lgkmcnt(5)\n"
    "v_mfma_f32_16x16x32_f16 v[138:141], v[34:37], v[174:177], v[138:141]\n"
    "ds_read_b128 v[190:193], v243 offset:896\n"
    "buffer_load_dwordx4 a[88:91], v241, s[52:55], s16 offen offset:0 sc1\n"
    "v_fma_mix_f32 v226, v213, v254, v226 op_sel:[0,0,0] op_sel_hi:[1,0,0]\n"
    "v_mfma_f32_16x16x32_f16 v[142:145], v[50:53], v[174:177], v[142:145]\n"
    "s_waitcnt lgkmcnt(1)\n"
    "s_barrier\n"
    "ds_read_b128 v[194:197], v245 offset:0\n"
    "ds_read_b128 v[202:205], v245 offset:9216\n"
    "ds_read_b128 v[198:201], v245 offset:256\n"
    "ds_read_b128 v[206:209], v245 offset:9472\n"
    "v_fma_mix_f32 v227, v213, v255, v227 op_sel:[1,0,0] op_sel_hi:[1,0,0]\n"
    "v_mfma_f32_16x16x32_f16 v[138:141], v[30:33], v[178:181], v[138:141]\n"
    "ds_read_b128 v[162:165], v243 offset:1024\n"
    "v_mfma_f32_16x16x32_f16 v[142:145], v[46:49], v[178:181], v[142:145]\n"
    "v_mfma_f32_16x16x32_f16 v[138:141], v[26:29], v[182:185], v[138:141]\n"
    "ds_read_b128 v[166:169], v243 offset:1152\n"
    "buffer_load_dwordx4 a[92:95], v241, s[52:55], s17 offen offset:0 sc1\n"
    "v_mfma_f32_16x16x32_f16 v[142:145], v[42:45], v[182:185], v[142:145]\n"
    "s_waitcnt lgkmcnt(4)\n"
    "v_add_f32 v194, v194, v202\n"
    "v_add_f32 v195, v195, v203\n"
    "v_add_f32 v196, v196, v204\n"
    "v_add_f32_e64 v197, v197, v205\n"
    "v_mfma_f32_16x16x32_f16 v[138:141], v[22:25], v[186:189], v[138:141]\n"
    "ds_read_b128 v[170:173], v243 offset:1280\n"
    "v_fmac_f32 v214, v194, v239\n"
    "v_fmac_f32 v215, v195, v239\n"
    "v_fmac_f32_e64 v216, v196, v239\n"
    "v_mfma_f32_16x16x32_f16 v[142:145], v[38:41], v[186:189], v[142:145]\n"
    "v_fmac_f32_e64 v217, v197, v239\n"
    "v_cvt_pk_bf16_f32 v202, v214, v215\n"
    "v_cvt_pk_bf16_f32 v203, v216, v217\n"
    "s_waitcnt lgkmcnt(3)\n"
    "v_add_f32 v198, v198, v206\n"
    "v_mfma_f32_16x16x32_f16 v[138:141], v[18:21], v[190:193], v[138:141]\n"
    "ds_read_b128 v[174:177], v243 offset:1408\n"
    "buffer_load_dwordx4 a[96:99], v241, s[52:55], s16 offen offset:1024 sc1\n"
    "v_add_f32 v199, v199, v207\n"
    "v_add_f32 v200, v200, v208\n"
    "v_add_f32_e64 v201, v201, v209\n"
    "v_mfma_f32_16x16x32_f16 v[142:145], v[34:37], v[190:193], v[142:145]\n"
    "v_fmac_f32 v224, v198, v239\n"
    "v_fmac_f32 v225, v199, v239\n"
    "v_fmac_f32 v226, v200, v239\n"
    "v_fmac_f32_e64 v227, v201, v239\n"
    "s_waitcnt lgkmcnt(3)\n"
    "v_mfma_f32_16x16x32_f16 v[138:141], v[14:17], v[162:165], v[138:141]\n"
    "ds_read_b128 v[178:181], v243 offset:1536\n"
    "v_cvt_pk_bf16_f32 v204, v224, v225\n"
    "v_cvt_pk_bf16_f32 v205, v226, v227\n"
    "v_mfma_f32_16x16x32_f16 v[142:145], v[30:33], v[162:165], v[142:145]\n"
    "ds_read_b128 v[182:185], v243 offset:1664\n"
    "buffer_store_dwordx4 v[202:205], v247, s[52:55], s14 offen sc1\n"
    "s_add_u32 s14, s14, 917504\n"
    "s_waitcnt lgkmcnt(4)\n"
    "v_mfma_f32_16x16x32_f16 v[138:141], v[10:13], v[166:169], v[138:141]\n"
    "ds_read_b128 v[186:189], v243 offset:1792\n"
    "buffer_load_dwordx4 a[100:103], v241, s[52:55], s17 offen offset:1024 sc1\n"
    "v_mfma_f32_16x16x32_f16 v[142:145], v[26:29], v[166:169], v[142:145]\n"
    "ds_read_b128 v[190:193], v243 offset:1920\n"
    "s_waitcnt lgkmcnt(5)\n"
    "v_mfma_f32_16x16x32_f16 v[138:141], v[6:9], v[170:173], v[138:141]\n"
    "ds_read_b128 v[162:165], v243 offset:0\n"
    "v_mfma_f32_16x16x32_f16 v[142:145], v[22:25], v[170:173], v[142:145]\n"
    "s_waitcnt lgkmcnt(5)\n"
    "v_mfma_f32_16x16x32_f16 v[138:141], v[2:5], v[174:177], v[138:141]\n"
    "buffer_load_dwordx4 a[104:107], v241, s[52:55], s16 offen offset:2048 sc1\n"
    "v_mfma_f32_16x16x32_f16 v[142:145], v[18:21], v[174:177], v[142:145]\n"
    "s_waitcnt lgkmcnt(4)\n"
    "v_mfma_f32_16x16x32_f16 v[142:145], v[14:17], v[178:181], v[142:145]\n"
    "ds_read_b128 v[166:169], v243 offset:128\n"
    "s_waitcnt lgkmcnt(4)\n"
    "v_mfma_f32_16x16x32_f16 v[142:145], v[10:13], v[182:185], v[142:145]\n"
    "s_waitcnt lgkmcnt(3)\n"
    "v_mfma_f32_16x16x32_f16 v[142:145], v[6:9], v[186:189], v[142:145]\n"
    "buffer_load_dwordx4 a[108:111], v241, s[52:55], s17 offen offset:2048 sc1\n"
    "s_waitcnt lgkmcnt(2)\n"
    "v_mfma_f32_16x16x32_f16 v[142:145], v[2:5], v[190:193], v[142:145]\n"
    "s_waitcnt lgkmcnt(1)\n"
    "v_mfma_f32_16x16x32_f16 v[146:149], v[78:81], v[162:165], 0\n"
    "ds_read_b128 v[170:173], v243 offset:256\n"
    "v_mfma_f32_16x16x32_f16 v[150:153], v[94:97], v[162:165], 0\n"
    "v_mfma_f32_16x16x32_f16 v[154:157], v[110:113], v[162:165], 0\n"
    "s_add_u32 s16, s18, s22\n"
    "buffer_load_dwordx4 a[112:115], v233, s[52:55], s16 offen offset:0 sc1\n"
    "s_add_u32 s17, s16, 28672\n"
    "v_mfma_f32_16x16x32_f16 v[158:161], v[126:129], v[162:165], 0\n"
    "s_waitcnt lgkmcnt(1)\n"
    "v_mfma_f32_16x16x32_f16 v[146:149], v[74:77], v[166:169], v[146:149]\n"
    "ds_read_b128 v[174:177], v243 offset:384\n"
    "ds_write_b128 v244, v[138:141] offset:2304\n"
    "ds_write_b128 v244, v[142:145] offset:3456\n"
    "v_mfma_f32_16x16x32_f16 v[150:153], v[90:93], v[166:169], v[150:153]\n"
    "v_mfma_f32_16x16x32_f16 v[154:157], v[106:109], v[166:169], v[154:157]\n"
    "buffer_load_dwordx4 a[116:119], v233, s[52:55], s17 offen offset:0 sc1\n"
    "v_mfma_f32_16x16x32_f16 v[158:161], v[122:125], v[166:169], v[158:161]\n"
    "s_waitcnt lgkmcnt(0)\n"
    "s_barrier\n"
    "v_mfma_f32_16x16x32_f16 v[146:149], v[70:73], v[170:173], v[146:149]\n"
    "ds_read_b128 v[178:181], v243 offset:512\n"
    "v_mfma_f32_16x16x32_f16 v[150:153], v[86:89], v[170:173], v[150:153]\n"
    "v_mfma_f32_16x16x32_f16 v[154:157], v[102:105], v[170:173], v[154:157]\n"
    "buffer_load_dwordx4 a[120:123], v233, s[52:55], s16 offen offset:1024 sc1\n"
    "v_mfma_f32_16x16x32_f16 v[158:161], v[118:121], v[170:173], v[158:161]\n"
    "v_mfma_f32_16x16x32_f16 v[146:149], v[66:69], v[174:177], v[146:149]\n"
    "ds_read_b128 v[182:185], v243 offset:640\n"
    "v_mfma_f32_16x16x32_f16 v[150:153], v[82:85], v[174:177], v[150:153]\n"
    "v_mfma_f32_16x16x32_f16 v[154:157], v[98:101], v[174:177], v[154:157]\n"
    "buffer_load_dwordx4 a[124:127], v233, s[52:55], s17 offen offset:1024 sc1\n"
    "v_mfma_f32_16x16x32_f16 v[158:161], v[114:117], v[174:177], v[158:161]\n"
    "s_waitcnt lgkmcnt(1)\n"
    "v_mfma_f32_16x16x32_f16 v[146:149], v[62:65], v[178:181], v[146:149]\n"
    "ds_read_b128 v[186:189], v243 offset:768\n"
    "v_mfma_f32_16x16x32_f16 v[150:153], v[78:81], v[178:181], v[150:153]\n"
    "v_mfma_f32_16x16x32_f16 v[154:157], v[94:97], v[178:181], v[154:157]\n"
    "buffer_load_dwordx4 a[128:131], v233, s[52:55], s16 offen offset:2048 sc1\n"
    "v_mfma_f32_16x16x32_f16 v[158:161], v[110:113], v[178:181], v[158:161]\n"
    "s_waitcnt lgkmcnt(1)\n"
    "v_mfma_f32_16x16x32_f16 v[146:149], v[58:61], v[182:185], v[146:149]\n"
    "ds_read_b128 v[190:193], v243 offset:896\n"
    "v_mfma_f32_16x16x32_f16 v[150:153], v[74:77], v[182:185], v[150:153]\n"
    "v_mfma_f32_16x16x32_f16 v[154:157], v[90:93], v[182:185], v[154:157]\n"
    "buffer_load_dwordx4 a[132:135], v233, s[52:55], s17 offen offset:2048 sc1\n"
    "v_mfma_f32_16x16x32_f16 v[158:161], v[106:109], v[182:185], v[158:161]\n"
    "s_waitcnt lgkmcnt(1)\n"
    "v_mfma_f32_16x16x32_f16 v[146:149], v[54:57], v[186:189], v[146:149]\n"
    "ds_read_b128 v[162:165], v243 offset:1024\n"
    "v_mfma_f32_16x16x32_f16 v[150:153], v[70:73], v[186:189], v[150:153]\n"
    "v_mfma_f32_16x16x32_f16 v[154:157], v[86:89], v[186:189], v[154:157]\n"
    "buffer_load_dwordx4 a[136:139], v233, s[52:55], s16 offen offset:3072 sc1\n"
    "v_mfma_f32_16x16x32_f16 v[158:161], v[102:105], v[186:189], v[158:161]\n"
    "s_waitcnt lgkmcnt(1)\n"
    "v_mfma_f32_16x16x32_f16 v[146:149], v[50:53], v[190:193], v[146:149]\n"
    "ds_read_b128 v[166:169], v243 offset:1152\n"
    "v_mfma_f32_16x16x32_f16 v[150:153], v[66:69], v[190:193], v[150:153]\n"
    "v_mfma_f32_16x16x32_f16 v[154:157], v[82:85], v[190:193], v[154:157]\n"
    "buffer_load_dwordx4 a[140:143], v233, s[52:55], s17 offen offset:3072 sc1\n"
    "v_mfma_f32_16x16x32_f16 v[158:161], v[98:101], v[190:193], v[158:161]\n"
    "s_branch Lconvm_%=\n"
    "Lconv1_%=:\n"
    "s_waitcnt lgkmcnt(3)\n"
    "v_mfma_f32_16x16x32_f16 v[130:133], v[14:17], v[162:165], 0\n"
    "ds_read_b128 v[178:181], v243 offset:512\n"
    "v_mfma_f32_16x16x32_f16 v[134:137], v[30:33], v[162:165], 0\n"
    "ds_read_b128 v[182:185], v243 offset:640\n"
    "s_waitcnt lgkmcnt(4)\n"
    "v_mfma_f32_16x16x32_f16 v[130:133], v[10:13], v[166:169], v[130:133]\n"
    "ds_read_b128 v[186:189], v243 offset:768\n"
    "buffer_load_dwordx4 a[72:75], v233, s[52:55], s16 offen offset:2048 sc1\n"
    "v_mfma_f32_16x16x32_f16 v[134:137], v[26:29], v[166:169], v[134:137]\n"
    "ds_read_b128 v[190:193], v243 offset:896\n"
    "s_waitcnt lgkmcnt(5)\n"
    "v_mfma_f32_16x16x32_f16 v[130:133], v[6:9], v[170:173], v[130:133]\n"
    "ds_read_b128 v[162:165], v243 offset:0\n"
    "v_mfma_f32_16x16x32_f16 v[134:137], v[22:25], v[170:173], v[134:137]\n"
    "s_waitcnt lgkmcnt(5)\n"
    "v_mfma_f32_16x16x32_f16 v[130:133], v[2:5], v[174:177], v[130:133]\n"
    "ds_read_b128 v[166:169], v243 offset:128\n"
    "buffer_load_dwordx4 a[76:79], v233, s[52:55], s17 offen offset:2048 sc1\n"
    "v_mfma_f32_16x16x32_f16 v[134:137], v[18:21], v[174:177], v[134:137]\n"
    "s_waitcnt lgkmcnt(5)\n"
    "v_mfma_f32_16x16x32_f16 v[134:137], v[14:17], v[178:181], v[134:137]\n"
    "ds_read_b128 v[170:173], v243 offset:256\n"
    "s_waitcnt lgkmcnt(5)\n"
    "v_mfma_f32_16x16x32_f16 v[134:137], v[10:13], v[182:185], v[134:137]\n"
    "s_waitcnt lgkmcnt(4)\n"
    "v_mfma_f32_16x16x32_f16 v[134:137], v[6:9], v[186:189], v[134:137]\n"
    "ds_read_b128 v[174:177], v243 offset:384\n"
    "buffer_load_dwordx4 a[80:83], v233, s[52:55], s16 offen offset:3072 sc1\n"
    "s_waitcnt lgkmcnt(4)\n"
    "v_mfma_f32_16x16x32_f16 v[134:137], v[2:5], v[190:193], v[134:137]\n"
    "s_waitcnt lgkmcnt(3)\n"
    "v_mfma_f32_16x16x32_f16 v[138:141], v[46:49], v[162:165], 0\n"
    "ds_read_b128 v[178:181], v243 offset:512\n"
    "v_mfma_f32_16x16x32_f16 v[142:145], v[62:65], v[162:165], 0\n"
    "s_waitcnt lgkmcnt(3)\n"
    "v_mfma_f32_16x16x32_f16 v[138:141], v[42:45], v[166:169], v[138:141]\n"
    "ds_read_b128 v[182:185], v243 offset:640\n"
    "buffer_load_dwordx4 a[84:87], v233, s[52:55], s17 offen offset:3072 sc1\n"
    "v_mfma_f32_16x16x32_f16 v[142:145], v[58:61], v[166:169], v[142:145]\n"
    "s_waitcnt lgkmcnt(3)\n"
    "v_mfma_f32_16x16x32_f16 v[138:141], v[38:41], v[170:173], v[138:141]\n"
    "ds_read_b128 v[186:189], v243 offset:768\n"
    "ds_write_b128 v244, v[130:133] offset:0\n"
    "ds_write_b128 v244, v[134:137] offset:1152\n"
    "v_mfma_f32_16x16x32_f16 v[142:145], v[54:57], v[170:173], v[142:145]\n"
    "s_waitcnt lgkmcnt(5)\n"
    "v_mfma_f32_16x16x32_f16 v[138:141], v[34:37], v[174:177], v[138:141]\n"
    "ds_read_b128 v[190:193], v243 offset:896\n"
    "buffer_load_dwordx4 a[88:91], v241, s[52:55], s16 offen offset:0 sc1\n"
    "v_mfma_f32_16x16x32_f16 v[142:145], v[50:53], v[174:177], v[142:145]\n"
    "s_waitcnt lgkmcnt(1)\n"
    "s_barrier\n"
    "v_mfma_f32_16x16x32_f16 v[138:141], v[30:33], v[178:181], v[138:141]\n"
    "ds_read_b128 v[162:165], v243 offset:1024\n"
    "v_mfma_f32_16x16x32_f16 v[142:145], v[46:49], v[178:181], v[142:145]\n"
    "v_mfma_f32_16x16x32_f16 v[138:141], v[26:29], v[182:185], v[138:141]\n"
    "ds_read_b128 v[166:169], v243 offset:1152\n"
    "buffer_load_dwordx4 a[92:95], v241, s[52:55], s17 offen offset:0 sc1\n"
    "v_mfma_f32_16x16x32_f16 v[142:145], v[42:45], v[182:185], v[142:145]\n"
    "v_mfma_f32_16x16x32_f16 v[138:141], v[22:25], v[186:189], v[138:141]\n"
    "ds_read_b128 v[170:173], v243 offset:1280\n"
    "v_mfma_f32_16x16x32_f16 v[142:145], v[38:41], v[186:189], v[142:145]\n"
    "s_waitcnt lgkmcnt(3)\n"
    "v_mfma_f32_16x16x32_f16 v[138:141], v[18:21], v[190:193], v[138:141]\n"
    "ds_read_b128 v[174:177], v243 offset:1408\n"
    "buffer_load_dwordx4 a[96:99], v241, s[52:55], s16 offen offset:1024 sc1\n"
    "v_mfma_f32_16x16x32_f16 v[142:145], v[34:37], v[190:193], v[142:145]\n"
    "ds_read_b128 v[218:221], v242 offset:8192\n"
    "ds_read_b128 v[214:217], v242 offset:0\n"
    "s_waitcnt lgkmcnt(5)\n"
    "v_mfma_f32_16x16x32_f16 v[138:141], v[14:17], v[162:165], v[138:141]\n"
    "ds_read_b128 v[178:181], v243 offset:1536\n"
    "ds_read_b128 v[210:213], v246 offset:0\n"
    "v_mfma_f32_16x16x32_f16 v[142:145], v[30:33], v[162:165], v[142:145]\n"
    "ds_read_b128 v[182:185], v243 offset:1664\n"
    "ds_read_b128 v[252:255], v242 offset:8208\n"
    "s_waitcnt lgkmcnt(8)\n"
    "v_mfma_f32_16x16x32_f16 v[138:141], v[10:13], v[166:169], v[138:141]\n"
    "ds_read_b128 v[186:189], v243 offset:1792\n"
    "ds_read_b128 v[224:227], v242 offset:16\n"
    "buffer_load_dwordx4 a[100:103], v241, s[52:55], s17 offen offset:1024 sc1\n"
    "v_mfma_f32_16x16x32_f16 v[142:145], v[26:29], v[166:169], v[142:145]\n"
    "ds_read_b128 v[190:193], v243 offset:1920\n"
    "s_waitcnt lgkmcnt(10)\n"
    "v_mfma_f32_16x16x32_f16 v[138:141], v[6:9], v[170:173], v[138:141]\n"
    "ds_read_b128 v[162:165], v243 offset:0\n"
    "v_mfma_f32_16x16x32_f16 v[142:145], v[22:25], v[170:173], v[142:145]\n"
    "s_waitcnt lgkmcnt(10)\n"
    "v_mfma_f32_16x16x32_f16 v[138:141], v[2:5], v[174:177], v[138:141]\n"
    "buffer_load_dwordx4 a[104:107], v241, s[52:55], s16 offen offset:2048 sc1\n"
    "v_mfma_f32_16x16x32_f16 v[142:145], v[18:21], v[174:177], v[142:145]\n"
    "s_waitcnt lgkmcnt(9)\n"
    "v_add_f32 v218, s82, v218\n"
    "v_add_f32 v219, s82, v219\n"
    "v_add_f32_e64 v220, s82, v220\n"
    "s_waitcnt lgkmcnt(7)\n"
    "v_mfma_f32_16x16x32_f16 v[142:145], v[14:17], v[178:181], v[142:145]\n"
    "ds_read_b128 v[166:169], v243 offset:128\n"
    "v_add_f32 v221, s82, v221\n"
    "s_waitcnt lgkmcnt(6)\n"
    "v_mfma_f32_16x16x32_f16 v[142:145], v[10:13], v[182:185], v[142:145]\n"
    "v_fma_mix_f32 v214, v210, v218, v214 op_sel:[0,0,0] op_sel_hi:[1,0,0]\n"
    "s_waitcnt lgkmcnt(4)\n"
    "v_mfma_f32_16x16x32_f16 v[142:145], v[6:9], v[186:189], v[142:145]\n"
    "buffer_load_dwordx4 a[108:111], v241, s[52:55], s17 offen offset:2048 sc1\n"
    "v_fma_mix_f32 v215, v210, v219, v215 op_sel:[1,0,0] op_sel_hi:[1,0,0]\n"
    "s_waitcnt lgkmcnt(2)\n"
    "v_mfma_f32_16x16x32_f16 v[142:145], v[2:5], v[190:193], v[142:145]\n"
    "v_fma_mix_f32 v216, v211, v220, v216 op_sel:[0,0,0] op_sel_hi:[1,0,0]\n"
    "s_waitcnt lgkmcnt(1)\n"
    "v_mfma_f32_16x16x32_f16 v[146:149], v[78:81], v[162:165], 0\n"
    "ds_read_b128 v[170:173], v243 offset:256\n"
    "v_fma_mix_f32 v217, v211, v221, v217 op_sel:[1,0,0] op_sel_hi:[1,0,0]\n"
    "v_mfma_f32_16x16x32_f16 v[150:153], v[94:97], v[162:165], 0\n"
    "v_add_f32 v252, s82, v252\n"
    "v_add_f32 v253, s82, v253\n"
    "v_add_f32 v254, s82, v254\n"
    "v_mfma_f32_16x16x32_f16 v[154:157], v[110:113], v[162:165], 0\n"
    "v_add_f32_e64 v255, s82, v255\n"
    "v_fma_mix_f32 v224, v212, v252, v224 op_sel:[0,0,0] op_sel_hi:[1,0,0]\n"
    "s_add_u32 s16, s18, s22\n"
    "buffer_load_dwordx4 a[112:115], v233, s[52:55], s16 offen offset:0 sc1\n"
    "s_add_u32 s17, s16, 28672\n"
    "v_mfma_f32_16x16x32_f16 v[158:161], v[126:129], v[162:165], 0\n"
    "v_fma_mix_f32 v225, v212, v253, v225 op_sel:[1,0,0] op_sel_hi:[1,0,0]\n"
    "s_waitcnt lgkmcnt(1)\n"
    "v_mfma_f32_16x16x32_f16 v[146:149], v[74:77], v[166:169], v[146:149]\n"
    "ds_read_b128 v[174:177], v243 offset:384\n"
    "ds_write_b128 v244, v[138:141] offset:2304\n"
    "ds_write_b128 v244, v[142:145] offset:3456\n"
    "v_mfma_f32_16x16x32_f16 v[150:153], v[90:93], v[166:169], v[150:153]\n"
    "v_fma_mix_f32 v226, v213, v254, v226 op_sel:[0,0,0] op_sel_hi:[1,0,0]\n"
    "v_mfma_f32_16x16x32_f16 v[154:157], v[106:109], v[166:169], v[154:157]\n"
    "buffer_load_dwordx4 a[116:119], v233, s[52:55], s17 offen offset:0 sc1\n"
    "v_fma_mix_f32 v227, v213, v255, v227 op_sel:[1,0,0] op_sel_hi:[1,0,0]\n"
    "v_mfma_f32_16x16x32_f16 v[158:161], v[122:125], v[166:169], v[158:161]\n"
    "s_waitcnt lgkmcnt(0)\n"
    "s_barrier\n"
    "ds_read_b128 v[194:197], v245 offset:0\n"
    "ds_read_b128 v[202:205], v245 offset:9216\n"
    "ds_read_b128 v[198:201], v245 offset:256\n"
    "ds_read_b128 v[206:209], v245 offset:9472\n"
    "v_mfma_f32_16x16x32_f16 v[146:149], v[70:73], v[170:173], v[146:149]\n"
    "ds_read_b128 v[178:181], v243 offset:512\n"
    "v_mfma_f32_16x16x32_f16 v[150:153], v[86:89], v[170:173], v[150:153]\n"
    "v_mfma_f32_16x16x32_f16 v[154:157], v[102:105], v[170:173], v[154:157]\n"
    "buffer_load_dwordx4 a[120:123], v233, s[52:55], s16 offen offset:1024 sc1\n"
    "v_mfma_f32_16x16x32_f16 v[158:161], v[118:121], v[170:173], v[158:161]\n"
    "s_waitcnt lgkmcnt(3)\n"
    "v_add_f32 v194, v194, v202\n"
    "v_add_f32 v195, v195, v203\n"
    "v_add_f32 v196, v196, v204\n"
    "v_add_f32_e64 v197, v197, v205\n"
    "v_mfma_f32_16x16x32_f16 v[146:149], v[66:69], v[174:177], v[146:149]\n"
    "ds_read_b128 v[182:185], v243 offset:640\n"
    "v_fmac_f32 v214, v194, v239\n"
    "v_fmac_f32 v215, v195, v239\n"
    "v_fmac_f32_e64 v216, v196, v239\n"
    "v_mfma_f32_16x16x32_f16 v[150:153], v[82:85], v[174:177], v[150:153]\n"
    "v_fmac_f32_e64 v217, v197, v239\n"
    "v_cvt_pk_bf16_f32 v202, v214, v215\n"
    "v_cvt_pk_bf16_f32 v203, v216, v217\n"
    "s_waitcnt lgkmcnt(2)\n"
    "v_add_f32 v198, v198, v206\n"
    "v_mfma_f32_16x16x32_f16 v[154:157], v[98:101], v[174:177], v[154:157]\n"
    "buffer_load_dwordx4 a[124:127], v233, s[52:55], s17 offen offset:1024 sc1\n"
    "v_add_f32 v199, v199, v207\n"
    "v_add_f32 v200, v200, v208\n"
    "v_add_f32 v201, v201, v209\n"
    "v_fmac_f32 v224, v198, v239\n"
    "v_mfma_f32_16x16x32_f16 v[158:161], v[114:117], v[174:177], v[158:161]\n"
    "v_fmac_f32 v225, v199, v239\n"
    "v_fmac_f32 v226, v200, v239\n"
    "v_fmac_f32_e64 v227, v201, v239\n"
    "v_cvt_pk_bf16_f32 v204, v224, v225\n"
    "s_waitcnt lgkmcnt(1)\n"
    "v_mfma_f32_16x16x32_f16 v[146:149], v[62:65], v[178:181], v[146:149]\n"
    "ds_read_b128 v[186:189], v243 offset:768\n"
    "v_cvt_pk_bf16_f32 v205, v226, v227\n"
    "v_mfma_f32_16x16x32_f16 v[150:153], v[78:81], v[178:181], v[150:153]\n"
    "buffer_store_dwordx4 v[202:205], v247, s[52:55], s14 offen sc1\n"
    "s_add_u32 s14, s14, 917504\n"
    "v_mfma_f32_16x16x32_f16 v[154:157], v[94:97], v[178:181], v[154:157]\n"
    "buffer_load_dwordx4 a[128:131], v233, s[52:55], s16 offen offset:2048 sc1\n"
    "v_mfma_f32_16x16x32_f16 v[158:161], v[110:113], v[178:181], v[158:161]\n"
    "s_waitcnt lgkmcnt(1)\n"
    "v_mfma_f32_16x16x32_f16 v[146:149], v[58:61], v[182:185], v[146:149]\n"
    "ds_read_b128 v[190:193], v243 offset:896\n"
    "v_mfma_f32_16x16x32_f16 v[150:153], v[74:77], v[182:185], v[150:153]\n"
    "v_mfma_f32_16x16x32_f16 v[154:157], v[90:93], v[182:185], v[154:157]\n"
    "buffer_load_dwordx4 a[132:135], v233, s[52:55], s17 offen offset:2048 sc1\n"
    "v_mfma_f32_16x16x32_f16 v[158:161], v[106:109], v[182:185], v[158:161]\n"
    "s_waitcnt lgkmcnt(1)\n"
    "v_mfma_f32_16x16x32_f16 v[146:149], v[54:57], v[186:189], v[146:149]\n"
    "ds_read_b128 v[162:165], v243 offset:1024\n"
    "v_mfma_f32_16x16x32_f16 v[150:153], v[70:73], v[186:189], v[150:153]\n"
    "v_mfma_f32_16x16x32_f16 v[154:157], v[86:89], v[186:189], v[154:157]\n"
    "buffer_load_dwordx4 a[136:139], v233, s[52:55], s16 offen offset:3072 sc1\n"
    "v_mfma_f32_16x16x32_f16 v[158:161], v[102:105], v[186:189], v[158:161]\n"
    "s_waitcnt lgkmcnt(1)\n"
    "v_mfma_f32_16x16x32_f16 v[146:149], v[50:53], v[190:193], v[146:149]\n"
    "ds_read_b128 v[166:169], v243 offset:1152\n"
    "v_mfma_f32_16x16x32_f16 v[150:153], v[66:69], v[190:193], v[150:153]\n"
    "v_mfma_f32_16x16x32_f16 v[154:157], v[82:85], v[190:193], v[154:157]\n"
    "buffer_load_dwordx4 a[140:143], v233, s[52:55], s17 offen offset:3072 sc1\n"
    "v_mfma_f32_16x16x32_f16 v[158:161], v[98:101], v[190:193], v[158:161]\n"
    "Lconvm_%=:\n"
    "s_waitcnt lgkmcnt(1)\n"
    "v_mfma_f32_16x16x32_f16 v[146:149], v[46:49], v[162:165], v[146:149]\n"
    "ds_read_b128 v[170:173], v243 offset:1280\n"
    "v_mfma_f32_16x16x32_f16 v[150:153], v[62:65], v[162:165], v[150:153]\n"
    "v_mfma_f32_16x16x32_f16 v[154:157], v[78:81], v[162:165], v[154:157]\n"
    "buffer_load_dwordx4 a[144:147], v241, s[52:55], s16 offen offset:0 sc1\n"
    "v_mfma_f32_16x16x32_f16 v[158:161], v[94:97], v[162:165], v[158:161]\n"
    "s_waitcnt lgkmcnt(1)\n"
    "v_mfma_f32_16x16x32_f16 v[146:149], v[42:45], v[166:169], v[146:149]\n"
    "ds_read_b128 v[174:177], v243 offset:1408\n"
    "v_mfma_f32_16x16x32_f16 v[150:153], v[58:61], v[166:169], v[150:153]\n"
    "v_mfma_f32_16x16x32_f16 v[154:157], v[74:77], v[166:169], v[154:157]\n"
    "buffer_load_dwordx4 a[148:151], v241, s[52:55], s17 offen offset:0 sc1\n"
    "v_mfma_f32_16x16x32_f16 v[158:161], v[90:93], v[166:169], v[158:161]\n"
    "s_waitcnt lgkmcnt(1)\n"
    "v_mfma_f32_16x16x32_f16 v[146:149], v[38:41], v[170:173], v[146:149]\n"
    "ds_read_b128 v[178:181], v243 offset:1536\n"
    "v_mfma_f32_16x16x32_f16 v[150:153], v[54:57], v[170:173], v[150:153]\n"
    "v_mfma_f32_16x16x32_f16 v[154:157], v[70:73], v[170:173], v[154:157]\n"
    "buffer_load_dwordx4 a[152:155], v241, s[52:55], s16 offen offset:1024 sc1\n"
    "v_mfma_f32_16x16x32_f16 v[158:161], v[86:89], v[170:173], v[158:161]\n"
    "s_waitcnt lgkmcnt(1)\n"
    "v_mfma_f32_16x16x32_f16 v[146:149], v[34:37], v[174:177], v[146:149]\n"
    "ds_read_b128 v[182:185], v243 offset:1664\n"
    "v_mfma_f32_16x16x32_f16 v[150:153], v[50:53], v[174:177], v[150:153]\n"
    "v_mfma_f32_16x16x32_f16 v[154:157], v[66:69], v[174:177], v[154:157]\n"
    "buffer_load_dwordx4 a[156:159], v241, s[52:55], s17 offen offset:1024 sc1\n"
    "v_mfma_f32_16x16x32_f16 v[158:161], v[82:85], v[174:177], v[158:161]\n"
    "s_waitcnt lgkmcnt(1)\n"
    "v_mfma_f32_16x16x32_f16 v[146:149], v[30:33], v[178:181], v[146:149]\n"
    "ds_read_b128 v[186:189], v243 offset:1792\n"
    "v_mfma_f32_16x16x32_f16 v[150:153], v[46:49], v[178:181], v[150:153]\n"
    "v_mfma_f32_16x16x32_f16 v[154:157], v[62:65], v[178:181], v[154:157]\n"
    "buffer_load_dwordx4 a[160:163], v241, s[52:55], s16 offen offset:2048 sc1\n"
    "v_mfma_f32_16x16x32_f16 v[158:161], v[78:81], v[178:181], v[158:161]\n"
    "s_waitcnt lgkmcnt(1)\n"
    "v_mfma_f32_16x16x32_f16 v[146:149], v[26:29], v[182:185], v[146:149]\n"
    "ds_read_b128 v[190:193], v243 offset:1920\n"
    "v_mfma_f32_16x16x32_f16 v[150:153], v[42:45], v[182:185], v[150:153]\n"
    "v_mfma_f32_16x16x32_f16 v[154:157], v[58:61], v[182:185], v[154:157]\n"
    "buffer_load_dwordx4 a[164:167], v241, s[52:55], s17 offen offset:2048 sc1\n"
    "v_mfma_f32_16x16x32_f16 v[158:161], v[74:77], v[182:185], v[158:161]\n"
    "s_waitcnt lgkmcnt(1)\n"
    "v_mfma_f32_16x16x32_f16 v[146:149], v[22:25], v[186:189], v[146:149]\n"
    "ds_read_b128 v[162:165], v243 offset:2048\n"
    "v_mfma_f32_16x16x32_f16 v[150:153], v[38:41], v[186:189], v[150:153]\n"
    "v_mfma_f32_16x16x32_f16 v[154:157], v[54:57], v[186:189], v[154:157]\n"
    "s_add_u32 s16, s18, s23\n"
    "buffer_load_dwordx4 a[168:171], v233, s[52:55], s16 offen offset:0 sc1\n"
    "s_add_u32 s17, s16, 28672\n"
    "v_mfma_f32_16x16x32_f16 v[158:161], v[70:73], v[186:189], v[158:161]\n"
    "s_waitcnt lgkmcnt(1)\n"
    "v_mfma_f32_16x16x32_f16 v[146:149], v[18:21], v[190:193], v[146:149]\n"
    "ds_read_b128 v[166:169], v243 offset:2176\n"
    "v_mfma_f32_16x16x32_f16 v[150:153], v[34:37], v[190:193], v[150:153]\n"
    "v_mfma_f32_16x16x32_f16 v[154:157], v[50:53], v[190:193], v[154:157]\n"
    "buffer_load_dwordx4 a[172:175], v233, s[52:55], s17 offen offset:0 sc1\n"
    "v_mfma_f32_16x16x32_f16 v[158:161], v[66:69], v[190:193], v[158:161]\n"
    "s_waitcnt lgkmcnt(1)\n"
    "v_mfma_f32_16x16x32_f16 v[146:149], v[14:17], v[162:165], v[146:149]\n"
    "ds_read_b128 v[170:173], v243 offset:2304\n"
    "v_mfma_f32_16x16x32_f16 v[150:153], v[30:33], v[162:165], v[150:153]\n"
    "v_mfma_f32_16x16x32_f16 v[154:157], v[46:49], v[162:165], v[154:157]\n"
    "buffer_load_dwordx4 a[176:179], v233, s[52:55], s16 offen offset:1024 sc1\n"
    "v_mfma_f32_16x16x32_f16 v[158:161], v[62:65], v[162:165], v[158:161]\n"
    "s_waitcnt lgkmcnt(1)\n"
    "v_mfma_f32_16x16x32_f16 v[146:149], v[10:13], v[166:169], v[146:149]\n"
    "ds_read_b128 v[174:177], v243 offset:2432\n"
    "v_mfma_f32_16x16x32_f16 v[150:153], v[26:29], v[166:169], v[150:153]\n"
    "v_mfma_f32_16x16x32_f16 v[154:157], v[42:45], v[166:169], v[154:157]\n"
    "buffer_load_dwordx4 a[180:183], v233, s[52:55], s17 offen offset:1024 sc1\n"
    "v_mfma_f32_16x16x32_f16 v[158:161], v[58:61], v[166:169], v[158:161]\n"
    "s_mul_i32 s15, s9, 28672\n"
    "s_mul_i32 s64, s57, 7168\n"
    "s_waitcnt lgkmcnt(1)\n"
    "v_mfma_f32_16x16x32_f16 v[146:149], v[6:9], v[170:173], v[146:149]\n"
    "ds_read_b128 v[178:181], v243 offset:2560\n"
    "s_add_u32 s15, s15, s64\n"
    "v_mfma_f32_16x16x32_f16 v[150:153], v[22:25], v[170:173], v[150:153]\n"
    "s_add_u32 s15, s15, 8613888\n"
    "v_mfma_f32_16x16x32_f16 v[154:157], v[38:41], v[170:173], v[154:157]\n"
    "v_mfma_f32_16x16x32_f16 v[158:161], v[54:57], v[170:173], v[158:161]\n"
    "ds_read_b128 v[182:185], v243 offset:2688\n"
    "s_waitcnt lgkmcnt(2)\n"
    "v_mfma_f32_16x16x32_f16 v[146:149], v[2:5], v[174:177], v[146:149]\n"
    "buffer_load_dwordx4 v[102:105], v233, s[52:55], s15 offen offset:0 sc1\n"
    "v_mfma_f32_16x16x32_f16 v[150:153], v[18:21], v[174:177], v[150:153]\n"
    "v_mfma_f32_16x16x32_f16 v[154:157], v[34:37], v[174:177], v[154:157]\n"
    "ds_read_b128 v[186:189], v243 offset:2816\n"
    "v_mfma_f32_16x16x32_f16 v[158:161], v[50:53], v[174:177], v[158:161]\n"
    "s_waitcnt lgkmcnt(2)\n"
    "v_mfma_f32_16x16x32_f16 v[150:153], v[14:17], v[178:181], v[150:153]\n"
    "buffer_load_dwordx4 v[106:109], v233, s[52:55], s15 offen offset:1024 sc1\n"
    "v_mfma_f32_16x16x32_f16 v[154:157], v[30:33], v[178:181], v[154:157]\n"
    "ds_read_b128 v[190:193], v243 offset:2944\n"
    "v_mfma_f32_16x16x32_f16 v[158:161], v[46:49], v[178:181], v[158:161]\n"
    "s_waitcnt lgkmcnt(2)\n"
    "v_mfma_f32_16x16x32_f16 v[150:153], v[10:13], v[182:185], v[150:153]\n"
    "v_mfma_f32_16x16x32_f16 v[154:157], v[26:29], v[182:185], v[154:157]\n"
    "ds_read_b128 v[162:165], v243 offset:3072\n"
    "buffer_load_dwordx4 v[110:113], v233, s[52:55], s15 offen offset:2048 sc1\n"
    "v_mfma_f32_16x16x32_f16 v[158:161], v[42:45], v[182:185], v[158:161]\n"
    "s_waitcnt lgkmcnt(2)\n"
    "v_mfma_f32_16x16x32_f16 v[150:153], v[6:9], v[186:189], v[150:153]\n"
    "ds_read_b128 v[166:169], v243 offset:3200\n"
    "v_mfma_f32_16x16x32_f16 v[154:157], v[22:25], v[186:189], v[154:157]\n"
    "v_mfma_f32_16x16x32_f16 v[158:161], v[38:41], v[186:189], v[158:161]\n"
    "ds_read_b128 v[170:173], v243 offset:3328\n"
    "buffer_load_dwordx4 v[114:117], v233, s[52:55], s15 offen offset:3072 sc1\n"
    "s_waitcnt lgkmcnt(3)\n"
    "v_mfma_f32_16x16x32_f16 v[150:153], v[2:5], v[190:193], v[150:153]\n"
    "v_mfma_f32_16x16x32_f16 v[154:157], v[18:21], v[190:193], v[154:157]\n"
    "ds_read_b128 v[174:177], v243 offset:3456\n"
    "v_mfma_f32_16x16x32_f16 v[158:161], v[34:37], v[190:193], v[158:161]\n"
    "s_waitcnt lgkmcnt(3)\n"
    "v_mfma_f32_16x16x32_f16 v[154:157], v[14:17], v[162:165], v[154:157]\n"
    "ds_read_b128 v[178:181], v243 offset:3584\n"
    "buffer_load_dwordx4 v[118:121], v241, s[52:55], s15 offen offset:0 sc1\n"
    "v_mfma_f32_16x16x32_f16 v[158:161], v[30:33], v[162:165], v[158:161]\n"
    "ds_read_b128 v[182:185], v243 offset:3712\n"
    "s_waitcnt lgkmcnt(4)\n"
    "v_mfma_f32_16x16x32_f16 v[154:157], v[10:13], v[166:169], v[154:157]\n"
    "ds_read_b128 v[186:189], v243 offset:3840\n"
    "v_mfma_f32_16x16x32_f16 v[158:161], v[26:29], v[166:169], v[158:161]\n"
    "ds_read_b128 v[190:193], v243 offset:3968\n"
    "s_waitcnt lgkmcnt(5)\n"
    "v_mfma_f32_16x16x32_f16 v[154:157], v[6:9], v[170:173], v[154:157]\n"
    "buffer_load_dwordx4 v[122:125], v241, s[52:55], s15 offen offset:1024 sc1\n"
    "v_mfma_f32_16x16x32_f16 v[158:161], v[22:25], v[170:173], v[158:161]\n"
    "s_waitcnt lgkmcnt(4)\n"
    "v_mfma_f32_16x16x32_f16 v[154:157], v[2:5], v[174:177], v[154:157]\n"
    "v_mfma_f32_16x16x32_f16 v[158:161], v[18:21], v[174:177], v[158:161]\n"
    "s_waitcnt lgkmcnt(3)\n"
    "v_mfma_f32_16x16x32_f16 v[158:161], v[14:17], v[178:181], v[158:161]\n"
    "buffer_load_dwordx4 v[126:129], v241, s[52:55], s15 offen offset:2048 sc1\n"
    "s_waitcnt lgkmcnt(2)\n"
    "v_mfma_f32_16x16x32_f16 v[158:161], v[10:13], v[182:185], v[158:161]\n"
    "s_waitcnt lgkmcnt(1)\n"
    "v_mfma_f32_16x16x32_f16 v[158:161], v[6:9], v[186:189], v[158:161]\n"
    "s_waitcnt lgkmcnt(0)\n"
    "v_mfma_f32_16x16x32_f16 v[158:161], v[2:5], v[190:193], v[158:161]\n"
    "buffer_load_dwordx4 a[184:187], v233, s[52:55], s16 offen offset:2048 sc1\n"
    "s_waitcnt vmcnt(7)\n"
    "v_max3_u32 v234, v102, v103, v104\n"
    "s_waitcnt vmcnt(6)\n"
    "v_max3_u32 v234, v234, v105, v106\n"
    "v_max3_u32 v234, v234, v107, v108\n"
    "s_waitcnt vmcnt(5)\n"
    "v_max3_u32 v234, v234, v109, v110\n"
    "v_max3_u32 v234, v234, v111, v112\n"
    "s_waitcnt vmcnt(4)\n"
    "v_max3_u32 v234, v234, v113, v114\n"
    "ds_write_b128 v244, v[146:149] offset:4608\n"
    "ds_write_b128 v244, v[150:153] offset:5760\n"
    "ds_write_b128 v244, v[154:157] offset:6912\n"
    "ds_write_b128 v244, v[158:161] offset:8064\n"
    "buffer_load_dwordx4 a[188:191], v233, s[52:55], s17 offen offset:2048 sc1\n"
    "v_max3_u32 v234, v234, v115, v116\n"
    "s_waitcnt vmcnt(4)\n"
    "v_max3_u32 v234, v234, v117, v118\n"
    "v_max3_u32 v234, v234, v119, v120\n"
    "s_waitcnt vmcnt(3)\n"
    "v_max3_u32 v234, v234, v121, v122\n"
    "v_max3_u32 v234, v234, v123, v124\n"
    "s_waitcnt vmcnt(2)\n"
    "v_max3_u32 v234, v234, v125, v126\n"
    "v_max3_u32 v234, v234, v127, v128\n"
    "v_max_u32 v234, v234, v129\n"
    "v_cmp_eq_u32 vcc, -1, v234\n"
    "s_cbranch_vccnz Lslow_tile0_%=\n"
    "Lback_tile0_%=:\n"
    "ds_write_b128 v223, v[102:105] offset:0\n"
    "ds_write_b128 v223, v[106:109] offset:1024\n"
    "ds_write_b128 v223, v[110:113] offset:2048\n"
    "ds_write_b128 v223, v[114:117] offset:3072\n"
    "ds_write_b128 v223, v[118:121] offset:4096\n"
    "ds_write_b128 v223, v[122:125] offset:5120\n"
    "ds_write_b128 v223, v[126:129] offset:6144\n"
    "buffer_load_dwordx4 a[192:195], v233, s[52:55], s16 offen offset:3072 sc1\n"
    "buffer_load_dwordx4 a[196:199], v233, s[52:55], s17 offen offset:3072 sc1\n"
    "s_waitcnt lgkmcnt(0)\n"
    "s_barrier\n"
    "ds_read_b64_tr_b16 v[194:195], v248 offset:0\n"
    "ds_read_b64_tr_b16 v[196:197], v248 offset:256\n"
    "ds_read_b64_tr_b16 v[198:199], v248 offset:1024\n"
    "ds_read_b64_tr_b16 v[200:201], v248 offset:1280\n"
    "ds_read_b64_tr_b16 v[202:203], v248 offset:2048\n"
    "ds_read_b64_tr_b16 v[204:205], v248 offset:2304\n"
    "ds_read_b128 v[190:193], v242 offset:12288\n"
    "ds_read_b128 v[182:185], v242 offset:4096\n"
    "ds_read_b128 v[178:181], v246 offset:2048\n"
    "s_waitcnt lgkmcnt(7)\n"
    "v_mfma_f32_32x32x16_bf16 v[130:145], a[0:3], v[194:197], 0\n"
    "ds_read_b64_tr_b16 v[206:207], v248 offset:3072\n"
    "ds_read_b128 v[224:227], v242 offset:12304\n"
    "ds_read_b128 v[186:189], v242 offset:4112\n"
    "s_lshl2_add_u32 s27, s8, s57\n"
    "s_lshl_b32 s35, s27, 2\n"
    "s_lshl_b32 s27, s27, 8\n"
    "s_add_u32 s27, s27, 8585216\n"
    "s_add_u32 s67, s27, 14336\n"
    "v_and_b32_e64 v218, 31, v222\n"
    "v_mfma_f32_32x32x16_bf16 v[146:161], a[4:7], v[194:197], 0\n"
    "ds_read_b64_tr_b16 v[208:209], v248 offset:3328\n"
    "ds_read_b128 v[162:165], v245 offset:4608\n"
    "ds_read_b128 v[170:173], v245 offset:13824\n"
    "v_lshlrev_b32 v218, 3, v218\n"
    "s_lshl2_add_u32 s26, s10, s57\n"
    "s_mul_i32 s25, s26, 57344\n"
    "s_add_u32 s25, s25, 6979584\n"
    "s_lshl_b32 s26, s26, 7\n"
    "s_add_u32 s26, s26, 4096\n"
    "s_waitcnt lgkmcnt(11)\n"
    "v_mfma_f32_32x32x16_bf16 v[130:145], a[8:11], v[198:201], v[130:145]\n"
    "ds_read_b64_tr_b16 v[194:195], v248 offset:4096\n"
    "ds_read_b128 v[166:169], v245 offset:4864\n"
    "ds_read_b128 v[174:177], v245 offset:14080\n"
    "v_mfma_f32_32x32x16_bf16 v[146:161], a[12:15], v[198:201], v[146:161]\n"
    "ds_read_b64_tr_b16 v[196:197], v248 offset:4352\n"
    "s_add_u32 s15, s15, 458752\n"
    "buffer_load_dwordx4 v[102:105], v233, s[52:55], s15 offen offset:0 sc1\n"
    "s_waitcnt lgkmcnt(13)\n"
    "v_mfma_f32_32x32x16_bf16 v[130:145], a[16:19], v[202:205], v[130:145]\n"
    "ds_read_b64_tr_b16 v[198:199], v248 offset:5120\n"
    "s_waitcnt lgkmcnt(13)\n"
    "v_add_f32 v190, s82, v190\n"
    "v_add_f32 v191, s82, v191\n"
    "v_add_f32 v192, s82, v192\n"
    "v_add_f32 v193, s82, v193\n"
    "v_mfma_f32_32x32x16_bf16 v[146:161], a[20:23], v[202:205], v[146:161]\n"
    "ds_read_b64_tr_b16 v[200:201], v248 offset:5376\n"
    "buffer_load_dwordx4 v[106:109], v233, s[52:55], s15 offen offset:1024 sc1\n"
    "s_waitcnt lgkmcnt(12)\n"
    "v_fma_mix_f32 v182, v178, v190, v182 op_sel:[0,0,0] op_sel_hi:[1,0,0]\n"
    "s_waitcnt lgkmcnt(8)\n"
    "v_mfma_f32_32x32x16_bf16 v[130:145], a[24:27], v[206:209], v[130:145]\n"
    "ds_read_b64_tr_b16 v[202:203], v248 offset:6144\n"
    "v_fma_mix_f32 v183, v178, v191, v183 op_sel:[1,0,0] op_sel_hi:[1,0,0]\n"
    "v_fma_mix_f32 v184, v179, v192, v184 op_sel:[0,0,0] op_sel_hi:[1,0,0]\n"
    "v_mfma_f32_32x32x16_bf16 v[146:161], a[28:31], v[206:209], v[146:161]\n"
    "ds_read_b64_tr_b16 v[204:205], v248 offset:6400\n"
    "buffer_load_dwordx4 v[110:113], v233, s[52:55], s15 offen offset:2048 sc1\n"
    "v_fma_mix_f32 v185, v179, v193, v185 op_sel:[1,0,0] op_sel_hi:[1,0,0]\n"
    "v_add_f32 v224, s82, v224\n"
    "s_waitcnt lgkmcnt(4)\n"
    "v_mfma_f32_32x32x16_bf16 v[130:145], a[32:35], v[194:197], v[130:145]\n"
    "ds_read_b64_tr_b16 v[206:207], v249 offset:0\n"
    "v_add_f32 v225, s82, v225\n"
    "v_add_f32 v226, s82, v226\n"
    "v_add_f32_e64 v227, s82, v227\n"
    "v_fma_mix_f32 v186, v180, v224, v186 op_sel:[0,0,0] op_sel_hi:[1,0,0]\n"
    "v_mfma_f32_32x32x16_bf16 v[146:161], a[36:39], v[194:197], v[146:161]\n"
    "ds_read_b64_tr_b16 v[208:209], v249 offset:256\n"
    "buffer_load_dwordx4 v[114:117], v233, s[52:55], s15 offen offset:3072 sc1\n"
    "v_fma_mix_f32 v187, v180, v225, v187 op_sel:[1,0,0] op_sel_hi:[1,0,0]\n"
    "s_waitcnt lgkmcnt(4)\n"
    "v_mfma_f32_32x32x16_bf16 v[130:145], a[40:43], v[198:201], v[130:145]\n"
    "ds_read_b64_tr_b16 v[194:195], v249 offset:1024\n"
    "v_fma_mix_f32 v188, v181, v226, v188 op_sel:[0,0,0] op_sel_hi:[1,0,0]\n"
    "v_fma_mix_f32 v189, v181, v227, v189 op_sel:[1,0,0] op_sel_hi:[1,0,0]\n"
    "v_add_f32 v162, v162, v170\n"
    "v_mfma_f32_32x32x16_bf16 v[146:161], a[44:47], v[198:201], v[146:161]\n"
    "ds_read_b64_tr_b16 v[196:197], v249 offset:1280\n"
    "buffer_load_dwordx4 v[118:121], v241, s[52:55], s15 offen offset:0 sc1\n"
    "v_add_f32 v163, v163, v171\n"
    "v_add_f32 v164, v164, v172\n"
    "v_add_f32 v165, v165, v173\n"
    "s_waitcnt lgkmcnt(4)\n"
    "v_mfma_f32_32x32x16_bf16 v[130:145], a[48:51], v[202:205], v[130:145]\n"
    "ds_read_b64_tr_b16 v[198:199], v249 offset:2048\n"
    "v_fmac_f32 v182, v162, v239\n"
    "v_fmac_f32 v183, v163, v239\n"
    "v_fmac_f32 v184, v164, v239\n"
    "v_fmac_f32 v185, v165, v239\n"
    "v_cvt_pk_bf16_f32 v170, v182, v183\n"
    "v_mfma_f32_32x32x16_bf16 v[146:161], a[52:55], v[202:205], v[146:161]\n"
    "ds_read_b64_tr_b16 v[200:201], v249 offset:2304\n"
    "buffer_load_dwordx4 v[122:125], v241, s[52:55], s15 offen offset:1024 sc1\n"
    "v_cvt_pk_bf16_f32 v171, v184, v185\n"
    "v_add_f32 v166, v166, v174\n"
    "v_add_f32_e64 v167, v167, v175\n"
    "s_waitcnt lgkmcnt(4)\n"
    "v_mfma_f32_32x32x16_bf16 v[130:145], a[56:59], v[206:209], v[130:145]\n"
    "ds_read_b64_tr_b16 v[202:203], v249 offset:3072\n"
    "v_add_f32 v168, v168, v176\n"
    "v_add_f32 v169, v169, v177\n"
    "v_fmac_f32 v186, v166, v239\n"
    "v_fmac_f32 v187, v167, v239\n"
    "v_fmac_f32_e64 v188, v168, v239\n"
    "v_mfma_f32_32x32x16_bf16 v[146:161], a[60:63], v[206:209], v[146:161]\n"
    "ds_read_b64_tr_b16 v[204:205], v249 offset:3328\n"
    "buffer_load_dwordx4 v[126:129], v241, s[52:55], s15 offen offset:2048 sc1\n"
    "v_fmac_f32_e64 v189, v169, v239\n"
    "v_cvt_pk_bf16_f32 v172, v186, v187\n"
    "v_cvt_pk_bf16_f32 v173, v188, v189\n"
    "s_waitcnt lgkmcnt(4)\n"
    "v_mfma_f32_32x32x16_bf16 v[130:145], a[64:67], v[194:197], v[130:145]\n"
    "ds_read_b64_tr_b16 v[206:207], v249 offset:4096\n"
    "buffer_store_dwordx4 v[170:173], v247, s[52:55], s14 offen sc1\n"
    "v_lshlrev_b32_e64 v1, 1, v218\n"
    "s_lshl_b32 s36, s9, 9\n"
    "s_add_u32 s36, s36, 65536\n"
    "v_mfma_f32_32x32x16_bf16 v[146:161], a[68:71], v[194:197], v[146:161]\n"
    "ds_read_b64_tr_b16 v[208:209], v249 offset:4352\n"
    "buffer_load_dwordx2 v[214:215], v218, s[52:55], s27 offen sc1\n"
    "buffer_load_dwordx2 v[216:217], v218, s[52:55], s67 offen sc1\n"
    "s_lshl_b32 s64, s57, 13\n"
    "s_waitcnt lgkmcnt(4)\n"
    "v_mfma_f32_32x32x16_bf16 v[130:145], a[72:75], v[198:201], v[130:145]\n"
    "ds_read_b64_tr_b16 v[194:195], v249 offset:5120\n"
    "s_mov_b32 m0, s64\n"
    "s_lshl_b32 s64, s9, 8\n"
    "s_add_u32 s35, s35, s64\n"
    "s_add_u32 s35, s35, 12288\n"
    "s_mov_b32 s64, 0\n"
    "v_mfma_f32_32x32x16_bf16 v[146:161], a[76:79], v[198:201], v[146:161]\n"
    "ds_read_b64_tr_b16 v[196:197], v249 offset:5376\n"
    "buffer_load_dword v240, off, s[52:55], s26 sc1\n"
    "s_cmp_ge_u32 s64, 7\n"
    "s_cselect_b32 s65, 7, 0\n"
    "s_sub_u32 s64, s64, s65\n"
    "s_waitcnt lgkmcnt(4)\n"
    "v_mfma_f32_32x32x16_bf16 v[130:145], a[80:83], v[202:205], v[130:145]\n"
    "ds_read_b64_tr_b16 v[198:199], v249 offset:6144\n"
    "s_lshl_b32 s28, s64, 12\n"
    "s_add_u32 s64, s64, 1\n"
    "s_cmp_ge_u32 s64, 7\n"
    "s_cselect_b32 s65, 7, 0\n"
    "s_sub_u32 s64, s64, s65\n"
    "v_mfma_f32_32x32x16_bf16 v[146:161], a[84:87], v[202:205], v[146:161]\n"
    "ds_read_b64_tr_b16 v[200:201], v249 offset:6400\n"
    "buffer_load_dwordx4 a[200:203], v241, s[52:55], s16 offen offset:0 sc1\n"
    "s_lshl_b32 s29, s64, 12\n"
    "s_add_u32 s64, s64, 1\n"
    "s_cmp_ge_u32 s64, 7\n"
    "s_waitcnt lgkmcnt(4)\n"
    "v_mfma_f32_32x32x16_bf16 v[130:145], a[88:91], v[206:209], v[130:145]\n"
    "ds_read_b64_tr_b16 v[202:203], v250 offset:0\n"
    "s_cselect_b32 s65, 7, 0\n"
    "s_sub_u32 s64, s64, s65\n"
    "s_lshl_b32 s30, s64, 12\n"
    "s_add_u32 s64, s64, 1\n"
    "s_cmp_ge_u32 s64, 7\n"
    "v_mfma_f32_32x32x16_bf16 v[146:161], a[92:95], v[206:209], v[146:161]\n"
    "ds_read_b64_tr_b16 v[204:205], v250 offset:256\n"
    "buffer_load_dwordx4 a[204:207], v241, s[52:55], s17 offen offset:0 sc1\n"
    "s_cselect_b32 s65, 7, 0\n"
    "s_sub_u32 s64, s64, s65\n"
    "s_lshl_b32 s31, s64, 12\n"
    "s_waitcnt lgkmcnt(4)\n"
    "v_mfma_f32_32x32x16_bf16 v[130:145], a[96:99], v[194:197], v[130:145]\n"
    "ds_read_b64_tr_b16 v[206:207], v250 offset:1024\n"
    "s_add_u32 s64, s64, 1\n"
    "s_cmp_ge_u32 s64, 7\n"
    "s_cselect_b32 s65, 7, 0\n"
    "s_sub_u32 s64, s64, s65\n"
    "s_lshl_b32 s32, s64, 12\n"
    "v_mfma_f32_32x32x16_bf16 v[146:161], a[100:103], v[194:197], v[146:161]\n"
    "ds_read_b64_tr_b16 v[208:209], v250 offset:1280\n"
    "buffer_load_dwordx4 a[208:211], v241, s[52:55], s16 offen offset:1024 sc1\n"
    "s_add_u32 s64, s64, 1\n"
    "s_cmp_ge_u32 s64, 7\n"
    "s_cselect_b32 s65, 7, 0\n"
    "s_waitcnt lgkmcnt(4)\n"
    "v_mfma_f32_32x32x16_bf16 v[130:145], a[104:107], v[198:201], v[130:145]\n"
    "ds_read_b64_tr_b16 v[194:195], v250 offset:2048\n"
    "s_sub_u32 s64, s64, s65\n"
    "s_lshl_b32 s33, s64, 12\n"
    "s_add_u32 s64, s64, 1\n"
    "s_cmp_ge_u32 s64, 7\n"
    "s_cselect_b32 s65, 7, 0\n"
    "v_mfma_f32_32x32x16_bf16 v[146:161], a[108:111], v[198:201], v[146:161]\n"
    "ds_read_b64_tr_b16 v[196:197], v250 offset:2304\n"
    "buffer_load_dwordx4 a[212:215], v241, s[52:55], s17 offen offset:1024 sc1\n"
    "s_sub_u32 s64, s64, s65\n"
    "s_lshl_b32 s34, s64, 12\n"
    "s_add_u32 s64, s64, 1\n"
    "s_waitcnt lgkmcnt(4)\n"
    "v_mfma_f32_32x32x16_bf16 v[130:145], a[112:115], v[202:205], v[130:145]\n"
    "ds_read_b64_tr_b16 v[198:199], v250 offset:3072\n"
    "s_mul_i32 s19, s9, 112\n"
    "s_lshl3_add_u32 s19, s8, s19\n"
    "s_lshl1_add_u32 s19, s57, s19\n"
    "s_lshl_b32 s19, s19, 10\n"
    "s_add_u32 s19, s19, 8613888\n"
    "v_mfma_f32_32x32x16_bf16 v[146:161], a[116:119], v[202:205], v[146:161]\n"
    "ds_read_b64_tr_b16 v[200:201], v250 offset:3328\n"
    "buffer_load_dwordx4 a[216:219], v241, s[52:55], s16 offen offset:2048 sc1\n"
    "s_waitcnt lgkmcnt(4)\n"
    "v_mfma_f32_32x32x16_bf16 v[130:145], a[120:123], v[206:209], v[130:145]\n"
    "ds_read_b64_tr_b16 v[202:203], v250 offset:4096\n"
    "v_mfma_f32_32x32x16_bf16 v[146:161], a[124:127], v[206:209], v[146:161]\n"
    "ds_read_b64_tr_b16 v[204:205], v250 offset:4352\n"
    "buffer_load_dwordx4 a[220:223], v241, s[52:55], s17 offen offset:2048 sc1\n"
    "s_waitcnt lgkmcnt(4)\n"
    "v_mfma_f32_32x32x16_bf16 v[130:145], a[128:131], v[194:197], v[130:145]\n"
    "ds_read_b64_tr_b16 v[206:207], v250 offset:5120\n"
    "v_mfma_f32_32x32x16_bf16 v[146:161], a[132:135], v[194:197], v[146:161]\n"
    "ds_read_b64_tr_b16 v[208:209], v250 offset:5376\n"
    "s_waitcnt vmcnt(6)\n"
    "v_cmp_ne_u32_e64 vcc, s60, v240\n"
    "s_cbranch_vccnz Lslow_f2_%=\n"
    "Lback_f2_%=:\n"
    "s_waitcnt lgkmcnt(4)\n"
    "v_mfma_f32_32x32x16_bf16 v[130:145], a[136:139], v[198:201], v[130:145]\n"
    "ds_read_b64_tr_b16 v[194:195], v250 offset:6144\n"
    "v_xor_b32 v252, s60, v215\n"
    "v_xor_b32 v253, s60, v217\n"
    "v_or_b32 v252, v252, v253\n"
    "v_cmp_ne_u32 vcc, 0, v252\n"
    "s_cbranch_vccnz Lslow_sg_%=\n"
    "Lback_sg_%=:\n"
    "s_lshl1_add_u32 s24, s28, s25\n"
    "buffer_load_dwordx4 v[2:5], v233, s[52:55], s24 offen offset:0 sc1\n"
    "v_mfma_f32_32x32x16_bf16 v[146:161], a[140:143], v[198:201], v[146:161]\n"
    "ds_read_b64_tr_b16 v[196:197], v250 offset:6400\n"
    "v_cmp_gt_u32 vcc, 32, v222\n"
    "v_mov_b32 v220, 0\n"
    "v_mov_b32 v221, 0\n"
    "v_cndmask_b32 v252, 0, v214, vcc\n"
    "v_cndmask_b32 v253, 0, v216, vcc\n"
    "s_waitcnt lgkmcnt(4)\n"
    "v_mfma_f32_32x32x16_bf16 v[130:145], a[144:147], v[202:205], v[130:145]\n"
    "ds_read_b64_tr_b16 v[198:199], v251 offset:0\n"
    "buffer_load_dwordx4 v[6:9], v233, s[52:55], s24 offen offset:1024 sc1\n"
    "v_cvt_pk_bf16_f32 v211, v252, 0\n"
    "v_cvt_pk_bf16_f32 v215, v253, 0\n"
    "v_lshlrev_b32 v254, 16, v211\n"
    "v_lshlrev_b32 v255, 16, v215\n"
    "v_sub_f32_e64 v254, v252, v254\n"
    "v_mfma_f32_32x32x16_bf16 v[146:161], a[148:151], v[202:205], v[146:161]\n"
    "ds_read_b64_tr_b16 v[200:201], v251 offset:256\n"
    "v_sub_f32_e64 v255, v253, v255\n"
    "v_cvt_pk_bf16_f32 v210, v252, v254\n"
    "v_cvt_pk_bf16_f32 v214, v253, v255\n"
    "v_mov_b32 v212, 0\n"
    "v_mov_b32_e64 v213, 0\n"
    "s_waitcnt lgkmcnt(4)\n"
    "v_mfma_f32_32x32x16_bf16 v[130:145], a[152:155], v[206:209], v[130:145]\n"
    "ds_read_b64_tr_b16 v[202:203], v251 offset:1024\n"
    "v_mov_b32 v216, 0\n"
    "v_mov_b32 v217, 0\n"
    "v_mfma_f32_32x32x16_bf16 v[146:161], a[156:159], v[206:209], v[146:161]\n"
    "ds_read_b64_tr_b16 v[204:205], v251 offset:1280\n"
    "buffer_load_dwordx4 v[252:255], v1, s[52:55], s36 offen sc1\n"
    "buffer_load_dwordx4 v[10:13], v233, s[52:55], s24 offen offset:2048 sc1\n"
    "v_max3_u32 v234, v102, v103, v104\n"
    "v_max3_u32 v234, v234, v105, v106\n"
    "v_max3_u32 v234, v234, v107, v108\n"
    "s_waitcnt lgkmcnt(4)\n"
    "v_mfma_f32_32x32x16_bf16 v[130:145], a[160:163], v[194:197], v[130:145]\n"
    "s_nop 0\n"
    "ds_read_b64_tr_b16 v[206:207], v251 offset:2048\n"
    "v_max3_u32 v234, v234, v109, v110\n"
    "v_max3_u32 v234, v234, v111, v112\n"
    "v_max3_u32 v234, v234, v113, v114\n"
    "v_max3_u32 v234, v234, v115, v116\n"
    "v_max3_u32 v234, v234, v117, v118\n"
    "v_mfma_f32_32x32x16_bf16 v[146:161], a[164:167], v[194:197], v[146:161]\n"
    "ds_read_b64_tr_b16 v[208:209], v251 offset:2304\n"
    "v_max3_u32 v234, v234, v119, v120\n"
    "v_max3_u32 v234, v234, v121, v122\n"
    "v_max3_u32 v234, v234, v123, v124\n"
    "v_max3_u32 v234, v234, v125, v126\n"
    "v_max3_u32 v234, v234, v127, v128\n"
    "s_waitcnt lgkmcnt(4)\n"
    "v_mfma_f32_32x32x16_bf16 v[130:145], a[168:171], v[198:201], v[130:145]\n"
    "ds_read_b64_tr_b16 v[194:195], v251 offset:3072\n"
    "v_max_u32 v234, v234, v129\n"
    "v_cmp_eq_u32 vcc, -1, v234\n"
    "s_cbranch_vccnz Lslow_tile1_%=\n"
    "Lback_tile1_%=:\n"
    "buffer_load_dwordx4 v[14:17], v233, s[52:55], s24 offen offset:3072 sc1\n"
    "v_mfma_f32_32x32x16_bf16 v[146:161], a[172:175], v[198:201], v[146:161]\n"
    "ds_read_b64_tr_b16 v[196:197], v251 offset:3328\n"
    "ds_write_b128 v223, v[102:105] offset:28672\n"
    "s_waitcnt lgkmcnt(5)\n"
    "v_mfma_f32_32x32x16_bf16 v[130:145], a[176:179], v[202:205], v[130:145]\n"
    "ds_read_b64_tr_b16 v[198:199], v251 offset:4096\n"
    "ds_write_b128 v223, v[106:109] offset:29696\n"
    "v_mfma_f32_32x32x16_bf16 v[146:161], a[180:183], v[202:205], v[146:161]\n"
    "ds_read_b64_tr_b16 v[200:201], v251 offset:4352\n"
    "ds_write_b128 v223, v[110:113] offset:30720\n"
    "buffer_load_dwordx4 v[18:21], v241, s[52:55], s24 offen offset:0 sc1\n"
    "s_waitcnt lgkmcnt(7)\n"
    "v_mfma_f32_32x32x16_bf16 v[130:145], a[184:187], v[206:209], v[130:145]\n"
    "ds_read_b64_tr_b16 v[202:203], v251 offset:5120\n"
    "ds_write_b128 v223, v[114:117] offset:31744\n"
    "v_mfma_f32_32x32x16_bf16 v[146:161], a[188:191], v[206:209], v[146:161]\n"
    "ds_read_b64_tr_b16 v[204:205], v251 offset:5376\n"
    "ds_write_b128 v223, v[118:121] offset:32768\n"
    "s_waitcnt lgkmcnt(9)\n"
    "v_mfma_f32_32x32x16_bf16 v[130:145], a[192:195], v[194:197], v[130:145]\n"
    "ds_read_b64_tr_b16 v[206:207], v251 offset:6144\n"
    "ds_write_b128 v223, v[122:125] offset:33792\n"
    "ds_write_b128 v223, v[126:129] offset:34816\n"
    "buffer_load_dwordx4 v[22:25], v241, s[52:55], s24 offen offset:1024 sc1\n"
    "v_mfma_f32_32x32x16_bf16 v[146:161], a[196:199], v[194:197], v[146:161]\n"
    "ds_read_b64_tr_b16 v[208:209], v251 offset:6400\n"
    "s_waitcnt lgkmcnt(1)\n"
    "s_barrier\n"
    "s_waitcnt vmcnt(12)\n"
    "v_mfma_f32_32x32x16_bf16 v[130:145], a[200:203], v[198:201], v[130:145]\n"
    "ds_read_b64_tr_b16 v[194:195], v248 offset:28672\n"
    "s_waitcnt vmcnt(11)\n"
    "v_mfma_f32_32x32x16_bf16 v[146:161], a[204:207], v[198:201], v[146:161]\n"
    "ds_read_b64_tr_b16 v[196:197], v248 offset:28928\n"
    "buffer_load_dwordx4 v[26:29], v241, s[52:55], s24 offen offset:2048 sc1\n"
    "s_waitcnt vmcnt(11)\n"
    "v_mfma_f32_32x32x16_bf16 v[130:145], a[208:211], v[202:205], v[130:145]\n"
    "ds_read_b64_tr_b16 v[198:199], v248 offset:29696\n"
    "s_waitcnt vmcnt(5)\n"
    "v_xor_b32 v243, s60, v253\n"
    "v_xor_b32 v228, s60, v255\n"
    "v_or_b32 v243, v243, v228\n"
    "v_cmp_ne_u32 vcc, 0, v243\n"
    "s_cbranch_vccnz Lslow_st0_%=\n"
    "Lback_st0_%=:\n"
    "v_mfma_f32_32x32x16_bf16 v[146:161], a[212:215], v[202:205], v[146:161]\n"
    "ds_read_b64_tr_b16 v[200:201], v248 offset:29952\n"
    "v_cvt_pk_bf16_f32 v218, v252, v252\n"
    "v_rsq_f32 v243, v254\n"
    "v_lshlrev_b32 v253, 16, v218\n"
    "v_mov_b32 v228, v254\n"
    "s_waitcnt lgkmcnt(4)\n"
    "v_mfma_f32_32x32x16_bf16 v[130:145], a[216:219], v[206:209], v[130:145]\n"
    "ds_read_b64_tr_b16 v[202:203], v248 offset:30720\n"
    "buffer_load_dwordx4 v[30:33], v241, s[52:55], s24 offen offset:3072 sc1\n"
    "v_sub_f32_e64 v253, v252, v253\n"
    "v_mul_f32 v243, 0xbfb8aa3b, v243\n"
    "v_cvt_pk_bf16_f32 v219, v253, 0\n"
    "s_add_u32 s36, s36, 8192\n"
    "v_mfma_f32_32x32x16_bf16 v[146:161], a[220:223], v[206:209], v[146:161]\n"
    "ds_read_b64_tr_b16 v[204:205], v248 offset:30976\n"
    "v_mfma_f32_32x32x16_bf16 v[130:145], v[210:213], v[218:221], v[130:145]\n"
    "v_mfma_f32_32x32x16_bf16 v[146:161], v[214:217], v[218:221], v[146:161]\n"
    "s_lshl1_add_u32 s24, s29, s25\n"
    "buffer_load_dwordx4 v[34:37], v233, s[52:55], s24 offen offset:0 sc1\n"
    "s_waitcnt lgkmcnt(4)\n"
    "v_mfma_f32_32x32x16_bf16 v[162:177], a[0:3], v[194:197], 0\n"
    "ds_read_b64_tr_b16 v[206:207], v248 offset:31744\n"
    "s_add_u32 s19, s19, 1835008\n"
    "v_mfma_f32_32x32x16_bf16 v[178:193], a[4:7], v[194:197], 0\n"
    "ds_read_b64_tr_b16 v[208:209], v248 offset:32000\n"
    "s_add_u32 s15, s15, 458752\n"
    "buffer_load_dwordx4 v[102:105], v233, s[52:55], s15 offen offset:0 sc1\n"
    "s_waitcnt lgkmcnt(4)\n"
    "v_mfma_f32_32x32x16_bf16 v[162:177], a[8:11], v[198:201], v[162:177]\n"
    "ds_read_b64_tr_b16 v[194:195], v248 offset:32768\n"
    "v_mul_f32 v252, v130, v243\n"
    "v_mul_f32 v253, v131, v243\n"
    "v_exp_f32 v252, v252\n"
    "v_mfma_f32_32x32x16_bf16 v[178:193], a[12:15], v[198:201], v[178:193]\n"
    "ds_read_b64_tr_b16 v[196:197], v248 offset:33024\n"
    "buffer_load_dwordx4 v[106:109], v233, s[52:55], s15 offen offset:1024 sc1\n"
    "v_exp_f32 v253, v253\n"
    "v_mul_f32_e64 v146, v130, v146\n"
    "s_waitcnt lgkmcnt(4)\n"
    "v_mfma_f32_32x32x16_bf16 v[162:177], a[16:19], v[202:205], v[162:177]\n"
    "ds_read_b64_tr_b16 v[198:199], v248 offset:33792\n"
    "v_mul_f32 v147, v131, v147\n"
    "v_mul_f32 v254, v132, v243\n"
    "v_mul_f32_e64 v255, v133, v243\n"
    "v_fma_f32 v252, v252, v228, v228\n"
    "v_fma_f32 v253, v253, v228, v228\n"
    "v_mfma_f32_32x32x16_bf16 v[178:193], a[20:23], v[202:205], v[178:193]\n"
    "ds_read_b64_tr_b16 v[200:201], v248 offset:34048\n"
    "buffer_load_dwordx4 v[110:113], v233, s[52:55], s15 offen offset:2048 sc1\n"
    "v_rcp_f32 v252, v252\n"
    "s_waitcnt lgkmcnt(4)\n"
    "v_mfma_f32_32x32x16_bf16 v[162:177], a[24:27], v[206:209], v[162:177]\n"
    "ds_read_b64_tr_b16 v[202:203], v248 offset:34816\n"
    "v_rcp_f32 v253, v253\n"
    "v_exp_f32 v254, v254\n"
    "v_mfma_f32_32x32x16_bf16 v[178:193], a[28:31], v[206:209], v[178:193]\n"
    "ds_read_b64_tr_b16 v[204:205], v248 offset:35072\n"
    "buffer_load_dwordx4 v[114:117], v233, s[52:55], s15 offen offset:3072 sc1\n"
    "v_exp_f32 v255, v255\n"
    "v_mul_f32_e64 v148, v132, v148\n"
    "s_waitcnt lgkmcnt(4)\n"
    "v_mfma_f32_32x32x16_bf16 v[162:177], a[32:35], v[194:197], v[162:177]\n"
    "ds_read_b64_tr_b16 v[206:207], v249 offset:28672\n"
    "v_mul_f32 v149, v133, v149\n"
    "v_mul_f32 v224, v134, v243\n"
    "v_mul_f32 v225, v135, v243\n"
    "v_mul_f32 v146, v146, v252\n"
    "v_mul_f32_e64 v147, v147, v253\n"
    "v_mfma_f32_32x32x16_bf16 v[178:193], a[36:39], v[194:197], v[178:193]\n"
    "ds_read_b64_tr_b16 v[208:209], v249 offset:28928\n"
    "buffer_load_dwordx4 v[118:121], v241, s[52:55], s15 offen offset:0 sc1\n"
    "v_cvt_pk_bf16_f32 v130, v146, v147\n"
    "v_fma_f32 v254, v254, v228, v228\n"
    "v_fma_f32 v255, v255, v228, v228\n"
    "s_waitcnt lgkmcnt(4)\n"
    "v_mfma_f32_32x32x16_bf16 v[162:177], a[40:43], v[198:201], v[162:177]\n"
    "ds_read_b64_tr_b16 v[194:195], v249 offset:29696\n"
    "v_rcp_f32 v254, v254\n"
    "v_rcp_f32_e64 v255, v255\n"
    "v_mfma_f32_32x32x16_bf16 v[178:193], a[44:47], v[198:201], v[178:193]\n"
    "ds_read_b64_tr_b16 v[196:197], v249 offset:29952\n"
    "buffer_load_dwordx4 v[122:125], v241, s[52:55], s15 offen offset:1024 sc1\n"
    "v_exp_f32 v224, v224\n"
    "s_waitcnt lgkmcnt(4)\n"
    "v_mfma_f32_32x32x16_bf16 v[162:177], a[48:51], v[202:205], v[162:177]\n"
    "ds_read_b64_tr_b16 v[198:199], v249 offset:30720\n"
    "v_exp_f32 v225, v225\n"
    "v_mul_f32 v150, v134, v150\n"
    "v_mul_f32 v151, v135, v151\n"
    "v_mul_f32 v226, v136, v243\n"
    "v_mfma_f32_32x32x16_bf16 v[178:193], a[52:55], v[202:205], v[178:193]\n"
    "ds_read_b64_tr_b16 v[200:201], v249 offset:30976\n"
    "buffer_load_dwordx4 v[126:129], v241, s[52:55], s15 offen offset:2048 sc1\n"
    "v_mul_f32 v227, v137, v243\n"
    "v_mul_f32 v148, v148, v254\n"
    "v_mul_f32 v149, v149, v255\n"
    "s_waitcnt lgkmcnt(4)\n"
    "v_mfma_f32_32x32x16_bf16 v[162:177], a[56:59], v[206:209], v[162:177]\n"
    "ds_read_b64_tr_b16 v[202:203], v249 offset:31744\n"
    "v_cvt_pk_bf16_f32 v131, v148, v149\n"
    "v_fma_f32 v224, v224, v228, v228\n"
    "v_fma_f32 v225, v225, v228, v228\n"
    "v_rcp_f32_e64 v224, v224\n"
    "v_mfma_f32_32x32x16_bf16 v[178:193], a[60:63], v[206:209], v[178:193]\n"
    "ds_read_b64_tr_b16 v[204:205], v249 offset:32000\n"
    "buffer_load_dwordx4 v[38:41], v233, s[52:55], s24 offen offset:1024 sc1\n"
    "v_rcp_f32 v225, v225\n"
    "v_exp_f32_e64 v226, v226\n"
    "s_waitcnt lgkmcnt(4)\n"
    "v_mfma_f32_32x32x16_bf16 v[162:177], a[64:67], v[194:197], v[162:177]\n"
    "ds_read_b64_tr_b16 v[206:207], v249 offset:32768\n"
    "v_exp_f32 v227, v227\n"
    "v_mul_f32 v152, v136, v152\n"
    "v_mul_f32 v153, v137, v153\n"
    "v_mul_f32 v252, v138, v243\n"
    "v_mfma_f32_32x32x16_bf16 v[178:193], a[68:71], v[194:197], v[178:193]\n"
    "ds_read_b64_tr_b16 v[208:209], v249 offset:33024\n"
    "buffer_load_dwordx4 v[42:45], v233, s[52:55], s24 offen offset:2048 sc1\n"
    "v_mul_f32 v253, v139, v243\n"
    "v_mul_f32 v150, v150, v224\n"
    "v_mul_f32_e64 v151, v151, v225\n"
    "v_cvt_pk_bf16_f32 v132, v150, v151\n"
    "v_fma_f32 v226, v226, v228, v228\n"
    "s_waitcnt lgkmcnt(4)\n"
    "v_mfma_f32_32x32x16_bf16 v[162:177], a[72:75], v[198:201], v[162:177]\n"
    "ds_read_b64_tr_b16 v[194:195], v249 offset:33792\n"
    "v_fma_f32 v227, v227, v228, v228\n"
    "v_rcp_f32 v226, v226\n"
    "v_rcp_f32_e64 v227, v227\n"
    "v_mfma_f32_32x32x16_bf16 v[178:193], a[76:79], v[198:201], v[178:193]\n"
    "ds_read_b64_tr_b16 v[196:197], v249 offset:34048\n"
    "v_exp_f32 v252, v252\n"
    "v_exp_f32 v253, v253\n"
    "v_mul_f32 v154, v138, v154\n"
    "s_waitcnt lgkmcnt(4)\n"
    "v_mfma_f32_32x32x16_bf16 v[162:177], a[80:83], v[202:205], v[162:177]\n"
    "ds_read_b64_tr_b16 v[198:199], v249 offset:34816\n"
    "buffer_load_dwordx4 v[46:49], v233, s[52:55], s24 offen offset:3072 sc1\n"
    "v_mul_f32 v155, v139, v155\n"
    "v_mul_f32 v254, v140, v243\n"
    "v_mul_f32 v255, v141, v243\n"
    "v_mul_f32 v152, v152, v226\n"
    "v_mul_f32_e64 v153, v153, v227\n"
    "v_mfma_f32_32x32x16_bf16 v[178:193], a[84:87], v[202:205], v[178:193]\n"
    "ds_read_b64_tr_b16 v[200:201], v249 offset:35072\n"
    "v_cvt_pk_bf16_f32 v133, v152, v153\n"
    "v_fma_f32 v252, v252, v228, v228\n"
    "v_fma_f32 v253, v253, v228, v228\n"
    "v_rcp_f32 v252, v252\n"
    "s_waitcnt lgkmcnt(4)\n"
    "v_mfma_f32_32x32x16_bf16 v[162:177], a[88:91], v[206:209], v[162:177]\n"
    "ds_read_b64_tr_b16 v[202:203], v250 offset:28672\n"
    "buffer_store_dwordx4 v[130:133], v233, s[52:55], s19 offen offset:0 sc1\n"
    "v_rcp_f32_e64 v253, v253\n"
    "v_mfma_f32_32x32x16_bf16 v[178:193], a[92:95], v[206:209], v[178:193]\n"
    "ds_read_b64_tr_b16 v[204:205], v250 offset:28928\n"
    "buffer_load_dwordx4 v[50:53], v241, s[52:55], s24 offen offset:0 sc1\n"
    "v_exp_f32 v254, v254\n"
    "v_exp_f32 v255, v255\n"
    "v_mul_f32 v156, v140, v156\n"
    "s_waitcnt lgkmcnt(4)\n"
    "v_mfma_f32_32x32x16_bf16 v[162:177], a[96:99], v[194:197], v[162:177]\n"
    "ds_read_b64_tr_b16 v[206:207], v250 offset:29696\n"
    "v_mul_f32 v157, v141, v157\n"
    "v_mul_f32 v224, v142, v243\n"
    "v_mul_f32 v225, v143, v243\n"
    "v_mul_f32 v154, v154, v252\n"
    "v_mul_f32_e64 v155, v155, v253\n"
    "v_mfma_f32_32x32x16_bf16 v[178:193], a[100:103], v[194:197], v[178:193]\n"
    "ds_read_b64_tr_b16 v[208:209], v250 offset:29952\n"
    "v_cvt_pk_bf16_f32 v134, v154, v155\n"
    "v_fma_f32 v254, v254, v228, v228\n"
    "v_fma_f32 v255, v255, v228, v228\n"
    "v_rcp_f32 v254, v254\n"
    "s_waitcnt lgkmcnt(4)\n"
    "v_mfma_f32_32x32x16_bf16 v[162:177], a[104:107], v[198:201], v[162:177]\n"
    "ds_read_b64_tr_b16 v[194:195], v250 offset:30720\n"
    "buffer_load_dwordx4 v[54:57], v241, s[52:55], s24 offen offset:1024 sc1\n"
    "v_rcp_f32 v255, v255\n"
    "v_exp_f32 v224, v224\n"
    "v_mfma_f32_32x32x16_bf16 v[178:193], a[108:111], v[198:201], v[178:193]\n"
    "ds_read_b64_tr_b16 v[196:197], v250 offset:30976\n"
    "v_exp_f32 v225, v225\n"
    "v_mul_f32 v158, v142, v158\n"
    "v_mul_f32 v159, v143, v159\n"
    "v_mul_f32_e64 v226, v144, v243\n"
    "s_waitcnt lgkmcnt(4)\n"
    "v_mfma_f32_32x32x16_bf16 v[162:177], a[112:115], v[202:205], v[162:177]\n"
    "ds_read_b64_tr_b16 v[198:199], v250 offset:31744\n"
    "v_mul_f32 v227, v145, v243\n"
    "v_mul_f32 v156, v156, v254\n"
    "v_mul_f32_e64 v157, v157, v255\n"
    "v_cvt_pk_bf16_f32 v135, v156, v157\n"
    "v_fma_f32 v224, v224, v228, v228\n"
    "v_mfma_f32_32x32x16_bf16 v[178:193], a[116:119], v[202:205], v[178:193]\n"
    "ds_read_b64_tr_b16 v[200:201], v250 offset:32000\n"
    "buffer_load_dwordx4 v[58:61], v241, s[52:55], s24 offen offset:2048 sc1\n"
    "v_fma_f32 v225, v225, v228, v228\n"
    "v_rcp_f32 v224, v224\n"
    "v_rcp_f32_e64 v225, v225\n"
    "s_waitcnt lgkmcnt(4)\n"
    "v_mfma_f32_32x32x16_bf16 v[162:177], a[120:123], v[206:209], v[162:177]\n"
    "ds_read_b64_tr_b16 v[202:203], v250 offset:32768\n"
    "v_exp_f32 v226, v226\n"
    "v_exp_f32 v227, v227\n"
    "v_mul_f32_e64 v160, v144, v160\n"
    "v_mfma_f32_32x32x16_bf16 v[178:193], a[124:127], v[206:209], v[178:193]\n"
    "ds_read_b64_tr_b16 v[204:205], v250 offset:33024\n"
    "buffer_load_dwordx4 v[62:65], v241, s[52:55], s24 offen offset:3072 sc1\n"
    "v_mul_f32 v161, v145, v161\n"
    "v_mul_f32 v158, v158, v224\n"
    "v_mul_f32_e64 v159, v159, v225\n"
    "v_cvt_pk_bf16_f32 v136, v158, v159\n"
    "v_fma_f32 v226, v226, v228, v228\n"
    "s_waitcnt lgkmcnt(4)\n"
    "v_mfma_f32_32x32x16_bf16 v[162:177], a[128:131], v[194:197], v[162:177]\n"
    "ds_read_b64_tr_b16 v[206:207], v250 offset:33792\n"
    "v_fma_f32 v227, v227, v228, v228\n"
    "v_rcp_f32 v226, v226\n"
    "v_rcp_f32_e64 v227, v227\n"
    "v_mfma_f32_32x32x16_bf16 v[178:193], a[132:135], v[194:197], v[178:193]\n"
    "ds_read_b64_tr_b16 v[208:209], v250 offset:34048\n"
    "v_mul_f32 v160, v160, v226\n"
    "v_mul_f32 v161, v161, v227\n"
    "v_cvt_pk_bf16_f32 v137, v160, v161\n"
    "s_waitcnt lgkmcnt(4)\n"
    "v_mfma_f32_32x32x16_bf16 v[162:177], a[136:139], v[198:201], v[162:177]\n"
    "ds_read_b64_tr_b16 v[194:195], v250 offset:34816\n"
    "buffer_store_dwordx4 v[134:137], v233, s[52:55], s19 offen offset:1024 sc1\n"
    "s_waitcnt vmcnt(15)\n"
    "v_max3_u32 v234, v102, v103, v104\n"
    "s_waitcnt vmcnt(14)\n"
    "v_max3_u32 v234, v234, v105, v106\n"
    "v_max3_u32 v234, v234, v107, v108\n"
    "s_lshl1_add_u32 s24, s30, s25\n"
    "buffer_load_dwordx4 v[66:69], v233, s[52:55], s24 offen offset:0 sc1\n"
    "v_mfma_f32_32x32x16_bf16 v[178:193], a[140:143], v[198:201], v[178:193]\n"
    "ds_read_b64_tr_b16 v[196:197], v250 offset:35072\n"
    "buffer_load_dwordx4 v[252:255], v1, s[52:55], s36 offen sc1\n"
    "s_waitcnt vmcnt(15)\n"
    "v_max3_u32 v234, v234, v109, v110\n"
    "v_max3_u32 v234, v234, v111, v112\n"
    "s_waitcnt vmcnt(14)\n"
    "v_max3_u32 v234, v234, v113, v114\n"
    "s_waitcnt lgkmcnt(4)\n"
    "v_mfma_f32_32x32x16_bf16 v[162:177], a[144:147], v[202:205], v[162:177]\n"
    "ds_read_b64_tr_b16 v[198:199], v251 offset:28672\n"
    "v_max3_u32 v234, v234, v115, v116\n"
    "s_waitcnt vmcnt(13)\n"
    "v_max3_u32 v234, v234, v117, v118\n"
    "v_max3_u32 v234, v234, v119, v120\n"
    "s_waitcnt vmcnt(12)\n"
    "v_max3_u32 v234, v234, v121, v122\n"
    "v_max3_u32 v234, v234, v123, v124\n"
    "v_mfma_f32_32x32x16_bf16 v[178:193], a[148:151], v[202:205], v[178:193]\n"
    "ds_read_b64_tr_b16 v[200:201], v251 offset:28928\n"
    "s_waitcnt vmcnt(11)\n"
    "v_max3_u32 v234, v234, v125, v126\n"
    "v_max3_u32 v234, v234, v127, v128\n"
    "v_max_u32 v234, v234, v129\n"
    "v_cmp_eq_u32_e64 vcc, -1, v234\n"
    "s_cbranch_vccnz Lslow_tile2_%=\n"
    "Lback_tile2_%=:\n"
    "buffer_load_dwordx4 v[70:73], v233, s[52:55], s24 offen offset:1024 sc1\n"
    "s_waitcnt lgkmcnt(4)\n"
    "v_mfma_f32_32x32x16_bf16 v[162:177], a[152:155], v[206:209], v[162:177]\n"
    "ds_read_b64_tr_b16 v[202:203], v251 offset:29696\n"
    "ds_write_b128 v223, v[102:105] offset:57344\n"
    "v_mfma_f32_32x32x16_bf16 v[178:193], a[156:159], v[206:209], v[178:193]\n"
    "ds_read_b64_tr_b16 v[204:205], v251 offset:29952\n"
    "ds_write_b128 v223, v[106:109] offset:58368\n"
    "s_waitcnt lgkmcnt(6)\n"
    "v_mfma_f32_32x32x16_bf16 v[162:177], a[160:163], v[194:197], v[162:177]\n"
    "ds_read_b64_tr_b16 v[206:207], v251 offset:30720\n"
    "ds_write_b128 v223, v[110:113] offset:59392\n"
    "buffer_load_dwordx4 v[74:77], v233, s[52:55], s24 offen offset:2048 sc1\n"
    "v_mfma_f32_32x32x16_bf16 v[178:193], a[164:167], v[194:197], v[178:193]\n"
    "ds_read_b64_tr_b16 v[208:209], v251 offset:30976\n"
    "ds_write_b128 v223, v[114:117] offset:60416\n"
    "s_waitcnt lgkmcnt(8)\n"
    "v_mfma_f32_32x32x16_bf16 v[162:177], a[168:171], v[198:201], v[162:177]\n"
    "ds_read_b64_tr_b16 v[194:195], v251 offset:31744\n"
    "ds_write_b128 v223, v[118:121] offset:61440\n"
    "v_mfma_f32_32x32x16_bf16 v[178:193], a[172:175], v[198:201], v[178:193]\n"
    "ds_read_b64_tr_b16 v[196:197], v251 offset:32000\n"
    "ds_write_b128 v223, v[122:125] offset:62464\n"
    "buffer_load_dwordx4 v[78:81], v233, s[52:55], s24 offen offset:3072 sc1\n"
    "s_waitcnt lgkmcnt(9)\n"
    "v_mfma_f32_32x32x16_bf16 v[162:177], a[176:179], v[202:205], v[162:177]\n"
    "ds_read_b64_tr_b16 v[198:199], v251 offset:32768\n"
    "ds_write_b128 v223, v[126:129] offset:63488\n"
    "v_mfma_f32_32x32x16_bf16 v[178:193], a[180:183], v[202:205], v[178:193]\n"
    "ds_read_b64_tr_b16 v[200:201], v251 offset:33024\n"
    "buffer_load_dwordx4 v[82:85], v241, s[52:55], s24 offen offset:0 sc1\n"
    "s_waitcnt lgkmcnt(8)\n"
    "v_mfma_f32_32x32x16_bf16 v[162:177], a[184:187], v[206:209], v[162:177]\n"
    "ds_read_b64_tr_b16 v[202:203], v251 offset:33792\n"
    "v_mfma_f32_32x32x16_bf16 v[178:193], a[188:191], v[206:209], v[178:193]\n"
    "ds_read_b64_tr_b16 v[204:205], v251 offset:34048\n"
    "s_waitcnt lgkmcnt(6)\n"
    "v_mfma_f32_32x32x16_bf16 v[162:177], a[192:195], v[194:197], v[162:177]\n"
    "ds_read_b64_tr_b16 v[206:207], v251 offset:34816\n"
    "buffer_load_dwordx4 v[86:89], v241, s[52:55], s24 offen offset:1024 sc1\n"
    "v_mfma_f32_32x32x16_bf16 v[178:193], a[196:199], v[194:197], v[178:193]\n"
    "ds_read_b64_tr_b16 v[208:209], v251 offset:35072\n"
    "s_waitcnt lgkmcnt(5)\n"
    "s_barrier\n"
    "s_waitcnt lgkmcnt(4)\n"
    "v_mfma_f32_32x32x16_bf16 v[162:177], a[200:203], v[198:201], v[162:177]\n"
    "ds_read_b64_tr_b16 v[194:195], v248 offset:57344\n"
    "v_mfma_f32_32x32x16_bf16 v[178:193], a[204:207], v[198:201], v[178:193]\n"
    "ds_read_b64_tr_b16 v[196:197], v248 offset:57600\n"
    "buffer_load_dwordx4 v[90:93], v241, s[52:55], s24 offen offset:2048 sc1\n"
    "s_waitcnt lgkmcnt(4)\n"
    "v_mfma_f32_32x32x16_bf16 v[162:177], a[208:211], v[202:205], v[162:177]\n"
    "ds_read_b64_tr_b16 v[198:199], v248 offset:58368\n"
    "s_waitcnt vmcnt(6)\n"
    "v_xor_b32 v244, s60, v253\n"
    "v_xor_b32 v229, s60, v255\n"
    "v_or_b32 v244, v244, v229\n"
    "v_cmp_ne_u32 vcc, 0, v244\n"
    "s_cbranch_vccnz Lslow_st1_%=\n"
    "Lback_st1_%=:\n"
    "v_mfma_f32_32x32x16_bf16 v[178:193], a[212:215], v[202:205], v[178:193]\n"
    "ds_read_b64_tr_b16 v[200:201], v248 offset:58624\n"
    "v_cvt_pk_bf16_f32 v218, v252, v252\n"
    "v_rsq_f32 v244, v254\n"
    "v_lshlrev_b32 v253, 16, v218\n"
    "v_mov_b32 v229, v254\n"
    "s_waitcnt lgkmcnt(4)\n"
    "v_mfma_f32_32x32x16_bf16 v[162:177], a[216:219], v[206:209], v[162:177]\n"
    "ds_read_b64_tr_b16 v[202:203], v248 offset:59392\n"
    "buffer_load_dwordx4 v[94:97], v241, s[52:55], s24 offen offset:3072 sc1\n"
    "v_sub_f32_e64 v253, v252, v253\n"
    "v_mul_f32 v244, 0xbfb8aa3b, v244\n"
    "v_cvt_pk_bf16_f32 v219, v253, 0\n"
    "s_add_u32 s36, s36, 8192\n"
    "v_mfma_f32_32x32x16_bf16 v[178:193], a[220:223], v[206:209], v[178:193]\n"
    "ds_read_b64_tr_b16 v[204:205], v248 offset:59648\n"
    "s_mov_b64 exec, 1\n"
    "buffer_store_dword v231, off, s[52:55], s35 offset:0 sc1\n"
    "s_mov_b64 exec, -1\n"
    "s_add_u32 s35, s35, 4096\n"
    "v_mfma_f32_32x32x16_bf16 v[162:177], v[210:213], v[218:221], v[162:177]\n"
    "v_cmp_gt_u32 vcc, 2, v222\n"
    "s_lshl2_add_u32 s64, s58, s13\n"
    "s_lshl_b32 s65, s9, 1\n"
    "v_add_u32 v234, s65, v222\n"
    "v_mov_b32_e64 v240, s64\n"
    "s_mov_b32 s67, 8192\n"
    "v_cndmask_b32 v234, v240, v234, vcc\n"
    "v_lshlrev_b32 v234, 7, v234\n"
    "v_mfma_f32_32x32x16_bf16 v[178:193], v[214:217], v[218:221], v[178:193]\n"
    "s_lshl1_add_u32 s24, s31, s25\n"
    "buffer_load_dwordx4 v[98:101], v233, s[52:55], s24 offen offset:0 sc1\n"
    "buffer_load_dword v240, v234, s[52:55], s67 offen sc1\n"
    "s_waitcnt lgkmcnt(4)\n"
    "v_mfma_f32_32x32x16_bf16 v[130:145], a[0:3], v[194:197], 0\n"
    "ds_read_b64_tr_b16 v[206:207], v248 offset:60416\n"
    "s_add_u32 s19, s19, 1835008\n"
    "v_mfma_f32_32x32x16_bf16 v[146:161], a[4:7], v[194:197], 0\n"
    "ds_read_b64_tr_b16 v[208:209], v248 offset:60672\n"
    "s_add_u32 s15, s15, 458752\n"
    "buffer_load_dwordx4 v[102:105], v233, s[52:55], s15 offen offset:0 sc1\n"
    "s_waitcnt lgkmcnt(4)\n"
    "v_mfma_f32_32x32x16_bf16 v[130:145], a[8:11], v[198:201], v[130:145]\n"
    "ds_read_b64_tr_b16 v[194:195], v248 offset:61440\n"
    "v_mul_f32 v252, v162, v244\n"
    "v_mul_f32 v253, v163, v244\n"
    "v_exp_f32 v252, v252\n"
    "v_mfma_f32_32x32x16_bf16 v[146:161], a[12:15], v[198:201], v[146:161]\n"
    "ds_read_b64_tr_b16 v[196:197], v248 offset:61696\n"
    "buffer_load_dwordx4 v[106:109], v233, s[52:55], s15 offen offset:1024 sc1\n"
    "v_exp_f32 v253, v253\n"
    "v_mul_f32_e64 v178, v162, v178\n"
    "s_waitcnt lgkmcnt(4)\n"
    "v_mfma_f32_32x32x16_bf16 v[130:145], a[16:19], v[202:205], v[130:145]\n"
    "ds_read_b64_tr_b16 v[198:199], v248 offset:62464\n"
    "v_mul_f32 v179, v163, v179\n"
    "v_mul_f32 v254, v164, v244\n"
    "v_mul_f32_e64 v255, v165, v244\n"
    "v_fma_f32 v252, v252, v229, v229\n"
    "v_fma_f32 v253, v253, v229, v229\n"
    "v_mfma_f32_32x32x16_bf16 v[146:161], a[20:23], v[202:205], v[146:161]\n"
    "ds_read_b64_tr_b16 v[200:201], v248 offset:62720\n"
    "buffer_load_dwordx4 v[110:113], v233, s[52:55], s15 offen offset:2048 sc1\n"
    "v_rcp_f32 v252, v252\n"
    "s_waitcnt lgkmcnt(4)\n"
    "v_mfma_f32_32x32x16_bf16 v[130:145], a[24:27], v[206:209], v[130:145]\n"
    "ds_read_b64_tr_b16 v[202:203], v248 offset:63488\n"
    "v_rcp_f32 v253, v253\n"
    "v_exp_f32 v254, v254\n"
    "v_mfma_f32_32x32x16_bf16 v[146:161], a[28:31], v[206:209], v[146:161]\n"
    "ds_read_b64_tr_b16 v[204:205], v248 offset:63744\n"
    "buffer_load_dwordx4 v[114:117], v233, s[52:55], s15 offen offset:3072 sc1\n"
    "v_exp_f32 v255, v255\n"
    "v_mul_f32_e64 v180, v164, v180\n"
    "s_waitcnt lgkmcnt(4)\n"
    "v_mfma_f32_32x32x16_bf16 v[130:145], a[32:35], v[194:197], v[130:145]\n"
    "ds_read_b64_tr_b16 v[206:207], v249 offset:57344\n"
    "v_mul_f32 v181, v165, v181\n"
    "v_mul_f32 v224, v166, v244\n"
    "v_mul_f32 v225, v167, v244\n"
    "v_mul_f32 v178, v178, v252\n"
    "v_mul_f32_e64 v179, v179, v253\n"
    "v_mfma_f32_32x32x16_bf16 v[146:161], a[36:39], v[194:197], v[146:161]\n"
    "ds_read_b64_tr_b16 v[208:209], v249 offset:57600\n"
    "buffer_load_dwordx4 v[118:121], v241, s[52:55], s15 offen offset:0 sc1\n"
    "v_cvt_pk_bf16_f32 v162, v178, v179\n"
    "v_fma_f32 v254, v254, v229, v229\n"
    "v_fma_f32 v255, v255, v229, v229\n"
    "s_waitcnt lgkmcnt(4)\n"
    "v_mfma_f32_32x32x16_bf16 v[130:145], a[40:43], v[198:201], v[130:145]\n"
    "ds_read_b64_tr_b16 v[194:195], v249 offset:58368\n"
    "v_rcp_f32 v254, v254\n"
    "v_rcp_f32_e64 v255, v255\n"
    "v_mfma_f32_32x32x16_bf16 v[146:161], a[44:47], v[198:201], v[146:161]\n"
    "ds_read_b64_tr_b16 v[196:197], v249 offset:58624\n"
    "buffer_load_dwordx4 v[122:125], v241, s[52:55], s15 offen offset:1024 sc1\n"
    "v_exp_f32 v224, v224\n"
    "s_waitcnt lgkmcnt(4)\n"
    "v_mfma_f32_32x32x16_bf16 v[130:145], a[48:51], v[202:205], v[130:145]\n"
    "ds_read_b64_tr_b16 v[198:199], v249 offset:59392\n"
    "v_exp_f32 v225, v225\n"
    "v_mul_f32 v182, v166, v182\n"
    "v_mul_f32 v183, v167, v183\n"
    "v_mul_f32 v226, v168, v244\n"
    "v_mfma_f32_32x32x16_bf16 v[146:161], a[52:55], v[202:205], v[146:161]\n"
    "ds_read_b64_tr_b16 v[200:201], v249 offset:59648\n"
    "buffer_load_dwordx4 v[126:129], v241, s[52:55], s15 offen offset:2048 sc1\n"
    "v_mul_f32 v227, v169, v244\n"
    "v_mul_f32 v180, v180, v254\n"
    "v_mul_f32 v181, v181, v255\n"
    "s_waitcnt lgkmcnt(4)\n"
    "v_mfma_f32_32x32x16_bf16 v[130:145], a[56:59], v[206:209], v[130:145]\n"
    "ds_read_b64_tr_b16 v[202:203], v249 offset:60416\n"
    "v_cvt_pk_bf16_f32 v163, v180, v181\n"
    "v_fma_f32 v224, v224, v229, v229\n"
    "v_fma_f32 v225, v225, v229, v229\n"
    "v_rcp_f32_e64 v224, v224\n"
    "v_mfma_f32_32x32x16_bf16 v[146:161], a[60:63], v[206:209], v[146:161]\n"
    "ds_read_b64_tr_b16 v[204:205], v249 offset:60672\n"
    "buffer_load_dwordx4 a[224:227], v233, s[52:55], s24 offen offset:1024 sc1\n"
    "v_rcp_f32 v225, v225\n"
    "v_exp_f32_e64 v226, v226\n"
    "s_waitcnt lgkmcnt(4)\n"
    "v_mfma_f32_32x32x16_bf16 v[130:145], a[64:67], v[194:197], v[130:145]\n"
    "ds_read_b64_tr_b16 v[206:207], v249 offset:61440\n"
    "v_exp_f32 v227, v227\n"
    "v_mul_f32 v184, v168, v184\n"
    "v_mul_f32 v185, v169, v185\n"
    "v_mul_f32 v252, v170, v244\n"
    "v_mfma_f32_32x32x16_bf16 v[146:161], a[68:71], v[194:197], v[146:161]\n"
    "ds_read_b64_tr_b16 v[208:209], v249 offset:61696\n"
    "buffer_load_dwordx4 a[228:231], v233, s[52:55], s24 offen offset:2048 sc1\n"
    "v_mul_f32 v253, v171, v244\n"
    "v_mul_f32 v182, v182, v224\n"
    "v_mul_f32_e64 v183, v183, v225\n"
    "v_cvt_pk_bf16_f32 v164, v182, v183\n"
    "v_fma_f32 v226, v226, v229, v229\n"
    "s_waitcnt lgkmcnt(4)\n"
    "v_mfma_f32_32x32x16_bf16 v[130:145], a[72:75], v[198:201], v[130:145]\n"
    "ds_read_b64_tr_b16 v[194:195], v249 offset:62464\n"
    "v_fma_f32 v227, v227, v229, v229\n"
    "v_rcp_f32 v226, v226\n"
    "v_rcp_f32_e64 v227, v227\n"
    "v_mfma_f32_32x32x16_bf16 v[146:161], a[76:79], v[198:201], v[146:161]\n"
    "ds_read_b64_tr_b16 v[196:197], v249 offset:62720\n"
    "v_exp_f32 v252, v252\n"
    "v_exp_f32 v253, v253\n"
    "v_mul_f32 v186, v170, v186\n"
    "s_waitcnt lgkmcnt(4)\n"
    "v_mfma_f32_32x32x16_bf16 v[130:145], a[80:83], v[202:205], v[130:145]\n"
    "ds_read_b64_tr_b16 v[198:199], v249 offset:63488\n"
    "buffer_load_dwordx4 a[232:235], v233, s[52:55], s24 offen offset:3072 sc1\n"
    "v_mul_f32 v187, v171, v187\n"
    "v_mul_f32 v254, v172, v244\n"
    "v_mul_f32 v255, v173, v244\n"
    "v_mul_f32 v184, v184, v226\n"
    "v_mul_f32_e64 v185, v185, v227\n"
    "v_mfma_f32_32x32x16_bf16 v[146:161], a[84:87], v[202:205], v[146:161]\n"
    "ds_read_b64_tr_b16 v[200:201], v249 offset:63744\n"
    "v_cvt_pk_bf16_f32 v165, v184, v185\n"
    "v_fma_f32 v252, v252, v229, v229\n"
    "v_fma_f32 v253, v253, v229, v229\n"
    "v_rcp_f32 v252, v252\n"
    "s_waitcnt lgkmcnt(4)\n"
    "v_mfma_f32_32x32x16_bf16 v[130:145], a[88:91], v[206:209], v[130:145]\n"
    "ds_read_b64_tr_b16 v[202:203], v250 offset:57344\n"
    "buffer_store_dwordx4 v[162:165], v233, s[52:55], s19 offen offset:0 sc1\n"
    "v_rcp_f32_e64 v253, v253\n"
    "v_mfma_f32_32x32x16_bf16 v[146:161], a[92:95], v[206:209], v[146:161]\n"
    "ds_read_b64_tr_b16 v[204:205], v250 offset:57600\n"
    "buffer_load_dwordx4 a[236:239], v241, s[52:55], s24 offen offset:0 sc1\n"
    "v_exp_f32 v254, v254\n"
    "v_exp_f32 v255, v255\n"
    "v_mul_f32 v188, v172, v188\n"
    "s_waitcnt lgkmcnt(4)\n"
    "v_mfma_f32_32x32x16_bf16 v[130:145], a[96:99], v[194:197], v[130:145]\n"
    "ds_read_b64_tr_b16 v[206:207], v250 offset:58368\n"
    "v_mul_f32 v189, v173, v189\n"
    "v_mul_f32 v224, v174, v244\n"
    "v_mul_f32 v225, v175, v244\n"
    "v_mul_f32 v186, v186, v252\n"
    "v_mul_f32_e64 v187, v187, v253\n"
    "v_mfma_f32_32x32x16_bf16 v[146:161], a[100:103], v[194:197], v[146:161]\n"
    "ds_read_b64_tr_b16 v[208:209], v250 offset:58624\n"
    "v_cvt_pk_bf16_f32 v166, v186, v187\n"
    "v_fma_f32 v254, v254, v229, v229\n"
    "v_fma_f32 v255, v255, v229, v229\n"
    "v_rcp_f32 v254, v254\n"
    "s_waitcnt lgkmcnt(4)\n"
    "v_mfma_f32_32x32x16_bf16 v[130:145], a[104:107], v[198:201], v[130:145]\n"
    "ds_read_b64_tr_b16 v[194:195], v250 offset:59392\n"
    "buffer_load_dwordx4 a[240:243], v241, s[52:55], s24 offen offset:1024 sc1\n"
    "v_rcp_f32 v255, v255\n"
    "v_exp_f32 v224, v224\n"
    "v_mfma_f32_32x32x16_bf16 v[146:161], a[108:111], v[198:201], v[146:161]\n"
    "ds_read_b64_tr_b16 v[196:197], v250 offset:59648\n"
    "v_exp_f32 v225, v225\n"
    "v_mul_f32 v190, v174, v190\n"
    "v_mul_f32 v191, v175, v191\n"
    "v_mul_f32_e64 v226, v176, v244\n"
    "s_waitcnt lgkmcnt(4)\n"
    "v_mfma_f32_32x32x16_bf16 v[130:145], a[112:115], v[202:205], v[130:145]\n"
    "ds_read_b64_tr_b16 v[198:199], v250 offset:60416\n"
    "v_mul_f32 v227, v177, v244\n"
    "v_mul_f32 v188, v188, v254\n"
    "v_mul_f32_e64 v189, v189, v255\n"
    "v_cvt_pk_bf16_f32 v167, v188, v189\n"
    "v_fma_f32 v224, v224, v229, v229\n"
    "v_mfma_f32_32x32x16_bf16 v[146:161], a[116:119], v[202:205], v[146:161]\n"
    "ds_read_b64_tr_b16 v[200:201], v250 offset:60672\n"
    "buffer_load_dwordx4 a[244:247], v241, s[52:55], s24 offen offset:2048 sc1\n"
    "v_fma_f32 v225, v225, v229, v229\n"
    "v_rcp_f32 v224, v224\n"
    "v_rcp_f32_e64 v225, v225\n"
    "s_waitcnt lgkmcnt(4)\n"
    "v_mfma_f32_32x32x16_bf16 v[130:145], a[120:123], v[206:209], v[130:145]\n"
    "ds_read_b64_tr_b16 v[202:203], v250 offset:61440\n"
    "v_exp_f32 v226, v226\n"
    "v_exp_f32 v227, v227\n"
    "v_mul_f32 v192, v176, v192\n"
    "s_waitcnt vmcnt(14)\n"
    "v_cmp_ne_u32 vcc, s60, v240\n"
    "s_cbranch_vccnz Lslow_fc_%=\n"
    "Lback_fc_%=:\n"
    "v_mfma_f32_32x32x16_bf16 v[146:161], a[124:127], v[206:209], v[146:161]\n"
    "ds_read_b64_tr_b16 v[204:205], v250 offset:61696\n"
    "buffer_load_dwordx4 a[248:251], v241, s[52:55], s24 offen offset:3072 sc1\n"
    "v_mul_f32 v193, v177, v193\n"
    "v_mul_f32 v190, v190, v224\n"
    "v_mul_f32_e64 v191, v191, v225\n"
    "v_cvt_pk_bf16_f32 v168, v190, v191\n"
    "v_fma_f32 v226, v226, v229, v229\n"
    "s_waitcnt lgkmcnt(4)\n"
    "v_mfma_f32_32x32x16_bf16 v[130:145], a[128:131], v[194:197], v[130:145]\n"
    "ds_read_b64_tr_b16 v[206:207], v250 offset:62464\n"
    "v_fma_f32 v227, v227, v229, v229\n"
    "v_rcp_f32 v226, v226\n"
    "v_rcp_f32_e64 v227, v227\n"
    "v_mfma_f32_32x32x16_bf16 v[146:161], a[132:135], v[194:197], v[146:161]\n"
    "ds_read_b64_tr_b16 v[208:209], v250 offset:62720\n"
    "v_mul_f32 v192, v192, v226\n"
    "v_mul_f32 v193, v193, v227\n"
    "v_cvt_pk_bf16_f32 v169, v192, v193\n"
    "s_waitcnt lgkmcnt(4)\n"
    "v_mfma_f32_32x32x16_bf16 v[130:145], a[136:139], v[198:201], v[130:145]\n"
    "ds_read_b64_tr_b16 v[194:195], v250 offset:63488\n"
    "buffer_store_dwordx4 v[166:169], v233, s[52:55], s19 offen offset:1024 sc1\n"
    "s_waitcnt vmcnt(15)\n"
    "v_max3_u32 v234, v102, v103, v104\n"
    "s_waitcnt vmcnt(14)\n"
    "v_max3_u32 v234, v234, v105, v106\n"
    "v_max3_u32 v234, v234, v107, v108\n"
    "s_lshl1_add_u32 s24, s32, s25\n"
    "buffer_load_dwordx4 a[252:255], v233, s[52:55], s24 offen offset:0 sc1\n"
    "v_mfma_f32_32x32x16_bf16 v[146:161], a[140:143], v[198:201], v[146:161]\n"
    "ds_read_b64_tr_b16 v[196:197], v250 offset:63744\n"
    "buffer_load_dwordx4 v[252:255], v1, s[52:55], s36 offen sc1\n"
    "s_waitcnt vmcnt(15)\n"
    "v_max3_u32 v234, v234, v109, v110\n"
    "v_max3_u32 v234, v234, v111, v112\n"
    "s_waitcnt vmcnt(14)\n"
    "v_max3_u32 v234, v234, v113, v114\n"
    "s_waitcnt lgkmcnt(4)\n"
    "v_mfma_f32_32x32x16_bf16 v[130:145], a[144:147], v[202:205], v[130:145]\n"
    "ds_read_b64_tr_b16 v[198:199], v251 offset:57344\n"
    "v_max3_u32 v234, v234, v115, v116\n"
    "s_waitcnt vmcnt(13)\n"
    "v_max3_u32 v234, v234, v117, v118\n"
    "v_max3_u32 v234, v234, v119, v120\n"
    "s_waitcnt vmcnt(12)\n"
    "v_max3_u32 v234, v234, v121, v122\n"
    "v_max3_u32 v234, v234, v123, v124\n"
    "v_mfma_f32_32x32x16_bf16 v[146:161], a[148:151], v[202:205], v[146:161]\n"
    "ds_read_b64_tr_b16 v[200:201], v251 offset:57600\n"
    "s_waitcnt vmcnt(11)\n"
    "v_max3_u32 v234, v234, v125, v126\n"
    "v_max3_u32 v234, v234, v127, v128\n"
    "v_max_u32 v234, v234, v129\n"
    "v_cmp_eq_u32 vcc, -1, v234\n"
    "s_cbranch_vccnz Lslow_tile3_%=\n"
    "Lback_tile3_%=:\n"
    "s_lshl1_add_u32 s24, s33, s25\n"
    "buffer_load_dwordx4 v233, s[52:55], s24 offen offset:0 sc1 lds\n"
    "s_waitcnt lgkmcnt(4)\n"
    "v_mfma_f32_32x32x16_bf16 v[130:145], a[152:155], v[206:209], v[130:145]\n"
    "ds_read_b64_tr_b16 v[202:203], v251 offset:58368\n"
    "ds_write_b128 v223, v[102:105] offset:0\n"
    "v_mfma_f32_32x32x16_bf16 v[146:161], a[156:159], v[206:209], v[146:161]\n"
    "ds_read_b64_tr_b16 v[204:205], v251 offset:58624\n"
    "ds_write_b128 v223, v[106:109] offset:1024\n"
    "s_waitcnt lgkmcnt(6)\n"
    "v_mfma_f32_32x32x16_bf16 v[130:145], a[160:163], v[194:197], v[130:145]\n"
    "ds_read_b64_tr_b16 v[206:207], v251 offset:59392\n"
    "ds_write_b128 v223, v[110:113] offset:2048\n"
    "buffer_load_dwordx4 v233, s[52:55], s24 offen offset:1024 sc1 lds\n"
    "v_mfma_f32_32x32x16_bf16 v[146:161], a[164:167], v[194:197], v[146:161]\n"
    "ds_read_b64_tr_b16 v[208:209], v251 offset:59648\n"
    "ds_write_b128 v223, v[114:117] offset:3072\n"
    "s_waitcnt lgkmcnt(8)\n"
    "v_mfma_f32_32x32x16_bf16 v[130:145], a[168:171], v[198:201], v[130:145]\n"
    "ds_read_b64_tr_b16 v[194:195], v251 offset:60416\n"
    "ds_write_b128 v223, v[118:121] offset:4096\n"
    "v_mfma_f32_32x32x16_bf16 v[146:161], a[172:175], v[198:201], v[146:161]\n"
    "ds_read_b64_tr_b16 v[196:197], v251 offset:60672\n"
    "ds_write_b128 v223, v[122:125] offset:5120\n"
    "buffer_load_dwordx4 v233, s[52:55], s24 offen offset:2048 sc1 lds\n"
    "s_waitcnt lgkmcnt(9)\n"
    "v_mfma_f32_32x32x16_bf16 v[130:145], a[176:179], v[202:205], v[130:145]\n"
    "ds_read_b64_tr_b16 v[198:199], v251 offset:61440\n"
    "ds_write_b128 v223, v[126:129] offset:6144\n"
    "v_mfma_f32_32x32x16_bf16 v[146:161], a[180:183], v[202:205], v[146:161]\n"
    "ds_read_b64_tr_b16 v[200:201], v251 offset:61696\n"
    "buffer_load_dwordx4 v233, s[52:55], s24 offen offset:3072 sc1 lds\n"
    "s_waitcnt lgkmcnt(8)\n"
    "v_mfma_f32_32x32x16_bf16 v[130:145], a[184:187], v[206:209], v[130:145]\n"
    "ds_read_b64_tr_b16 v[202:203], v251 offset:62464\n"
    "v_mfma_f32_32x32x16_bf16 v[146:161], a[188:191], v[206:209], v[146:161]\n"
    "ds_read_b64_tr_b16 v[204:205], v251 offset:62720\n"
    "s_waitcnt lgkmcnt(6)\n"
    "v_mfma_f32_32x32x16_bf16 v[130:145], a[192:195], v[194:197], v[130:145]\n"
    "ds_read_b64_tr_b16 v[206:207], v251 offset:63488\n"
    "s_add_u32 m0, m0, 4096\n"
    "s_nop 0\n"
    "buffer_load_dwordx4 v241, s[52:55], s24 offen offset:0 sc1 lds\n"
    "v_mfma_f32_32x32x16_bf16 v[146:161], a[196:199], v[194:197], v[146:161]\n"
    "ds_read_b64_tr_b16 v[208:209], v251 offset:63744\n"
    "s_waitcnt lgkmcnt(5)\n"
    "s_barrier\n"
    "s_waitcnt lgkmcnt(4)\n"
    "v_mfma_f32_32x32x16_bf16 v[130:145], a[200:203], v[198:201], v[130:145]\n"
    "ds_read_b64_tr_b16 v[194:195], v248 offset:0\n"
    "v_mfma_f32_32x32x16_bf16 v[146:161], a[204:207], v[198:201], v[146:161]\n"
    "ds_read_b64_tr_b16 v[196:197], v248 offset:256\n"
    "buffer_load_dwordx4 v241, s[52:55], s24 offen offset:1024 sc1 lds\n"
    "s_waitcnt lgkmcnt(4)\n"
    "v_mfma_f32_32x32x16_bf16 v[130:145], a[208:211], v[202:205], v[130:145]\n"
    "ds_read_b64_tr_b16 v[198:199], v248 offset:1024\n"
    "s_waitcnt vmcnt(6)\n"
    "v_xor_b32 v243, s60, v253\n"
    "v_xor_b32 v228, s60, v255\n"
    "v_or_b32 v243, v243, v228\n"
    "v_cmp_ne_u32_e64 vcc, 0, v243\n"
    "s_cbranch_vccnz Lslow_st2_%=\n"
    "Lback_st2_%=:\n"
    "v_mfma_f32_32x32x16_bf16 v[146:161], a[212:215], v[202:205], v[146:161]\n"
    "ds_read_b64_tr_b16 v[200:201], v248 offset:1280\n"
    "v_cvt_pk_bf16_f32 v218, v252, v252\n"
    "v_rsq_f32 v243, v254\n"
    "v_lshlrev_b32 v253, 16, v218\n"
    "v_mov_b32 v228, v254\n"
    "s_waitcnt lgkmcnt(4)\n"
    "v_mfma_f32_32x32x16_bf16 v[130:145], a[216:219], v[206:209], v[130:145]\n"
    "ds_read_b64_tr_b16 v[202:203], v248 offset:2048\n"
    "buffer_load_dwordx4 v241, s[52:55], s24 offen offset:2048 sc1 lds\n"
    "v_sub_f32_e64 v253, v252, v253\n"
    "v_mul_f32 v243, 0xbfb8aa3b, v243\n"
    "v_cvt_pk_bf16_f32 v219, v253, 0\n"
    "s_add_u32 s36, s36, 8192\n"
    "v_mfma_f32_32x32x16_bf16 v[146:161], a[220:223], v[206:209], v[146:161]\n"
    "ds_read_b64_tr_b16 v[204:205], v248 offset:2304\n"
    "s_mov_b64 exec, 1\n"
    "buffer_store_dword v231, off, s[52:55], s35 offset:0 sc1\n"
    "s_mov_b64 exec, -1\n"
    "s_add_u32 s35, s35, 4096\n"
    "v_mfma_f32_32x32x16_bf16 v[130:145], v[210:213], v[218:221], v[130:145]\n"
    "v_min_u32 v232, 13, v222\n"
    "s_mul_i32 s67, s57, 56\n"
    "s_lshl_b32 s64, s11, 8\n"
    "s_add_u32 s67, s67, s64\n"
    "s_add_u32 s67, s67, 12288\n"
    "v_lshlrev_b32_e64 v232, 2, v232\n"
    "v_mfma_f32_32x32x16_bf16 v[146:161], v[214:217], v[218:221], v[146:161]\n"
    "buffer_load_dwordx4 v241, s[52:55], s24 offen offset:3072 sc1 lds\n"
    "s_mul_i32 s38, s11, 114688\n"
    "s_mul_i32 s65, s57, 28672\n"
    "s_add_u32 s38, s38, s65\n"
    "s_add_u32 s38, s38, 10448896\n"
    "s_waitcnt lgkmcnt(4)\n"
    "v_mfma_f32_32x32x16_bf16 v[162:177], a[0:3], v[194:197], 0\n"
    "ds_read_b64_tr_b16 v[206:207], v248 offset:3072\n"
    "s_add_u32 s19, s19, 1835008\n"
    "v_mfma_f32_32x32x16_bf16 v[178:193], a[4:7], v[194:197], 0\n"
    "ds_read_b64_tr_b16 v[208:209], v248 offset:3328\n"
    "s_mul_i32 s39, s11, 7\n"
    "s_add_u32 s39, s39, s10\n"
    "s_lshl_b32 s39, s39, 12\n"
    "s_add_u32 s39, s39, 8613888\n"
    "buffer_load_dwordx4 v[102:105], v230, s[52:55], s39 offen sc1\n"
    "s_waitcnt lgkmcnt(4)\n"
    "v_mfma_f32_32x32x16_bf16 v[162:177], a[8:11], v[198:201], v[162:177]\n"
    "ds_read_b64_tr_b16 v[194:195], v248 offset:4096\n"
    "v_mul_f32 v252, v130, v243\n"
    "v_mul_f32 v253, v131, v243\n"
    "v_exp_f32_e64 v252, v252\n"
    "v_mfma_f32_32x32x16_bf16 v[178:193], a[12:15], v[198:201], v[178:193]\n"
    "ds_read_b64_tr_b16 v[196:197], v248 offset:4352\n"
    "s_mul_i32 s39, s12, 7\n"
    "s_add_u32 s39, s39, s10\n"
    "s_lshl_b32 s39, s39, 12\n"
    "s_add_u32 s39, s39, 8613888\n"
    "buffer_load_dwordx4 v[106:109], v230, s[52:55], s39 offen sc1\n"
    "s_waitcnt lgkmcnt(4)\n"
    "v_mfma_f32_32x32x16_bf16 v[162:177], a[16:19], v[202:205], v[162:177]\n"
    "ds_read_b64_tr_b16 v[198:199], v248 offset:5120\n"
    "v_exp_f32 v253, v253\n"
    "v_mul_f32 v146, v130, v146\n"
    "v_mul_f32 v147, v131, v147\n"
    "v_mul_f32 v254, v132, v243\n"
    "v_mfma_f32_32x32x16_bf16 v[178:193], a[20:23], v[202:205], v[178:193]\n"
    "ds_read_b64_tr_b16 v[200:201], v248 offset:5376\n"
    "v_mul_f32_e64 v255, v133, v243\n"
    "v_fma_f32 v252, v252, v228, v228\n"
    "v_fma_f32 v253, v253, v228, v228\n"
    "v_rcp_f32 v252, v252\n"
    "s_lshl1_add_u32 s24, s32, s25\n"
    "buffer_load_dwordx4 a[0:3], v233, s[52:55], s24 offen offset:1024 sc1\n"
    "s_waitcnt lgkmcnt(4)\n"
    "v_mfma_f32_32x32x16_bf16 v[162:177], a[24:27], v[206:209], v[162:177]\n"
    "ds_read_b64_tr_b16 v[202:203], v248 offset:6144\n"
    "v_rcp_f32 v253, v253\n"
    "v_exp_f32_e64 v254, v254\n"
    "v_mfma_f32_32x32x16_bf16 v[178:193], a[28:31], v[206:209], v[178:193]\n"
    "ds_read_b64_tr_b16 v[204:205], v248 offset:6400\n"
    "buffer_load_dwordx4 a[4:7], v233, s[52:55], s24 offen offset:2048 sc1\n"
    "v_exp_f32 v255, v255\n"
    "v_mul_f32 v148, v132, v148\n"
    "v_mul_f32 v149, v133, v149\n"
    "v_mul_f32_e64 v224, v134, v243\n"
    "s_waitcnt lgkmcnt(4)\n"
    "v_mfma_f32_32x32x16_bf16 v[162:177], a[32:35], v[194:197], v[162:177]\n"
    "ds_read_b64_tr_b16 v[206:207], v249 offset:0\n"
    "v_mul_f32 v225, v135, v243\n"
    "v_mul_f32 v146, v146, v252\n"
    "v_mul_f32_e64 v147, v147, v253\n"
    "v_cvt_pk_bf16_f32 v130, v146, v147\n"
    "v_fma_f32 v254, v254, v228, v228\n"
    "v_mfma_f32_32x32x16_bf16 v[178:193], a[36:39], v[194:197], v[178:193]\n"
    "ds_read_b64_tr_b16 v[208:209], v249 offset:256\n"
    "buffer_load_dwordx4 a[8:11], v233, s[52:55], s24 offen offset:3072 sc1\n"
    "v_fma_f32 v255, v255, v228, v228\n"
    "v_rcp_f32 v254, v254\n"
    "v_rcp_f32_e64 v255, v255\n"
    "s_waitcnt lgkmcnt(4)\n"
    "v_mfma_f32_32x32x16_bf16 v[162:177], a[40:43], v[198:201], v[162:177]\n"
    "ds_read_b64_tr_b16 v[194:195], v249 offset:1024\n"
    "v_exp_f32 v224, v224\n"
    "v_exp_f32 v225, v225\n"
    "v_mul_f32_e64 v150, v134, v150\n"
    "v_mfma_f32_32x32x16_bf16 v[178:193], a[44:47], v[198:201], v[178:193]\n"
    "ds_read_b64_tr_b16 v[196:197], v249 offset:1280\n"
    "buffer_load_dwordx4 a[12:15], v241, s[52:55], s24 offen offset:0 sc1\n"
    "v_mul_f32 v151, v135, v151\n"
    "v_mul_f32 v226, v136, v243\n"
    "v_mul_f32 v227, v137, v243\n"
    "v_mul_f32 v148, v148, v254\n"
    "v_mul_f32 v149, v149, v255\n"
    "s_waitcnt lgkmcnt(4)\n"
    "v_mfma_f32_32x32x16_bf16 v[162:177], a[48:51], v[202:205], v[162:177]\n"
    "ds_read_b64_tr_b16 v[198:199], v249 offset:2048\n"
    "v_cvt_pk_bf16_f32 v131, v148, v149\n"
    "v_fma_f32 v224, v224, v228, v228\n"
    "v_fma_f32 v225, v225, v228, v228\n"
    "v_rcp_f32_e64 v224, v224\n"
    "v_mfma_f32_32x32x16_bf16 v[178:193], a[52:55], v[202:205], v[178:193]\n"
    "ds_read_b64_tr_b16 v[200:201], v249 offset:2304\n"
    "buffer_load_dwordx4 a[16:19], v241, s[52:55], s24 offen offset:1024 sc1\n"
    "v_rcp_f32 v225, v225\n"
    "v_exp_f32_e64 v226, v226\n"
    "s_waitcnt lgkmcnt(4)\n"
    "v_mfma_f32_32x32x16_bf16 v[162:177], a[56:59], v[206:209], v[162:177]\n"
    "ds_read_b64_tr_b16 v[202:203], v249 offset:3072\n"
    "v_exp_f32 v227, v227\n"
    "v_mul_f32 v152, v136, v152\n"
    "v_mul_f32 v153, v137, v153\n"
    "v_mul_f32 v252, v138, v243\n"
    "v_mfma_f32_32x32x16_bf16 v[178:193], a[60:63], v[206:209], v[178:193]\n"
    "ds_read_b64_tr_b16 v[204:205], v249 offset:3328\n"
    "buffer_load_dwordx4 a[20:23], v241, s[52:55], s24 offen offset:2048 sc1\n"
    "v_mul_f32 v253, v139, v243\n"
    "v_mul_f32 v150, v150, v224\n"
    "v_mul_f32_e64 v151, v151, v225\n"
    "v_cvt_pk_bf16_f32 v132, v150, v151\n"
    "v_fma_f32 v226, v226, v228, v228\n"
    "s_waitcnt lgkmcnt(4)\n"
    "v_mfma_f32_32x32x16_bf16 v[162:177], a[64:67], v[194:197], v[162:177]\n"
    "ds_read_b64_tr_b16 v[206:207], v249 offset:4096\n"
    "v_fma_f32 v227, v227, v228, v228\n"
    "v_rcp_f32 v226, v226\n"
    "v_rcp_f32_e64 v227, v227\n"
    "v_mfma_f32_32x32x16_bf16 v[178:193], a[68:71], v[194:197], v[178:193]\n"
    "ds_read_b64_tr_b16 v[208:209], v249 offset:4352\n"
    "buffer_load_dwordx4 a[24:27], v241, s[52:55], s24 offen offset:3072 sc1\n"
    "v_exp_f32 v252, v252\n"
    "v_exp_f32 v253, v253\n"
    "v_mul_f32 v154, v138, v154\n"
    "s_waitcnt lgkmcnt(4)\n"
    "v_mfma_f32_32x32x16_bf16 v[162:177], a[72:75], v[198:201], v[162:177]\n"
    "ds_read_b64_tr_b16 v[194:195], v249 offset:5120\n"
    "v_mul_f32 v155, v139, v155\n"
    "v_mul_f32 v254, v140, v243\n"
    "v_mul_f32 v255, v141, v243\n"
    "v_mul_f32 v152, v152, v226\n"
    "v_mul_f32_e64 v153, v153, v227\n"
    "v_mfma_f32_32x32x16_bf16 v[178:193], a[76:79], v[198:201], v[178:193]\n"
    "ds_read_b64_tr_b16 v[196:197], v249 offset:5376\n"
    "v_cvt_pk_bf16_f32 v133, v152, v153\n"
    "v_fma_f32 v252, v252, v228, v228\n"
    "v_fma_f32 v253, v253, v228, v228\n"
    "v_rcp_f32 v252, v252\n"
    "s_lshl1_add_u32 s24, s34, s25\n"
    "buffer_load_dwordx4 a[28:31], v233, s[52:55], s24 offen offset:0 sc1\n"
    "s_waitcnt lgkmcnt(4)\n"
    "v_mfma_f32_32x32x16_bf16 v[162:177], a[80:83], v[202:205], v[162:177]\n"
    "ds_read_b64_tr_b16 v[198:199], v249 offset:6144\n"
    "buffer_store_dwordx4 v[130:133], v233, s[52:55], s19 offen offset:0 sc1\n"
    "v_rcp_f32 v253, v253\n"
    "v_mfma_f32_32x32x16_bf16 v[178:193], a[84:87], v[202:205], v[178:193]\n"
    "ds_read_b64_tr_b16 v[200:201], v249 offset:6400\n"
    "buffer_load_dwordx4 a[32:35], v233, s[52:55], s24 offen offset:1024 sc1\n"
    "v_exp_f32 v254, v254\n"
    "v_exp_f32 v255, v255\n"
    "v_mul_f32 v156, v140, v156\n"
    "s_waitcnt lgkmcnt(4)\n"
    "v_mfma_f32_32x32x16_bf16 v[162:177], a[88:91], v[206:209], v[162:177]\n"
    "ds_read_b64_tr_b16 v[202:203], v250 offset:0\n"
    "v_mul_f32 v157, v141, v157\n"
    "v_mul_f32 v224, v142, v243\n"
    "v_mul_f32 v225, v143, v243\n"
    "v_mul_f32 v154, v154, v252\n"
    "v_mul_f32_e64 v155, v155, v253\n"
    "v_mfma_f32_32x32x16_bf16 v[178:193], a[92:95], v[206:209], v[178:193]\n"
    "ds_read_b64_tr_b16 v[204:205], v250 offset:256\n"
    "buffer_load_dword v240, v232, s[52:55], s67 offen sc1\n"
    "v_cvt_pk_bf16_f32 v134, v154, v155\n"
    "v_fma_f32 v254, v254, v228, v228\n"
    "v_fma_f32 v255, v255, v228, v228\n"
    "v_rcp_f32 v254, v254\n"
    "s_waitcnt lgkmcnt(4)\n"
    "v_mfma_f32_32x32x16_bf16 v[162:177], a[96:99], v[194:197], v[162:177]\n"
    "ds_read_b64_tr_b16 v[206:207], v250 offset:1024\n"
    "buffer_load_dwordx4 a[36:39], v233, s[52:55], s24 offen offset:2048 sc1\n"
    "v_rcp_f32 v255, v255\n"
    "v_exp_f32 v224, v224\n"
    "v_mfma_f32_32x32x16_bf16 v[178:193], a[100:103], v[194:197], v[178:193]\n"
    "ds_read_b64_tr_b16 v[208:209], v250 offset:1280\n"
    "v_exp_f32 v225, v225\n"
    "v_mul_f32 v158, v142, v158\n"
    "v_mul_f32 v159, v143, v159\n"
    "v_mul_f32_e64 v226, v144, v243\n"
    "s_waitcnt lgkmcnt(4)\n"
    "v_mfma_f32_32x32x16_bf16 v[162:177], a[104:107], v[198:201], v[162:177]\n"
    "ds_read_b64_tr_b16 v[194:195], v250 offset:2048\n"
    "buffer_load_dwordx4 a[40:43], v233, s[52:55], s24 offen offset:3072 sc1\n"
    "v_mul_f32 v227, v145, v243\n"
    "v_mul_f32 v156, v156, v254\n"
    "v_mul_f32_e64 v157, v157, v255\n"
    "v_cvt_pk_bf16_f32 v135, v156, v157\n"
    "v_fma_f32 v224, v224, v228, v228\n"
    "v_mfma_f32_32x32x16_bf16 v[178:193], a[108:111], v[198:201], v[178:193]\n"
    "ds_read_b64_tr_b16 v[196:197], v250 offset:2304\n"
    "v_fma_f32 v225, v225, v228, v228\n"
    "v_rcp_f32 v224, v224\n"
    "v_rcp_f32_e64 v225, v225\n"
    "s_waitcnt lgkmcnt(4)\n"
    "v_mfma_f32_32x32x16_bf16 v[162:177], a[112:115], v[202:205], v[162:177]\n"
    "ds_read_b64_tr_b16 v[198:199], v250 offset:3072\n"
    "buffer_load_dwordx4 a[44:47], v241, s[52:55], s24 offen offset:0 sc1\n"
    "v_exp_f32 v226, v226\n"
    "v_exp_f32 v227, v227\n"
    "v_mul_f32_e64 v160, v144, v160\n"
    "v_mfma_f32_32x32x16_bf16 v[178:193], a[116:119], v[202:205], v[178:193]\n"
    "ds_read_b64_tr_b16 v[200:201], v250 offset:3328\n"
    "v_mul_f32 v161, v145, v161\n"
    "v_mul_f32 v158, v158, v224\n"
    "v_mul_f32_e64 v159, v159, v225\n"
    "v_cvt_pk_bf16_f32 v136, v158, v159\n"
    "v_fma_f32 v226, v226, v228, v228\n"
    "s_waitcnt lgkmcnt(4)\n"
    "v_mfma_f32_32x32x16_bf16 v[162:177], a[120:123], v[206:209], v[162:177]\n"
    "ds_read_b64_tr_b16 v[202:203], v250 offset:4096\n"
    "buffer_load_dwordx4 a[48:51], v241, s[52:55], s24 offen offset:1024 sc1\n"
    "v_fma_f32 v227, v227, v228, v228\n"
    "v_rcp_f32 v226, v226\n"
    "v_rcp_f32_e64 v227, v227\n"
    "v_mfma_f32_32x32x16_bf16 v[178:193], a[124:127], v[206:209], v[178:193]\n"
    "ds_read_b64_tr_b16 v[204:205], v250 offset:4352\n"
    "v_mul_f32 v160, v160, v226\n"
    "v_mul_f32 v161, v161, v227\n"
    "v_cvt_pk_bf16_f32 v137, v160, v161\n"
    "s_waitcnt lgkmcnt(4)\n"
    "v_mfma_f32_32x32x16_bf16 v[162:177], a[128:131], v[194:197], v[162:177]\n"
    "ds_read_b64_tr_b16 v[206:207], v250 offset:5120\n"
    "buffer_store_dwordx4 v[134:137], v233, s[52:55], s19 offen offset:1024 sc1\n"
    "buffer_load_dwordx4 a[52:55], v241, s[52:55], s24 offen offset:2048 sc1\n"
    "v_mfma_f32_32x32x16_bf16 v[178:193], a[132:135], v[194:197], v[178:193]\n"
    "ds_read_b64_tr_b16 v[208:209], v250 offset:5376\n"
    "buffer_load_dwordx4 v[252:255], v1, s[52:55], s36 offen sc1\n"
    "s_waitcnt lgkmcnt(4)\n"
    "v_mfma_f32_32x32x16_bf16 v[162:177], a[136:139], v[198:201], v[162:177]\n"
    "ds_read_b64_tr_b16 v[194:195], v250 offset:6144\n"
    "buffer_load_dwordx4 a[56:59], v241, s[52:55], s24 offen offset:3072 sc1\n"
    "s_waitcnt vmcnt(20)\n"
    "v_max3_u32 v234, v102, v103, v104\n"
    "s_waitcnt vmcnt(19)\n"
    "v_max3_u32 v234, v234, v105, v106\n"
    "v_max3_u32 v234, v234, v107, v108\n"
    "v_max_u32 v234, v234, v109\n"
    "v_cmp_eq_u32 vcc, -1, v234\n"
    "v_mfma_f32_32x32x16_bf16 v[178:193], a[140:143], v[198:201], v[178:193]\n"
    "ds_read_b64_tr_b16 v[196:197], v250 offset:6400\n"
    "s_cbranch_vccnz Lslow_res_%=\n"
    "Lback_res_%=:\n"
    "v_add_u32 v234, 153024, v230\n"
    "s_waitcnt vmcnt(8)\n"
    "v_cmp_ne_u32_e64 vcc, s60, v240\n"
    "s_cbranch_vccnz Lslow_fh_%=\n"
    "Lback_fh_%=:\n"
    "s_waitcnt lgkmcnt(4)\n"
    "v_mfma_f32_32x32x16_bf16 v[162:177], a[144:147], v[202:205], v[162:177]\n"
    "ds_read_b64_tr_b16 v[198:199], v251 offset:0\n"
    "ds_write_b128 v234, v[102:105]\n"
    "s_add_u32 s37, s38, s28\n"
    "buffer_load_dwordx4 a[60:63], v233, s[52:55], s37 offen offset:0 sc1\n"
    "v_mfma_f32_32x32x16_bf16 v[178:193], a[148:151], v[202:205], v[178:193]\n"
    "ds_read_b64_tr_b16 v[200:201], v251 offset:256\n"
    "ds_write_b128 v234, v[106:109] offset:4096\n"
    "s_waitcnt lgkmcnt(6)\n"
    "v_mfma_f32_32x32x16_bf16 v[162:177], a[152:155], v[206:209], v[162:177]\n"
    "ds_read_b64_tr_b16 v[202:203], v251 offset:1024\n"
    "buffer_load_dwordx4 a[64:67], v233, s[52:55], s37 offen offset:1024 sc1\n"
    "v_mfma_f32_32x32x16_bf16 v[178:193], a[156:159], v[206:209], v[178:193]\n"
    "ds_read_b64_tr_b16 v[204:205], v251 offset:1280\n"
    "s_waitcnt lgkmcnt(6)\n"
    "v_mfma_f32_32x32x16_bf16 v[162:177], a[160:163], v[194:197], v[162:177]\n"
    "ds_read_b64_tr_b16 v[206:207], v251 offset:2048\n"
    "buffer_load_dwordx4 a[68:71], v233, s[52:55], s37 offen offset:2048 sc1\n"
    "v_mfma_f32_32x32x16_bf16 v[178:193], a[164:167], v[194:197], v[178:193]\n"
    "ds_read_b64_tr_b16 v[208:209], v251 offset:2304\n"
    "s_waitcnt lgkmcnt(5)\n"
    "v_mfma_f32_32x32x16_bf16 v[162:177], a[168:171], v[198:201], v[162:177]\n"
    "ds_read_b64_tr_b16 v[194:195], v251 offset:3072\n"
    "buffer_load_dwordx4 a[72:75], v233, s[52:55], s37 offen offset:3072 sc1\n"
    "v_mfma_f32_32x32x16_bf16 v[178:193], a[172:175], v[198:201], v[178:193]\n"
    "ds_read_b64_tr_b16 v[196:197], v251 offset:3328\n"
    "s_waitcnt lgkmcnt(4)\n"
    "v_mfma_f32_32x32x16_bf16 v[162:177], a[176:179], v[202:205], v[162:177]\n"
    "ds_read_b64_tr_b16 v[198:199], v251 offset:4096\n"
    "s_add_u32 s37, s38, s29\n"
    "buffer_load_dwordx4 a[76:79], v233, s[52:55], s37 offen offset:0 sc1\n"
    "v_mfma_f32_32x32x16_bf16 v[178:193], a[180:183], v[202:205], v[178:193]\n"
    "ds_read_b64_tr_b16 v[200:201], v251 offset:4352\n"
    "s_waitcnt lgkmcnt(4)\n"
    "v_mfma_f32_32x32x16_bf16 v[162:177], a[184:187], v[206:209], v[162:177]\n"
    "ds_read_b64_tr_b16 v[202:203], v251 offset:5120\n"
    "buffer_load_dwordx4 a[80:83], v233, s[52:55], s37 offen offset:1024 sc1\n"
    "v_mfma_f32_32x32x16_bf16 v[178:193], a[188:191], v[206:209], v[178:193]\n"
    "ds_read_b64_tr_b16 v[204:205], v251 offset:5376\n"
    "s_waitcnt lgkmcnt(4)\n"
    "v_mfma_f32_32x32x16_bf16 v[162:177], a[192:195], v[194:197], v[162:177]\n"
    "ds_read_b64_tr_b16 v[206:207], v251 offset:6144\n"
    "buffer_load_dwordx4 a[84:87], v233, s[52:55], s37 offen offset:2048 sc1\n"
    "v_mfma_f32_32x32x16_bf16 v[178:193], a[196:199], v[194:197], v[178:193]\n"
    "ds_read_b64_tr_b16 v[208:209], v251 offset:6400\n"
    "s_barrier\n"
    "s_waitcnt lgkmcnt(4)\n"
    "v_mfma_f32_32x32x16_bf16 v[162:177], a[200:203], v[198:201], v[162:177]\n"
    "buffer_load_dwordx4 a[88:91], v233, s[52:55], s37 offen offset:3072 sc1\n"
    "s_lshl_b32 s64, s57, 13\n"
    "v_add_u32 v232, s64, v233\n"
    "v_mfma_f32_32x32x16_bf16 v[178:193], a[204:207], v[198:201], v[178:193]\n"
    "ds_read_b128 a[172:175], v232 offset:0\n"
    "s_waitcnt lgkmcnt(3)\n"
    "v_mfma_f32_32x32x16_bf16 v[162:177], a[208:211], v[202:205], v[162:177]\n"
    "s_waitcnt vmcnt(9)\n"
    "v_xor_b32 v244, s60, v253\n"
    "v_xor_b32 v229, s60, v255\n"
    "v_or_b32 v244, v244, v229\n"
    "v_cmp_ne_u32_e64 vcc, 0, v244\n"
    "s_cbranch_vccnz Lslow_st3_%=\n"
    "Lback_st3_%=:\n"
    "v_cvt_pk_bf16_f32 v218, v252, v252\n"
    "s_add_u32 s37, s38, s30\n"
    "buffer_load_dwordx4 a[92:95], v233, s[52:55], s37 offen offset:0 sc1\n"
    "ds_read_b128 a[176:179], v232 offset:1024\n"
    "v_mfma_f32_32x32x16_bf16 v[178:193], a[212:215], v[202:205], v[178:193]\n"
    "ds_read_b128 a[180:183], v232 offset:2048\n"
    "v_rsq_f32 v244, v254\n"
    "v_lshlrev_b32 v253, 16, v218\n"
    "v_mov_b32 v229, v254\n"
    "v_sub_f32_e64 v253, v252, v253\n"
    "v_mul_f32 v244, 0xbfb8aa3b, v244\n"
    "s_waitcnt lgkmcnt(3)\n"
    "v_mfma_f32_32x32x16_bf16 v[162:177], a[216:219], v[206:209], v[162:177]\n"
    "buffer_load_dwordx4 a[96:99], v233, s[52:55], s37 offen offset:1024 sc1\n"
    "ds_read_b128 a[184:187], v232 offset:3072\n"
    "v_cvt_pk_bf16_f32 v219, v253, 0\n"
    "s_add_u32 s36, s36, 8192\n"
    "v_mfma_f32_32x32x16_bf16 v[178:193], a[220:223], v[206:209], v[178:193]\n"
    "ds_read_b128 a[188:191], v232 offset:4096\n"
    "v_mfma_f32_32x32x16_bf16 v[162:177], v[210:213], v[218:221], v[162:177]\n"
    "buffer_load_dwordx4 a[100:103], v233, s[52:55], s37 offen offset:2048 sc1\n"
    "ds_read_b128 a[192:195], v232 offset:5120\n"
    "v_mfma_f32_32x32x16_bf16 v[178:193], v[214:217], v[218:221], v[178:193]\n"
    "ds_read_b128 a[196:199], v232 offset:6144\n"
    "ds_read_b128 a[200:203], v232 offset:7168\n"
    "s_add_u32 s19, s19, 1835008\n"
    "s_nop 5\n"
    "v_mul_f32 v252, v162, v244\n"
    "v_mul_f32 v253, v163, v244\n"
    "v_exp_f32 v252, v252\n"
    "v_exp_f32 v253, v253\n"
    "buffer_load_dwordx4 a[104:107], v233, s[52:55], s37 offen offset:3072 sc1\n"
    "v_mul_f32 v178, v162, v178\n"
    "v_mul_f32 v179, v163, v179\n"
    "v_mul_f32 v254, v164, v244\n"
    "v_mul_f32 v255, v165, v244\n"
    "v_fma_f32 v252, v252, v229, v229\n"
    "v_fma_f32 v253, v253, v229, v229\n"
    "v_rcp_f32 v252, v252\n"
    "v_rcp_f32 v253, v253\n"
    "v_exp_f32 v254, v254\n"
    "v_exp_f32 v255, v255\n"
    "v_mul_f32 v180, v164, v180\n"
    "v_mul_f32 v181, v165, v181\n"
    "v_mul_f32 v224, v166, v244\n"
    "v_mul_f32_e64 v225, v167, v244\n"
    "s_add_u32 s37, s38, s31\n"
    "buffer_load_dwordx4 a[108:111], v233, s[52:55], s37 offen offset:0 sc1\n"
    "v_mul_f32 v178, v178, v252\n"
    "v_mul_f32 v179, v179, v253\n"
    "v_cvt_pk_bf16_f32 v162, v178, v179\n"
    "v_fma_f32 v254, v254, v229, v229\n"
    "v_fma_f32 v255, v255, v229, v229\n"
    "v_rcp_f32 v254, v254\n"
    "v_rcp_f32 v255, v255\n"
    "v_exp_f32 v224, v224\n"
    "v_exp_f32 v225, v225\n"
    "v_mul_f32 v182, v166, v182\n"
    "v_mul_f32 v183, v167, v183\n"
    "v_mul_f32 v226, v168, v244\n"
    "v_mul_f32 v227, v169, v244\n"
    "v_mul_f32_e64 v180, v180, v254\n"
    "buffer_load_dwordx4 a[112:115], v233, s[52:55], s37 offen offset:1024 sc1\n"
    "v_mul_f32_e64 v181, v181, v255\n"
    "v_cvt_pk_bf16_f32 v163, v180, v181\n"
    "v_fma_f32 v224, v224, v229, v229\n"
    "v_fma_f32 v225, v225, v229, v229\n"
    "v_rcp_f32 v224, v224\n"
    "v_rcp_f32 v225, v225\n"
    "v_exp_f32 v226, v226\n"
    "v_exp_f32 v227, v227\n"
    "v_mul_f32 v184, v168, v184\n"
    "v_mul_f32 v185, v169, v185\n"
    "v_mul_f32 v252, v170, v244\n"
    "v_mul_f32 v253, v171, v244\n"
    "v_mul_f32 v182, v182, v224\n"
    "v_mul_f32 v183, v183, v225\n"
    "buffer_load_dwordx4 a[116:119], v233, s[52:55], s37 offen offset:2048 sc1\n"
    "v_cvt_pk_bf16_f32 v164, v182, v183\n"
    "v_fma_f32 v226, v226, v229, v229\n"
    "v_fma_f32 v227, v227, v229, v229\n"
    "v_rcp_f32 v226, v226\n"
    "v_rcp_f32 v227, v227\n"
    "v_exp_f32 v252, v252\n"
    "v_exp_f32 v253, v253\n"
    "v_mul_f32 v186, v170, v186\n"
    "v_mul_f32 v187, v171, v187\n"
    "v_mul_f32 v254, v172, v244\n"
    "v_mul_f32 v255, v173, v244\n"
    "v_mul_f32 v184, v184, v226\n"
    "v_mul_f32 v185, v185, v227\n"
    "v_cvt_pk_bf16_f32 v165, v184, v185\n"
    "buffer_load_dwordx4 a[120:123], v233, s[52:55], s37 offen offset:3072 sc1\n"
    "buffer_store_dwordx4 v[162:165], v233, s[52:55], s19 offen offset:0 sc1\n"
    "v_fma_f32 v252, v252, v229, v229\n"
    "v_fma_f32 v253, v253, v229, v229\n"
    "v_rcp_f32 v252, v252\n"
    "v_rcp_f32 v253, v253\n"
    "v_exp_f32 v254, v254\n"
    "v_exp_f32 v255, v255\n"
    "v_mul_f32 v188, v172, v188\n"
    "v_mul_f32 v189, v173, v189\n"
    "v_mul_f32 v224, v174, v244\n"
    "v_mul_f32 v225, v175, v244\n"
    "v_mul_f32 v186, v186, v252\n"
    "v_mul_f32 v187, v187, v253\n"
    "v_cvt_pk_bf16_f32 v166, v186, v187\n"
    "v_fma_f32 v254, v254, v229, v229\n"
    "s_add_u32 s37, s38, s32\n"
    "buffer_load_dwordx4 a[124:127], v233, s[52:55], s37 offen offset:0 sc1\n"
    "v_fma_f32 v255, v255, v229, v229\n"
    "v_rcp_f32 v254, v254\n"
    "v_rcp_f32 v255, v255\n"
    "v_exp_f32 v224, v224\n"
    "v_exp_f32 v225, v225\n"
    "v_mul_f32 v190, v174, v190\n"
    "v_mul_f32 v191, v175, v191\n"
    "v_mul_f32 v226, v176, v244\n"
    "v_mul_f32 v227, v177, v244\n"
    "v_mul_f32 v188, v188, v254\n"
    "v_mul_f32_e64 v189, v189, v255\n"
    "v_cvt_pk_bf16_f32 v167, v188, v189\n"
    "v_fma_f32 v224, v224, v229, v229\n"
    "v_fma_f32 v225, v225, v229, v229\n"
    "buffer_load_dwordx4 a[128:131], v233, s[52:55], s37 offen offset:1024 sc1\n"
    "v_rcp_f32 v224, v224\n"
    "v_rcp_f32 v225, v225\n"
    "v_exp_f32 v226, v226\n"
    "v_exp_f32 v227, v227\n"
    "v_mul_f32 v192, v176, v192\n"
    "v_mul_f32 v193, v177, v193\n"
    "v_mul_f32 v190, v190, v224\n"
    "v_mul_f32 v191, v191, v225\n"
    "v_cvt_pk_bf16_f32 v168, v190, v191\n"
    "v_fma_f32 v226, v226, v229, v229\n"
    "v_fma_f32 v227, v227, v229, v229\n"
    "v_rcp_f32 v226, v226\n"
    "v_rcp_f32 v227, v227\n"
    "v_mul_f32_e64 v192, v192, v226\n"
    "buffer_load_dwordx4 a[132:135], v233, s[52:55], s37 offen offset:2048 sc1\n"
    "v_mul_f32_e64 v193, v193, v227\n"
    "v_cvt_pk_bf16_f32 v169, v192, v193\n"
    "buffer_store_dwordx4 v[166:169], v233, s[52:55], s19 offen offset:1024 sc1\n"
    "s_waitcnt vmcnt(20)\n"
    "v_mfma_f32_32x32x16_bf16 v[130:145], v[2:5], a[60:63], 0\n"
    "buffer_load_dwordx4 a[136:139], v233, s[52:55], s37 offen offset:3072 sc1\n"
    "s_lshl_b32 s64, s57, 13\n"
    "v_add_u32 v1, s64, v233\n"
    "s_lshl_b32 s64, s57, 10\n"
    "v_mfma_f32_32x32x16_bf16 v[146:161], v[6:9], a[60:63], 0\n"
    "v_add_u32_e64 v122, s64, v233\n"
    "v_lshl_add_u32 v224, v238, 2, v237\n"
    "v_lshl_add_u32 v224, v224, 6, v236\n"
    "s_waitcnt vmcnt(20)\n"
    "v_mfma_f32_32x32x16_bf16 v[130:145], v[10:13], a[64:67], v[130:145]\n"
    "s_lshl_b32 s64, s57, 9\n"
    "s_add_u32 s64, s64, 153024\n"
    "v_add_u32 v123, s64, v224\n"
    "s_add_u32 s37, s38, s33\n"
    "buffer_load_dwordx4 a[140:143], v233, s[52:55], s37 offen offset:0 sc1\n"
    "v_mfma_f32_32x32x16_bf16 v[146:161], v[14:17], a[64:67], v[146:161]\n"
    "v_and_b32_e64 v224, 31, v222\n"
    "v_mul_u32_u24 v224, 896, v224\n"
    "v_lshl_add_u32 v124, v238, 6, v224\n"
    "s_waitcnt vmcnt(20)\n"
    "v_mfma_f32_32x32x16_bf16 v[130:145], v[18:21], a[68:71], v[130:145]\n"
    "buffer_load_dwordx4 a[144:147], v233, s[52:55], s37 offen offset:1024 sc1\n"
    "v_min_u32 v125, 27, v222\n"
    "v_lshlrev_b32_e64 v125, 10, v125\n"
    "v_add_u32 v125, 1020, v125\n"
    "v_mfma_f32_32x32x16_bf16 v[146:161], v[22:25], a[68:71], v[146:161]\n"
    "s_mul_i32 s15, s12, 114688\n"
    "s_mul_i32 s65, s57, 28672\n"
    "s_add_u32 s15, s15, s65\n"
    "s_waitcnt vmcnt(20)\n"
    "v_mfma_f32_32x32x16_bf16 v[130:145], v[26:29], a[72:75], v[130:145]\n"
    "buffer_load_dwordx4 a[148:151], v233, s[52:55], s37 offen offset:2048 sc1\n"
    "s_add_u32 s15, s15, 10448896\n"
    "v_mfma_f32_32x32x16_bf16 v[146:161], v[30:33], a[72:75], v[146:161]\n"
    "s_waitcnt vmcnt(20)\n"
    "v_mfma_f32_32x32x16_bf16 v[130:145], v[34:37], a[76:79], v[130:145]\n"
    "buffer_load_dwordx4 a[152:155], v233, s[52:55], s37 offen offset:3072 sc1\n"
    "v_mfma_f32_32x32x16_bf16 v[146:161], v[38:41], a[76:79], v[146:161]\n"
    "s_waitcnt vmcnt(20)\n"
    "v_mfma_f32_32x32x16_bf16 v[130:145], v[42:45], a[80:83], v[130:145]\n"
    "s_add_u32 s37, s38, s34\n"
    "buffer_load_dwordx4 a[156:159], v233, s[52:55], s37 offen offset:0 sc1\n"
    "v_mfma_f32_32x32x16_bf16 v[146:161], v[46:49], a[80:83], v[146:161]\n"
    "s_waitcnt vmcnt(20)\n"
    "v_mfma_f32_32x32x16_bf16 v[130:145], v[50:53], a[84:87], v[130:145]\n"
    "buffer_load_dwordx4 a[160:163], v233, s[52:55], s37 offen offset:1024 sc1\n"
    "v_mfma_f32_32x32x16_bf16 v[146:161], v[54:57], a[84:87], v[146:161]\n"
    "s_waitcnt vmcnt(20)\n"
    "v_mfma_f32_32x32x16_bf16 v[130:145], v[58:61], a[88:91], v[130:145]\n"
    "buffer_load_dwordx4 a[164:167], v233, s[52:55], s37 offen offset:2048 sc1\n"
    "v_mfma_f32_32x32x16_bf16 v[146:161], v[62:65], a[88:91], v[146:161]\n"
    "s_waitcnt vmcnt(20)\n"
    "v_mfma_f32_32x32x16_bf16 v[130:145], v[66:69], a[92:95], v[130:145]\n"
    "buffer_load_dwordx4 a[168:171], v233, s[52:55], s37 offen offset:3072 sc1\n"
    "v_mfma_f32_32x32x16_bf16 v[146:161], v[70:73], a[92:95], v[146:161]\n"
    "s_waitcnt vmcnt(20)\n"
    "v_mfma_f32_32x32x16_bf16 v[130:145], v[74:77], a[96:99], v[130:145]\n"
    "buffer_load_dword v240, v125, s[52:55], s15 offen sc1\n"
    "v_mfma_f32_32x32x16_bf16 v[146:161], v[78:81], a[96:99], v[146:161]\n"
    "s_waitcnt vmcnt(20)\n"
    "v_mfma_f32_32x32x16_bf16 v[130:145], v[82:85], a[100:103], v[130:145]\n"
    "v_mfma_f32_32x32x16_bf16 v[146:161], v[86:89], a[100:103], v[146:161]\n"
    "s_waitcnt vmcnt(19)\n"
    "v_mfma_f32_32x32x16_bf16 v[130:145], v[90:93], a[104:107], v[130:145]\n"
    "v_mfma_f32_32x32x16_bf16 v[146:161], v[94:97], a[104:107], v[146:161]\n"
    "s_waitcnt vmcnt(18)\n"
    "v_mfma_f32_32x32x16_bf16 v[130:145], v[98:101], a[108:111], v[130:145]\n"
    "ds_read_b64_tr_b16 v[118:119], v123 offset:0\n"
    "ds_read_b64_tr_b16 v[120:121], v123 offset:2048\n"
    "s_mul_i32 s14, s11, 28672\n"
    "s_lshl_b32 s64, s10, 7\n"
    "v_mfma_f32_32x32x16_bf16 v[146:161], a[224:227], a[108:111], v[146:161]\n"
    "s_add_u32 s14, s14, s64\n"
    "s_lshl_b32 s64, s57, 4\n"
    "s_add_u32 s14, s14, s64\n"
    "s_waitcnt lgkmcnt(1)\n"
    "v_lshlrev_b32_e64 v210, 16, v118\n"
    "s_waitcnt vmcnt(17)\n"
    "v_mfma_f32_32x32x16_bf16 v[130:145], a[228:231], a[112:115], v[130:145]\n"
    "v_and_b32 v211, 0xffff0000, v118\n"
    "v_lshlrev_b32_e64 v212, 16, v119\n"
    "v_and_b32 v213, 0xffff0000, v119\n"
    "s_waitcnt lgkmcnt(0)\n"
    "v_lshlrev_b32 v214, 16, v120\n"
    "v_mfma_f32_32x32x16_bf16 v[146:161], a[232:235], a[112:115], v[146:161]\n"
    "v_and_b32 v215, 0xffff0000, v120\n"
    "v_lshlrev_b32_e64 v216, 16, v121\n"
    "v_and_b32 v217, 0xffff0000, v121\n"
    "s_waitcnt vmcnt(16)\n"
    "v_mfma_f32_32x32x16_bf16 v[130:145], a[236:239], a[116:119], v[130:145]\n"
    "v_mfma_f32_32x32x16_bf16 v[146:161], a[240:243], a[116:119], v[146:161]\n"
    "s_waitcnt vmcnt(15)\n"
    "v_mfma_f32_32x32x16_bf16 v[130:145], a[244:247], a[120:123], v[130:145]\n"
    "v_mfma_f32_32x32x16_bf16 v[146:161], a[248:251], a[120:123], v[146:161]\n"
    "s_waitcnt vmcnt(13)\n"
    "v_mfma_f32_32x32x16_bf16 v[130:145], a[252:255], a[124:127], v[130:145]\n"
    "s_waitcnt vmcnt(0)\n"
    "v_cmp_eq_u32_e64 vcc, -1, v240\n"
    "s_cbranch_vccnz Lslow_probe_%=\n"
    "Lback_probe_%=:\n"
    "s_mov_b32 s38, s15\n"
    "v_mfma_f32_32x32x16_bf16 v[146:161], a[0:3], a[124:127], v[146:161]\n"
    "s_add_u32 s37, s38, s28\n"
    "buffer_load_dwordx4 a[60:63], v233, s[52:55], s37 offen offset:0 sc1\n"
    "v_mfma_f32_32x32x16_bf16 v[130:145], a[4:7], a[128:131], v[130:145]\n"
    "v_mfma_f32_32x32x16_bf16 v[146:161], a[8:11], a[128:131], v[146:161]\n"
    "buffer_load_dwordx4 a[64:67], v233, s[52:55], s37 offen offset:1024 sc1\n"
    "v_mfma_f32_32x32x16_bf16 v[130:145], a[12:15], a[132:135], v[130:145]\n"
    "v_mfma_f32_32x32x16_bf16 v[146:161], a[16:19], a[132:135], v[146:161]\n"
    "buffer_load_dwordx4 a[68:71], v233, s[52:55], s37 offen offset:2048 sc1\n"
    "v_mfma_f32_32x32x16_bf16 v[130:145], a[20:23], a[136:139], v[130:145]\n"
    "v_mfma_f32_32x32x16_bf16 v[146:161], a[24:27], a[136:139], v[146:161]\n"
    "buffer_load_dwordx4 a[72:75], v233, s[52:55], s37 offen offset:3072 sc1\n"
    "v_mfma_f32_32x32x16_bf16 v[130:145], a[172:175], a[140:143], v[130:145]\n"
    "v_mfma_f32_32x32x16_bf16 v[146:161], a[176:179], a[140:143], v[146:161]\n"
    "s_add_u32 s37, s38, s29\n"
    "buffer_load_dwordx4 a[76:79], v233, s[52:55], s37 offen offset:0 sc1\n"
    "v_mfma_f32_32x32x16_bf16 v[130:145], a[180:183], a[144:147], v[130:145]\n"
    "v_mfma_f32_32x32x16_bf16 v[146:161], a[184:187], a[144:147], v[146:161]\n"
    "buffer_load_dwordx4 a[80:83], v233, s[52:55], s37 offen offset:1024 sc1\n"
    "v_mfma_f32_32x32x16_bf16 v[130:145], a[188:191], a[148:151], v[130:145]\n"
    "v_mfma_f32_32x32x16_bf16 v[146:161], a[192:195], a[148:151], v[146:161]\n"
    "buffer_load_dwordx4 a[84:87], v233, s[52:55], s37 offen offset:2048 sc1\n"
    "v_mfma_f32_32x32x16_bf16 v[130:145], a[196:199], a[152:155], v[130:145]\n"
    "v_mfma_f32_32x32x16_bf16 v[146:161], a[200:203], a[152:155], v[146:161]\n"
    "buffer_load_dwordx4 a[88:91], v233, s[52:55], s37 offen offset:3072 sc1\n"
    "v_mfma_f32_32x32x16_bf16 v[130:145], a[28:31], a[156:159], v[130:145]\n"
    "v_mfma_f32_32x32x16_bf16 v[146:161], a[32:35], a[156:159], v[146:161]\n"
    "s_add_u32 s37, s38, s30\n"
    "buffer_load_dwordx4 a[92:95], v233, s[52:55], s37 offen offset:0 sc1\n"
    "v_mfma_f32_32x32x16_bf16 v[130:145], a[36:39], a[160:163], v[130:145]\n"
    "v_mfma_f32_32x32x16_bf16 v[146:161], a[40:43], a[160:163], v[146:161]\n"
    "buffer_load_dwordx4 a[96:99], v233, s[52:55], s37 offen offset:1024 sc1\n"
    "v_mfma_f32_32x32x16_bf16 v[130:145], a[44:47], a[164:167], v[130:145]\n"
    "v_mfma_f32_32x32x16_bf16 v[146:161], a[48:51], a[164:167], v[146:161]\n"
    "buffer_load_dwordx4 a[100:103], v233, s[52:55], s37 offen offset:2048 sc1\n"
    "v_mfma_f32_32x32x16_bf16 v[130:145], a[52:55], a[168:171], v[130:145]\n"
    "v_mfma_f32_32x32x16_bf16 v[146:161], a[56:59], a[168:171], v[146:161]\n"
    "buffer_load_dwordx4 a[104:107], v233, s[52:55], s37 offen offset:3072 sc1\n"
    "s_waitcnt vmcnt(11)\n"
    "v_mfma_f32_32x32x16_bf16 v[162:177], v[2:5], a[60:63], 0\n"
    "s_add_u32 s37, s38, s31\n"
    "buffer_load_dwordx4 a[108:111], v233, s[52:55], s37 offen offset:0 sc1\n"
    "s_waitcnt vmcnt(11)\n"
    "v_mfma_f32_32x32x16_bf16 v[162:177], v[10:13], a[64:67], v[162:177]\n"
    "s_waitcnt vmcnt(10)\n"
    "v_mfma_f32_32x32x16_bf16 v[162:177], v[18:21], a[68:71], v[162:177]\n"
    "buffer_load_dwordx4 a[112:115], v233, s[52:55], s37 offen offset:1024 sc1\n"
    "s_waitcnt vmcnt(10)\n"
    "v_mfma_f32_32x32x16_bf16 v[162:177], v[26:29], a[72:75], v[162:177]\n"
    "ds_write_b128 v1, v[130:133] offset:0\n"
    "s_waitcnt vmcnt(9)\n"
    "v_mfma_f32_32x32x16_bf16 v[162:177], v[34:37], a[76:79], v[162:177]\n"
    "buffer_load_dwordx4 a[116:119], v233, s[52:55], s37 offen offset:2048 sc1\n"
    "v_mfma_f32_32x32x16_bf16 v[178:193], v[6:9], a[60:63], 0\n"
    "ds_write_b128 v1, v[134:137] offset:1024\n"
    "s_waitcnt vmcnt(9)\n"
    "v_mfma_f32_32x32x16_bf16 v[162:177], v[42:45], a[80:83], v[162:177]\n"
    "buffer_load_dwordx4 a[120:123], v233, s[52:55], s37 offen offset:3072 sc1\n"
    "v_mfma_f32_32x32x16_bf16 v[178:193], v[14:17], a[64:67], v[178:193]\n"
    "ds_write_b128 v1, v[138:141] offset:2048\n"
    "s_waitcnt vmcnt(9)\n"
    "v_mfma_f32_32x32x16_bf16 v[162:177], v[50:53], a[84:87], v[162:177]\n"
    "s_add_u32 s37, s38, s32\n"
    "buffer_load_dwordx4 a[124:127], v233, s[52:55], s37 offen offset:0 sc1\n"
    "v_mfma_f32_32x32x16_bf16 v[178:193], v[22:25], a[68:71], v[178:193]\n"
    "ds_write_b128 v1, v[142:145] offset:3072\n"
    "s_waitcnt vmcnt(9)\n"
    "v_mfma_f32_32x32x16_bf16 v[162:177], v[58:61], a[88:91], v[162:177]\n"
    "buffer_load_dwordx4 a[128:131], v233, s[52:55], s37 offen offset:1024 sc1\n"
    "v_mfma_f32_32x32x16_bf16 v[178:193], v[30:33], a[72:75], v[178:193]\n"
    "ds_write_b128 v1, v[146:149] offset:4096\n"
    "s_waitcnt vmcnt(9)\n"
    "v_mfma_f32_32x32x16_bf16 v[162:177], v[66:69], a[92:95], v[162:177]\n"
    "buffer_load_dwordx4 a[132:135], v233, s[52:55], s37 offen offset:2048 sc1\n"
    "v_mfma_f32_32x32x16_bf16 v[178:193], v[38:41], a[76:79], v[178:193]\n"
    "ds_write_b128 v1, v[150:153] offset:5120\n"
    "s_waitcnt vmcnt(9)\n"
    "v_mfma_f32_32x32x16_bf16 v[162:177], v[74:77], a[96:99], v[162:177]\n"
    "buffer_load_dwordx4 a[136:139], v233, s[52:55], s37 offen offset:3072 sc1\n"
    "v_mfma_f32_32x32x16_bf16 v[178:193], v[46:49], a[80:83], v[178:193]\n"
    "ds_write_b128 v1, v[154:157] offset:6144\n"
    "s_waitcnt vmcnt(9)\n"
    "v_mfma_f32_32x32x16_bf16 v[162:177], v[82:85], a[100:103], v[162:177]\n"
    "s_add_u32 s37, s38, s33\n"
    "buffer_load_dwordx4 a[140:143], v233, s[52:55], s37 offen offset:0 sc1\n"
    "v_mfma_f32_32x32x16_bf16 v[178:193], v[54:57], a[84:87], v[178:193]\n"
    "ds_write_b128 v1, v[158:161] offset:7168\n"
    "s_waitcnt vmcnt(9)\n"
    "v_mfma_f32_32x32x16_bf16 v[162:177], v[90:93], a[104:107], v[162:177]\n"
    "buffer_load_dwordx4 a[144:147], v233, s[52:55], s37 offen offset:1024 sc1\n"
    "v_mfma_f32_32x32x16_bf16 v[178:193], v[62:65], a[88:91], v[178:193]\n"
    "s_waitcnt vmcnt(9)\n"
    "v_mfma_f32_32x32x16_bf16 v[162:177], v[98:101], a[108:111], v[162:177]\n"
    "buffer_load_dwordx4 a[148:151], v233, s[52:55], s37 offen offset:2048 sc1\n"
    "v_mfma_f32_32x32x16_bf16 v[178:193], v[70:73], a[92:95], v[178:193]\n"
    "s_waitcnt lgkmcnt(0)\n"
    "s_barrier\n"
    "ds_read_b128 v[194:197], v122 offset:0\n"
    "ds_read_b128 v[198:201], v122 offset:4096\n"
    "ds_read_b128 v[202:205], v122 offset:8192\n"
    "ds_read_b128 v[206:209], v122 offset:12288\n"
    "s_waitcnt vmcnt(9)\n"
    "v_mfma_f32_32x32x16_bf16 v[162:177], a[228:231], a[112:115], v[162:177]\n"
    "buffer_load_dwordx4 a[152:155], v233, s[52:55], s37 offen offset:3072 sc1\n"
    "v_mfma_f32_32x32x16_bf16 v[178:193], v[78:81], a[96:99], v[178:193]\n"
    "ds_read_b128 v[102:105], v122 offset:16384\n"
    "ds_read_b128 v[106:109], v122 offset:20480\n"
    "ds_read_b128 v[110:113], v122 offset:24576\n"
    "ds_read_b128 v[114:117], v122 offset:28672\n"
    "s_waitcnt vmcnt(9)\n"
    "v_mfma_f32_32x32x16_bf16 v[162:177], a[236:239], a[116:119], v[162:177]\n"
    "s_add_u32 s37, s38, s34\n"
    "buffer_load_dwordx4 a[156:159], v233, s[52:55], s37 offen offset:0 sc1\n"
    "v_mfma_f32_32x32x16_bf16 v[178:193], v[86:89], a[100:103], v[178:193]\n"
    "s_waitcnt vmcnt(9)\n"
    "v_mfma_f32_32x32x16_bf16 v[162:177], a[244:247], a[120:123], v[162:177]\n"
    "buffer_load_dwordx4 a[160:163], v233, s[52:55], s37 offen offset:1024 sc1\n"
    "s_waitcnt lgkmcnt(7)\n"
    "v_add_f32 v210, v210, v194\n"
    "v_add_f32 v211, v211, v195\n"
    "v_add_f32_e64 v212, v212, v196\n"
    "v_mfma_f32_32x32x16_bf16 v[178:193], v[94:97], a[104:107], v[178:193]\n"
    "v_add_f32 v213, v213, v197\n"
    "s_waitcnt lgkmcnt(6)\n"
    "v_add_f32 v214, v214, v198\n"
    "v_add_f32 v215, v215, v199\n"
    "v_add_f32 v216, v216, v200\n"
    "v_add_f32_e64 v217, v217, v201\n"
    "s_waitcnt vmcnt(9)\n"
    "v_mfma_f32_32x32x16_bf16 v[162:177], a[252:255], a[124:127], v[162:177]\n"
    "buffer_load_dwordx4 a[164:167], v233, s[52:55], s37 offen offset:2048 sc1\n"
    "s_waitcnt lgkmcnt(5)\n"
    "v_add_f32 v210, v210, v202\n"
    "v_add_f32 v211, v211, v203\n"
    "v_add_f32 v212, v212, v204\n"
    "v_mfma_f32_32x32x16_bf16 v[178:193], a[224:227], a[108:111], v[178:193]\n"
    "v_add_f32 v213, v213, v205\n"
    "s_waitcnt lgkmcnt(4)\n"
    "v_add_f32 v214, v214, v206\n"
    "v_add_f32 v215, v215, v207\n"
    "v_add_f32 v216, v216, v208\n"
    "v_add_f32_e64 v217, v217, v209\n"
    "s_waitcnt vmcnt(9)\n"
    "v_mfma_f32_32x32x16_bf16 v[162:177], a[4:7], a[128:131], v[162:177]\n"
    "buffer_load_dwordx4 a[168:171], v233, s[52:55], s37 offen offset:3072 sc1\n"
    "s_waitcnt lgkmcnt(3)\n"
    "v_add_f32 v210, v210, v102\n"
    "v_add_f32 v211, v211, v103\n"
    "v_add_f32 v212, v212, v104\n"
    "v_mfma_f32_32x32x16_bf16 v[178:193], a[232:235], a[112:115], v[178:193]\n"
    "v_add_f32 v213, v213, v105\n"
    "s_waitcnt lgkmcnt(2)\n"
    "v_add_f32 v214, v214, v106\n"
    "v_add_f32 v215, v215, v107\n"
    "v_add_f32 v216, v216, v108\n"
    "v_add_f32_e64 v217, v217, v109\n"
    "s_waitcnt vmcnt(9)\n"
    "v_mfma_f32_32x32x16_bf16 v[162:177], a[12:15], a[132:135], v[162:177]\n"
    "s_waitcnt lgkmcnt(1)\n"
    "v_add_f32 v210, v210, v110\n"
    "v_add_f32 v211, v211, v111\n"
    "v_add_f32 v212, v212, v112\n"
    "v_add_f32 v213, v213, v113\n"
    "s_waitcnt lgkmcnt(0)\n"
    "v_add_f32_e64 v214, v214, v114\n"
    "v_mfma_f32_32x32x16_bf16 v[178:193], a[240:243], a[116:119], v[178:193]\n"
    "v_add_f32 v215, v215, v115\n"
    "v_add_f32 v216, v216, v116\n"
    "v_add_f32_e64 v217, v217, v117\n"
    "v_cvt_pk_bf16_f32 v118, v210, v211\n"
    "v_cvt_pk_bf16_f32 v119, v212, v213\n"
    "s_waitcnt vmcnt(8)\n"
    "v_mfma_f32_32x32x16_bf16 v[162:177], a[20:23], a[136:139], v[162:177]\n"
    "v_cvt_pk_bf16_f32 v120, v214, v215\n"
    "v_cvt_pk_bf16_f32 v121, v216, v217\n"
    "s_nop 0\n"
    "v_permlane32_swap_b32 v118, v120\n"
    "v_permlane32_swap_b32 v119, v121\n"
    "v_mfma_f32_32x32x16_bf16 v[178:193], a[248:251], a[120:123], v[178:193]\n"
    "buffer_store_dwordx4 v[118:121], v124, s[48:51], s14 offen\n"
    "s_waitcnt vmcnt(8)\n"
    "v_mfma_f32_32x32x16_bf16 v[162:177], a[172:175], a[140:143], v[162:177]\n"
    "ds_read_b64_tr_b16 v[118:119], v123 offset:4096\n"
    "ds_read_b64_tr_b16 v[120:121], v123 offset:6144\n"
    "s_mul_i32 s14, s12, 28672\n"
    "s_lshl_b32 s64, s10, 7\n"
    "s_add_u32 s14, s14, s64\n"
    "v_mfma_f32_32x32x16_bf16 v[178:193], a[0:3], a[124:127], v[178:193]\n"
    "s_lshl_b32 s64, s57, 4\n"
    "s_add_u32 s14, s14, s64\n"
    "s_waitcnt lgkmcnt(1)\n"
    "v_lshlrev_b32_e64 v210, 16, v118\n"
    "v_and_b32 v211, 0xffff0000, v118\n"
    "v_lshlrev_b32 v212, 16, v119\n"
    "s_waitcnt vmcnt(7)\n"
    "v_mfma_f32_32x32x16_bf16 v[162:177], a[180:183], a[144:147], v[162:177]\n"
    "v_and_b32 v213, 0xffff0000, v119\n"
    "s_waitcnt lgkmcnt(0)\n"
    "v_lshlrev_b32 v214, 16, v120\n"
    "v_and_b32 v215, 0xffff0000, v120\n"
    "v_lshlrev_b32_e64 v216, 16, v121\n"
    "v_and_b32 v217, 0xffff0000, v121\n"
    "v_mfma_f32_32x32x16_bf16 v[178:193], a[8:11], a[128:131], v[178:193]\n"
    "s_waitcnt vmcnt(6)\n"
    "v_mfma_f32_32x32x16_bf16 v[162:177], a[188:191], a[148:151], v[162:177]\n"
    "v_mfma_f32_32x32x16_bf16 v[178:193], a[16:19], a[132:135], v[178:193]\n"
    "s_waitcnt vmcnt(5)\n"
    "v_mfma_f32_32x32x16_bf16 v[162:177], a[196:199], a[152:155], v[162:177]\n"
    "v_mfma_f32_32x32x16_bf16 v[178:193], a[24:27], a[136:139], v[178:193]\n"
    "s_waitcnt vmcnt(4)\n"
    "v_mfma_f32_32x32x16_bf16 v[162:177], a[28:31], a[156:159], v[162:177]\n"
    "v_mfma_f32_32x32x16_bf16 v[178:193], a[176:179], a[140:143], v[178:193]\n"
    "s_waitcnt vmcnt(3)\n"
    "v_mfma_f32_32x32x16_bf16 v[162:177], a[36:39], a[160:163], v[162:177]\n"
    "v_mfma_f32_32x32x16_bf16 v[178:193], a[184:187], a[144:147], v[178:193]\n"
    "s_waitcnt vmcnt(2)\n"
    "v_mfma_f32_32x32x16_bf16 v[162:177], a[44:47], a[164:167], v[162:177]\n"
    "v_mfma_f32_32x32x16_bf16 v[178:193], a[192:195], a[148:151], v[178:193]\n"
    "s_waitcnt vmcnt(1)\n"
    "v_mfma_f32_32x32x16_bf16 v[162:177], a[52:55], a[168:171], v[162:177]\n"
    "v_mfma_f32_32x32x16_bf16 v[178:193], a[200:203], a[152:155], v[178:193]\n"
    "v_mfma_f32_32x32x16_bf16 v[178:193], a[32:35], a[156:159], v[178:193]\n"
    "v_mfma_f32_32x32x16_bf16 v[178:193], a[40:43], a[160:163], v[178:193]\n"
    "v_mfma_f32_32x32x16_bf16 v[178:193], a[48:51], a[164:167], v[178:193]\n"
    "s_nop 7\n"
    "ds_write_b128 v1, v[162:165] offset:32768\n"
    "ds_write_b128 v1, v[166:169] offset:33792\n"
    "v_mfma_f32_32x32x16_bf16 v[178:193], a[56:59], a[168:171], v[178:193]\n"
    "ds_write_b128 v1, v[170:173] offset:34816\n"
    "ds_write_b128 v1, v[174:177] offset:35840\n"
    "Lg2c_%=:\n"
    "s_nop 9\n"
    "v_cmp_u_f32 vcc, v178, v178\n"
    "s_cbranch_vccnz Lslow_late_%=\n"
    "Lback_late_%=:\n"
    "ds_write_b128 v1, v[178:181] offset:36864\n"
    "ds_write_b128 v1, v[182:185] offset:37888\n"
    "ds_write_b128 v1, v[186:189] offset:38912\n"
    "ds_write_b128 v1, v[190:193] offset:39936\n"
    "s_waitcnt lgkmcnt(0)\n"
    "s_barrier\n"
    "ds_read_b128 v[194:197], v122 offset:32768\n"
    "ds_read_b128 v[198:201], v122 offset:36864\n"
    "ds_read_b128 v[202:205], v122 offset:40960\n"
    "ds_read_b128 v[206:209], v122 offset:45056\n"
    "ds_read_b128 v[102:105], v122 offset:49152\n"
    "ds_read_b128 v[106:109], v122 offset:53248\n"
    "ds_read_b128 v[110:113], v122 offset:57344\n"
    "ds_read_b128 v[114:117], v122 offset:61440\n"
    "s_waitcnt lgkmcnt(7)\n"
    "v_add_f32 v210, v210, v194\n"
    "v_add_f32 v211, v211, v195\n"
    "v_add_f32 v212, v212, v196\n"
    "v_add_f32 v213, v213, v197\n"
    "s_waitcnt lgkmcnt(6)\n"
    "v_add_f32 v214, v214, v198\n"
    "v_add_f32 v215, v215, v199\n"
    "v_add_f32 v216, v216, v200\n"
    "v_add_f32 v217, v217, v201\n"
    "s_waitcnt lgkmcnt(5)\n"
    "v_add_f32 v210, v210, v202\n"
    "v_add_f32 v211, v211, v203\n"
    "v_add_f32 v212, v212, v204\n"
    "v_add_f32 v213, v213, v205\n"
    "s_waitcnt lgkmcnt(4)\n"
    "v_add_f32 v214, v214, v206\n"
    "v_add_f32 v215, v215, v207\n"
    "v_add_f32 v216, v216, v208\n"
    "v_add_f32 v217, v217, v209\n"
    "s_waitcnt lgkmcnt(3)\n"
    "v_add_f32 v210, v210, v102\n"
    "v_add_f32 v211, v211, v103\n"
    "v_add_f32 v212, v212, v104\n"
    "v_add_f32 v213, v213, v105\n"
    "s_waitcnt lgkmcnt(2)\n"
    "v_add_f32 v214, v214, v106\n"
    "v_add_f32 v215, v215, v107\n"
    "v_add_f32 v216, v216, v108\n"
    "v_add_f32 v217, v217, v109\n"
    "s_waitcnt lgkmcnt(1)\n"
    "v_add_f32 v210, v210, v110\n"
    "v_add_f32 v211, v211, v111\n"
    "v_add_f32 v212, v212, v112\n"
    "v_add_f32 v213, v213, v113\n"
    "s_waitcnt lgkmcnt(0)\n"
    "v_add_f32 v214, v214, v114\n"
    "v_add_f32 v215, v215, v115\n"
    "v_add_f32 v216, v216, v116\n"
    "v_add_f32 v217, v217, v117\n"
    "v_cvt_pk_bf16_f32 v118, v210, v211\n"
    "v_cvt_pk_bf16_f32 v119, v212, v213\n"
    "v_cvt_pk_bf16_f32 v120, v214, v215\n"
    "v_cvt_pk_bf16_f32 v121, v216, v217\n"
    "s_nop 0\n"
    "v_permlane32_swap_b32 v118, v120\n"
    "v_permlane32_swap_b32 v119, v121\n"
    "buffer_store_dwordx4 v[118:121], v124, s[48:51], s14 offen\n"
    "s_branch Lend_%=\n"
    "Lspare_%=:\n"
    "s_and_b32 s64, s58, 1\n"
    "s_lshl_b32 s64, s64, 5\n"
    "s_add_u32 s64, s64, s59\n"
    "s_and_b32 s65, s64, 48\n"
    "s_bfe_u32 s66, s64, 0x20002\n"
    "s_add_u32 s65, s65, s66\n"
    "s_and_b32 s66, s58, 6\n"
    "s_lshl1_add_u32 s65, s66, s65\n"
    "s_and_b32 s64, s64, 3\n"
    "s_lshl2_add_u32 s65, s65, s64\n"
    "s_mul_i32 s65, s65, 7168\n"
    "s_add_u32 s71, s65, 8613888\n"
    "buffer_store_dwordx4 v[10:13], v230, s[52:55], s71 offen sc1\n"
    "s_add_u32 s71, s71, 4096\n"
    "buffer_store_dwordx4 v[10:13], v14, s[52:55], s71 offen sc1\n"
    "s_lshl2_add_u32 s68, s58, s59\n"
    "s_sub_u32 s68, s68, 28\n"
    "v_lshrrev_b32 v224, 3, v222\n"
    "v_and_b32_e64 v225, 7, v222\n"
    "v_mul_u32_u24 v226, 896, v224\n"
    "v_lshl_add_u32 v15, v225, 4, v226\n"
    "s_and_b32 s64, s68, 3\n"
    "s_mul_i32 s64, s64, 28\n"
    "s_add_u32 s64, s64, s57\n"
    "s_mul_i32 s69, s64, 14336\n"
    "s_lshr_b32 s65, s68, 2\n"
    "s_lshl_b32 s65, s65, 7\n"
    "s_add_u32 s69, s69, s65\n"
    "buffer_load_dwordx4 v[16:19], v15, s[44:47], s69 offen\n"
    "s_add_u32 s69, s69, 7168\n"
    "buffer_load_dwordx4 v[20:23], v15, s[44:47], s69 offen\n"
    "s_add_u32 s69, s69, 50176\n"
    "buffer_load_dwordx4 v[24:27], v15, s[44:47], s69 offen\n"
    "s_add_u32 s69, s69, 7168\n"
    "buffer_load_dwordx4 v[28:31], v15, s[44:47], s69 offen\n"
    "s_add_u32 s69, s69, 50176\n"
    "s_mul_i32 s64, s57, 21504\n"
    "v_mul_u32_u24 v226, 192, v224\n"
    "v_lshl_add_u32 v226, v225, 4, v226\n"
    "v_add_u32 v72, s64, v226\n"
    "v_lshrrev_b32 v226, 5, v222\n"
    "v_bfe_u32 v227, v222, 2, 2\n"
    "v_lshl_add_u32 v226, v226, 2, v227\n"
    "v_mul_u32_u24 v226, 192, v226\n"
    "v_bfe_u32 v227, v222, 4, 1\n"
    "v_lshl_add_u32 v226, v227, 5, v226\n"
    "v_and_b32_e64 v227, 3, v222\n"
    "v_lshl_add_u32 v226, v227, 3, v226\n"
    "v_add_u32 v73, s64, v226\n"
    "v_lshlrev_b32 v74, 4, v222\n"
    "s_bitcmp1_b32 s68, 0\n"
    "s_cselect_b32 s72, s40, s36\n"
    "s_cselect_b32 s73, s41, s37\n"
    "s_mov_b32 s74, s38\n"
    "s_mov_b32 s75, 0x20000\n"
    "v_lshrrev_b32 v226, 4, v0\n"
    "v_and_b32 v227, 15, v0\n"
    "v_mul_u32_u24 v226, 3584, v226\n"
    "v_lshl_add_u32 v75, v227, 4, v226\n"
    "s_mul_i32 s70, s68, 28\n"
    "s_add_u32 s70, s70, s57\n"
    "s_lshl_b32 s70, s70, 11\n"
    "s_add_u32 s70, s70, 6979584\n"
    "v_and_b32 v78, 31, v0\n"
    "v_lshrrev_b32 v79, 5, v0\n"
    "v_lshlrev_b32 v125, 4, v79\n"
    "v_mul_u32_u24 v79, 912, v79\n"
    "v_cmp_gt_u32_e64 vcc, 28, v78\n"
    "v_lshl_add_u32 v106, v78, 5, v79\n"
    "v_add_u32 v106, 143680, v106\n"
    "v_mov_b32 v80, 150976\n"
    "v_cndmask_b32 v106, v80, v106, vcc\n"
    "v_and_b32 v78, 3, v0\n"
    "v_mul_u32_u24 v78, 1824, v78\n"
    "v_and_b32 v79, 0xfc, v0\n"
    "v_cmp_gt_u32 vcc, 128, v0\n"
    "v_add_u32 v78, 143680, v78\n"
    "v_add_u32 v107, v78, v79\n"
    "v_mov_b32 v80, 1073741824\n"
    "v_cndmask_b32 v124, v80, v230, vcc\n"
    "v_mov_b32 v76, 0\n"
    "v_mov_b32 v77, 0\n"
    "s_waitcnt vmcnt(8)\n"
    "v_dot2c_f32_bf16 v76, 0x3f803f80, v2\n"
    "v_dot2c_f32_bf16 v77, v2, v2\n"
    "v_dot2c_f32_bf16 v76, 0x3f803f80, v3\n"
    "v_dot2c_f32_bf16 v77, v3, v3\n"
    "v_dot2c_f32_bf16 v76, 0x3f803f80, v4\n"
    "v_dot2c_f32_bf16 v77, v4, v4\n"
    "v_dot2c_f32_bf16 v76, 0x3f803f80, v5\n"
    "v_dot2c_f32_bf16 v77, v5, v5\n"
    "s_waitcnt vmcnt(7)\n"
    "v_dot2c_f32_bf16 v76, 0x3f803f80, v6\n"
    "v_dot2c_f32_bf16 v77, v6, v6\n"
    "v_dot2c_f32_bf16 v76, 0x3f803f80, v7\n"
    "v_dot2c_f32_bf16 v77, v7, v7\n"
    "v_dot2c_f32_bf16 v76, 0x3f803f80, v8\n"
    "v_dot2c_f32_bf16 v77, v8, v8\n"
    "v_dot2c_f32_bf16 v76, 0x3f803f80, v9\n"
    "v_dot2c_f32_bf16 v77, v9, v9\n"
    "s_waitcnt vmcnt(6)\n"
    "v_add_u32 v231, 1, v1\n"
    "v_lshlrev_b32 v90, 16, v2\n"
    "v_and_b32 v91, 0xffff0000, v2\n"
    "v_add_f32_dpp v76, v76, v76 quad_perm:[1,0,3,2] row_mask:0xf bank_mask:0xf\n"
    "v_add_f32_dpp v77, v77, v77 quad_perm:[1,0,3,2] row_mask:0xf bank_mask:0xf\n"
    "v_lshlrev_b32_e64 v92, 16, v3\n"
    "v_and_b32 v93, 0xffff0000, v3\n"
    "v_add_f32_dpp v76, v76, v76 quad_perm:[2,3,0,1] row_mask:0xf bank_mask:0xf\n"
    "v_add_f32_dpp v77, v77, v77 quad_perm:[2,3,0,1] row_mask:0xf bank_mask:0xf\n"
    "v_lshlrev_b32_e64 v94, 16, v4\n"
    "v_and_b32 v95, 0xffff0000, v4\n"
    "v_add_f32_dpp v76, v76, v76 row_half_mirror row_mask:0xf bank_mask:0xf\n"
    "v_add_f32_dpp v77, v77, v77 row_half_mirror row_mask:0xf bank_mask:0xf\n"
    "v_lshlrev_b32_e64 v96, 16, v5\n"
    "v_and_b32 v97, 0xffff0000, v5\n"
    "v_add_f32_dpp v76, v76, v76 row_mirror row_mask:0xf bank_mask:0xf\n"
    "v_add_f32_dpp v77, v77, v77 row_mirror row_mask:0xf bank_mask:0xf\n"
    "v_lshlrev_b32_e64 v98, 16, v6\n"
    "v_and_b32 v99, 0xffff0000, v6\n"
    "v_mov_b32 v78, v76\n"
    "v_mov_b32 v79, v77\n"
    "v_readfirstlane_b32 s60, v231\n"
    "v_lshlrev_b32 v100, 16, v7\n"
    "v_permlane16_swap_b32 v76, v78\n"
    "v_permlane16_swap_b32 v77, v79\n"
    "v_add_f32 v76, v76, v78\n"
    "v_add_f32 v77, v77, v79\n"
    "v_mul_f32 v126, 0x3b124925, v76\n"
    "v_mov_b32 v80, 0x3727c5ac\n"
    "v_fmac_f32 v80, 0x3b124925, v77\n"
    "v_fma_f32 v80, -v126, v126, v80\n"
    "v_max_f32 v128, 0x3727c5ac, v80\n"
    "v_rsq_f32_e64 v80, v128\n"
    "v_and_b32 v101, 0xffff0000, v7\n"
    "v_lshlrev_b32_e64 v102, 16, v8\n"
    "v_and_b32 v103, 0xffff0000, v8\n"
    "v_lshlrev_b32_e64 v104, 16, v9\n"
    "v_and_b32 v105, 0xffff0000, v9\n"
    "v_mul_f32_e64 v76, -v126, v80\n"
    "v_mul_f32_e64 v128, v128, v80\n"
    "v_fma_f32 v90, v90, v80, v76\n"
    "v_fma_f32 v91, v91, v80, v76\n"
    "v_fma_f32 v92, v92, v80, v76\n"
    "v_fma_f32 v93, v93, v80, v76\n"
    "v_fma_f32 v94, v94, v80, v76\n"
    "v_fma_f32 v95, v95, v80, v76\n"
    "v_fma_f32 v96, v96, v80, v76\n"
    "v_fma_f32 v97, v97, v80, v76\n"
    "v_fma_f32 v98, v98, v80, v76\n"
    "v_fma_f32 v99, v99, v80, v76\n"
    "v_fma_f32 v100, v100, v80, v76\n"
    "v_fma_f32 v101, v101, v80, v76\n"
    "v_fma_f32 v102, v102, v80, v76\n"
    "v_fma_f32 v103, v103, v80, v76\n"
    "v_fma_f32 v104, v104, v80, v76\n"
    "v_fma_f32 v105, v105, v80, v76\n"
    "v_cvt_pk_f16_f32 v82, v90, v91\n"
    "v_cvt_pk_f16_f32 v83, v92, v93\n"
    "v_cvt_pk_f16_f32 v84, v94, v95\n"
    "v_cvt_pk_f16_f32 v85, v96, v97\n"
    "v_cvt_pk_f16_f32 v86, v98, v99\n"
    "v_cvt_pk_f16_f32 v87, v100, v101\n"
    "v_cvt_pk_f16_f32 v88, v102, v103\n"
    "v_cvt_pk_f16_f32 v89, v104, v105\n"
    "ds_write_b128 v106, v[82:85]\n"
    "ds_write_b128 v106, v[86:89] offset:16\n"
    "v_mov_b32 v109, v231\n"
    "v_mov_b32 v111, v231\n"
    "v_mov_b32 v113, v231\n"
    "v_mov_b32 v115, v231\n"
    "v_mov_b32 v117, v231\n"
    "v_mov_b32 v119, v231\n"
    "v_mov_b32 v121, v231\n"
    "v_mov_b32 v123, v231\n"
    "v_mov_b32 v127, v231\n"
    "v_mov_b32 v129, v231\n"
    "s_waitcnt lgkmcnt(0)\n"
    "s_barrier\n"
    "ds_read_b32 v108, v107 offset:0\n"
    "ds_read_b32 v110, v107 offset:912\n"
    "ds_read_b32 v112, v107 offset:256\n"
    "ds_read_b32 v114, v107 offset:1168\n"
    "ds_read_b32 v116, v107 offset:512\n"
    "ds_read_b32 v118, v107 offset:1424\n"
    "ds_read_b32 v120, v107 offset:768\n"
    "ds_read_b32 v122, v107 offset:1680\n"
    "s_mul_i32 s64, s56, 14336\n"
    "s_add_u32 s64, s64, 98304\n"
    "s_waitcnt lgkmcnt(6)\n"
    "buffer_store_dwordx4 v[108:111], v230, s[52:55], s64 offen sc1\n"
    "s_add_u32 s64, s64, 4096\n"
    "s_waitcnt lgkmcnt(4)\n"
    "buffer_store_dwordx4 v[112:115], v230, s[52:55], s64 offen sc1\n"
    "s_add_u32 s64, s64, 4096\n"
    "s_waitcnt lgkmcnt(2)\n"
    "buffer_store_dwordx4 v[116:119], v230, s[52:55], s64 offen sc1\n"
    "s_add_u32 s64, s64, 4096\n"
    "s_waitcnt lgkmcnt(0)\n"
    "buffer_store_dwordx4 v[120:123], v124, s[52:55], s64 offen sc1\n"
    "s_waitcnt vmcnt(8)\n"
    "s_lshl_b32 s64, s56, 7\n"
    "s_add_u32 s64, s64, 32768\n"
    "s_mov_b32 exec_lo, 1\n"
    "s_mov_b32 exec_hi, 1\n"
    "buffer_store_dwordx4 v[126:129], v125, s[52:55], s64 offen sc1\n"
    "s_mov_b64 exec, -1\n"
    "s_nop 0\n"
    "buffer_load_dwordx4 v[32:35], v15, s[44:47], s69 offen\n"
    "s_add_u32 s69, s69, 7168\n"
    "buffer_load_dwordx4 v[36:39], v15, s[44:47], s69 offen\n"
    "s_add_u32 s69, s69, 50176\n"
    "buffer_load_dwordx4 v[40:43], v15, s[44:47], s69 offen\n"
    "s_add_u32 s69, s69, 7168\n"
    "buffer_load_dwordx4 v[44:47], v15, s[44:47], s69 offen\n"
    "s_add_u32 s69, s69, 50176\n"
    "buffer_load_dwordx4 v[48:51], v15, s[44:47], s69 offen\n"
    "s_add_u32 s69, s69, 7168\n"
    "buffer_load_dwordx4 v[52:55], v15, s[44:47], s69 offen\n"
    "s_add_u32 s69, s69, 50176\n"
    "buffer_load_dwordx4 v[56:59], v15, s[44:47], s69 offen\n"
    "s_add_u32 s69, s69, 7168\n"
    "buffer_load_dwordx4 v[60:63], v15, s[44:47], s69 offen\n"
    "s_add_u32 s69, s69, 50176\n"
    "buffer_load_dwordx4 v[64:67], v15, s[44:47], s69 offen\n"
    "s_add_u32 s69, s69, 7168\n"
    "buffer_load_dwordx4 v[68:71], v15, s[44:47], s69 offen\n"
    "s_add_u32 s69, s69, 50176\n"
    "s_lshr_b32 s69, s68, 1\n"
    "s_lshl_b32 s69, s69, 8\n"
    "s_cmp_lt_u32 s68, 28\n"
    "s_cselect_b32 s64, 0, 1073741824\n"
    "v_add_u32 v1, s64, v74\n"
    "s_lshr_b32 s64, s68, 1\n"
    "s_and_b32 s65, s68, 1\n"
    "s_lshl_b32 s65, s65, 4\n"
    "s_sub_u32 s64, s64, s65\n"
    "s_add_u32 s64, s64, 48\n"
    "s_mul_i32 s76, s64, 114688\n"
    "s_add_u32 s76, s76, 10448896\n"
    "s_waitcnt vmcnt(18)\n"
    "ds_write_b128 v72, v[16:19] offset:0\n"
    "s_waitcnt vmcnt(17)\n"
    "ds_write_b128 v72, v[20:23] offset:1536\n"
    "ds_read_b64_tr_b16 v[188:189], v73 offset:0\n"
    "ds_read_b64_tr_b16 v[190:191], v73 offset:1536\n"
    "ds_read_b64_tr_b16 v[192:193], v73 offset:64\n"
    "ds_read_b64_tr_b16 v[194:195], v73 offset:1600\n"
    "buffer_load_dwordx4 v[76:79], v75, s[72:75], s69 offen\n"
    "s_add_u32 s69, s69, 57344\n"
    "s_waitcnt lgkmcnt(2)\n"
    "buffer_store_dwordx4 v[188:191], v1, s[52:55], s70 offen offset:0 sc1\n"
    "s_waitcnt lgkmcnt(0)\n"
    "buffer_store_dwordx4 v[192:195], v1, s[52:55], s70 offen offset:1024 sc1\n"
    "s_add_u32 s70, s70, 8192\n"
    "s_waitcnt vmcnt(19)\n"
    "ds_write_b128 v72, v[24:27] offset:3072\n"
    "s_waitcnt vmcnt(18)\n"
    "ds_write_b128 v72, v[28:31] offset:4608\n"
    "ds_read_b64_tr_b16 v[196:197], v73 offset:3072\n"
    "ds_read_b64_tr_b16 v[198:199], v73 offset:4608\n"
    "ds_read_b64_tr_b16 v[200:201], v73 offset:3136\n"
    "ds_read_b64_tr_b16 v[202:203], v73 offset:4672\n"
    "buffer_load_dwordx4 v[80:83], v75, s[72:75], s69 offen\n"
    "s_add_u32 s69, s69, 57344\n"
    "s_waitcnt lgkmcnt(2)\n"
    "buffer_store_dwordx4 v[196:199], v1, s[52:55], s70 offen offset:0 sc1\n"
    "s_waitcnt lgkmcnt(0)\n"
    "buffer_store_dwordx4 v[200:203], v1, s[52:55], s70 offen offset:1024 sc1\n"
    "s_add_u32 s70, s70, 8192\n"
    "s_waitcnt vmcnt(15)\n"
    "ds_write_b128 v72, v[32:35] offset:6144\n"
    "s_waitcnt vmcnt(14)\n"
    "ds_write_b128 v72, v[36:39] offset:7680\n"
    "ds_read_b64_tr_b16 v[188:189], v73 offset:6144\n"
    "ds_read_b64_tr_b16 v[190:191], v73 offset:7680\n"
    "ds_read_b64_tr_b16 v[192:193], v73 offset:6208\n"
    "ds_read_b64_tr_b16 v[194:195], v73 offset:7744\n"
    "buffer_load_dwordx4 v[84:87], v75, s[72:75], s69 offen\n"
    "s_add_u32 s69, s69, 57344\n"
    "s_waitcnt lgkmcnt(2)\n"
    "buffer_store_dwordx4 v[188:191], v1, s[52:55], s70 offen offset:0 sc1\n"
    "s_waitcnt lgkmcnt(0)\n"
    "buffer_store_dwordx4 v[192:195], v1, s[52:55], s70 offen offset:1024 sc1\n"
    "s_add_u32 s70, s70, 8192\n"
    "s_waitcnt vmcnt(16)\n"
    "ds_write_b128 v72, v[40:43] offset:9216\n"
    "s_waitcnt vmcnt(15)\n"
    "ds_write_b128 v72, v[44:47] offset:10752\n"
    "ds_read_b64_tr_b16 v[196:197], v73 offset:9216\n"
    "ds_read_b64_tr_b16 v[198:199], v73 offset:10752\n"
    "ds_read_b64_tr_b16 v[200:201], v73 offset:9280\n"
    "ds_read_b64_tr_b16 v[202:203], v73 offset:10816\n"
    "buffer_load_dwordx4 v[88:91], v75, s[72:75], s69 offen\n"
    "s_add_u32 s69, s69, 57344\n"
    "s_waitcnt lgkmcnt(2)\n"
    "buffer_store_dwordx4 v[196:199], v1, s[52:55], s70 offen offset:0 sc1\n"
    "s_waitcnt lgkmcnt(0)\n"
    "buffer_store_dwordx4 v[200:203], v1, s[52:55], s70 offen offset:1024 sc1\n"
    "s_add_u32 s70, s70, 8192\n"
    "s_waitcnt vmcnt(17)\n"
    "ds_write_b128 v72, v[48:51] offset:12288\n"
    "s_waitcnt vmcnt(16)\n"
    "ds_write_b128 v72, v[52:55] offset:13824\n"
    "ds_read_b64_tr_b16 v[188:189], v73 offset:12288\n"
    "ds_read_b64_tr_b16 v[190:191], v73 offset:13824\n"
    "ds_read_b64_tr_b16 v[192:193], v73 offset:12352\n"
    "ds_read_b64_tr_b16 v[194:195], v73 offset:13888\n"
    "buffer_load_dwordx4 v[92:95], v75, s[72:75], s69 offen\n"
    "s_add_u32 s69, s69, 57344\n"
    "s_waitcnt lgkmcnt(2)\n"
    "buffer_store_dwordx4 v[188:191], v1, s[52:55], s70 offen offset:0 sc1\n"
    "s_waitcnt lgkmcnt(0)\n"
    "buffer_store_dwordx4 v[192:195], v1, s[52:55], s70 offen offset:1024 sc1\n"
    "s_add_u32 s70, s70, 8192\n"
    "s_waitcnt vmcnt(18)\n"
    "ds_write_b128 v72, v[56:59] offset:15360\n"
    "s_waitcnt vmcnt(17)\n"
    "ds_write_b128 v72, v[60:63] offset:16896\n"
    "ds_read_b64_tr_b16 v[196:197], v73 offset:15360\n"
    "ds_read_b64_tr_b16 v[198:199], v73 offset:16896\n"
    "ds_read_b64_tr_b16 v[200:201], v73 offset:15424\n"
    "ds_read_b64_tr_b16 v[202:203], v73 offset:16960\n"
    "buffer_load_dwordx4 v[96:99], v75, s[72:75], s69 offen\n"
    "s_add_u32 s69, s69, 57344\n"
    "s_waitcnt lgkmcnt(2)\n"
    "buffer_store_dwordx4 v[196:199], v1, s[52:55], s70 offen offset:0 sc1\n"
    "s_waitcnt lgkmcnt(0)\n"
    "buffer_store_dwordx4 v[200:203], v1, s[52:55], s70 offen offset:1024 sc1\n"
    "s_add_u32 s70, s70, 8192\n"
    "s_waitcnt vmcnt(19)\n"
    "ds_write_b128 v72, v[64:67] offset:18432\n"
    "s_waitcnt vmcnt(18)\n"
    "ds_write_b128 v72, v[68:71] offset:19968\n"
    "ds_read_b64_tr_b16 v[188:189], v73 offset:18432\n"
    "ds_read_b64_tr_b16 v[190:191], v73 offset:19968\n"
    "ds_read_b64_tr_b16 v[192:193], v73 offset:18496\n"
    "ds_read_b64_tr_b16 v[194:195], v73 offset:20032\n"
    "buffer_load_dwordx4 v[100:103], v75, s[72:75], s69 offen\n"
    "s_add_u32 s69, s69, 57344\n"
    "s_waitcnt lgkmcnt(2)\n"
    "buffer_store_dwordx4 v[188:191], v1, s[52:55], s70 offen offset:0 sc1\n"
    "s_waitcnt lgkmcnt(0)\n"
    "buffer_store_dwordx4 v[192:195], v1, s[52:55], s70 offen offset:1024 sc1\n"
    "s_mov_b32 s66, 0x3f80\n"
    "s_mov_b32 s67, 0x3f800000\n"
    "v_mov_b32 v2, 0\n"
    "v_mov_b32 v3, 0\n"
    "v_mov_b32 v4, 0\n"
    "v_mov_b32 v5, 0\n"
    "v_mov_b32 v6, 0\n"
    "v_mov_b32 v7, 0\n"
    "v_mov_b32 v8, 0\n"
    "v_mov_b32_e64 v9, 0\n"
    "buffer_load_dwordx4 v[104:107], v75, s[72:75], s69 offen\n"
    "s_add_u32 s69, s69, 57344\n"
    "s_waitcnt vmcnt(21)\n"
    "v_dot2c_f32_bf16 v2, s66, v76\n"
    "v_dot2c_f32_bf16 v3, s67, v76\n"
    "v_dot2c_f32_bf16 v4, s66, v77\n"
    "v_dot2c_f32_bf16 v5, s67, v77\n"
    "v_dot2c_f32_bf16 v6, s66, v78\n"
    "v_dot2c_f32_bf16 v7, s67, v78\n"
    "v_dot2c_f32_bf16 v8, s66, v79\n"
    "v_dot2c_f32_bf16 v9, s67, v79\n"
    "buffer_load_dwordx4 v[108:111], v75, s[72:75], s69 offen\n"
    "s_add_u32 s69, s69, 57344\n"
    "s_waitcnt vmcnt(19)\n"
    "v_dot2c_f32_bf16 v2, s66, v80\n"
    "v_dot2c_f32_bf16 v3, s67, v80\n"
    "v_dot2c_f32_bf16 v4, s66, v81\n"
    "v_dot2c_f32_bf16 v5, s67, v81\n"
    "v_dot2c_f32_bf16 v6, s66, v82\n"
    "v_dot2c_f32_bf16 v7, s67, v82\n"
    "v_dot2c_f32_bf16 v8, s66, v83\n"
    "v_dot2c_f32_bf16 v9, s67, v83\n"
    "buffer_load_dwordx4 v[112:115], v75, s[72:75], s69 offen\n"
    "s_add_u32 s69, s69, 57344\n"
    "s_waitcnt vmcnt(17)\n"
    "v_dot2c_f32_bf16 v2, s66, v84\n"
    "v_dot2c_f32_bf16 v3, s67, v84\n"
    "v_dot2c_f32_bf16 v4, s66, v85\n"
    "v_dot2c_f32_bf16 v5, s67, v85\n"
    "v_dot2c_f32_bf16 v6, s66, v86\n"
    "v_dot2c_f32_bf16 v7, s67, v86\n"
    "v_dot2c_f32_bf16 v8, s66, v87\n"
    "v_dot2c_f32_bf16 v9, s67, v87\n"
    "buffer_load_dwordx4 v[116:119], v75, s[72:75], s69 offen\n"
    "s_add_u32 s69, s69, 57344\n"
    "s_waitcnt vmcnt(15)\n"
    "v_dot2c_f32_bf16 v2, s66, v88\n"
    "v_dot2c_f32_bf16 v3, s67, v88\n"
    "v_dot2c_f32_bf16 v4, s66, v89\n"
    "v_dot2c_f32_bf16 v5, s67, v89\n"
    "v_dot2c_f32_bf16 v6, s66, v90\n"
    "v_dot2c_f32_bf16 v7, s67, v90\n"
    "v_dot2c_f32_bf16 v8, s66, v91\n"
    "v_dot2c_f32_bf16 v9, s67, v91\n"
    "buffer_load_dwordx4 v[120:123], v75, s[72:75], s69 offen\n"
    "s_add_u32 s69, s69, 57344\n"
    "s_waitcnt vmcnt(13)\n"
    "v_dot2c_f32_bf16 v2, s66, v92\n"
    "v_dot2c_f32_bf16 v3, s67, v92\n"
    "v_dot2c_f32_bf16 v4, s66, v93\n"
    "v_dot2c_f32_bf16 v5, s67, v93\n"
    "v_dot2c_f32_bf16 v6, s66, v94\n"
    "v_dot2c_f32_bf16 v7, s67, v94\n"
    "v_dot2c_f32_bf16 v8, s66, v95\n"
    "v_dot2c_f32_bf16 v9, s67, v95\n"
    "buffer_load_dwordx4 v[124:127], v75, s[72:75], s69 offen\n"
    "s_add_u32 s69, s69, 57344\n"
    "s_waitcnt vmcnt(11)\n"
    "v_dot2c_f32_bf16 v2, s66, v96\n"
    "v_dot2c_f32_bf16 v3, s67, v96\n"
    "v_dot2c_f32_bf16 v4, s66, v97\n"
    "v_dot2c_f32_bf16 v5, s67, v97\n"
    "v_dot2c_f32_bf16 v6, s66, v98\n"
    "v_dot2c_f32_bf16 v7, s67, v98\n"
    "v_dot2c_f32_bf16 v8, s66, v99\n"
    "v_dot2c_f32_bf16 v9, s67, v99\n"
    "buffer_load_dwordx4 v[128:131], v75, s[72:75], s69 offen\n"
    "s_add_u32 s69, s69, 57344\n"
    "s_waitcnt vmcnt(9)\n"
    "v_dot2c_f32_bf16 v2, s66, v100\n"
    "v_dot2c_f32_bf16 v3, s67, v100\n"
    "v_dot2c_f32_bf16 v4, s66, v101\n"
    "v_dot2c_f32_bf16 v5, s67, v101\n"
    "v_dot2c_f32_bf16 v6, s66, v102\n"
    "v_dot2c_f32_bf16 v7, s67, v102\n"
    "v_dot2c_f32_bf16 v8, s66, v103\n"
    "v_dot2c_f32_bf16 v9, s67, v103\n"
    "buffer_load_dwordx4 v[132:135], v75, s[72:75], s69 offen\n"
    "s_add_u32 s69, s69, 57344\n"
    "buffer_store_dwordx4 v[10:13], v230, s[52:55], s76 offen sc1\n"
    "s_add_u32 s76, s76, 4096\n"
    "s_waitcnt vmcnt(8)\n"
    "v_dot2c_f32_bf16 v2, s66, v104\n"
    "v_dot2c_f32_bf16 v3, s67, v104\n"
    "v_dot2c_f32_bf16 v4, s66, v105\n"
    "v_dot2c_f32_bf16 v5, s67, v105\n"
    "v_dot2c_f32_bf16 v6, s66, v106\n"
    "v_dot2c_f32_bf16 v7, s67, v106\n"
    "v_dot2c_f32_bf16 v8, s66, v107\n"
    "v_dot2c_f32_bf16 v9, s67, v107\n"
    "buffer_load_dwordx4 v[136:139], v75, s[72:75], s69 offen\n"
    "s_add_u32 s69, s69, 57344\n"
    "buffer_store_dwordx4 v[10:13], v230, s[52:55], s76 offen sc1\n"
    "s_add_u32 s76, s76, 4096\n"
    "s_waitcnt vmcnt(9)\n"
    "v_dot2c_f32_bf16 v2, s66, v108\n"
    "v_dot2c_f32_bf16 v3, s67, v108\n"
    "v_dot2c_f32_bf16 v4, s66, v109\n"
    "v_dot2c_f32_bf16 v5, s67, v109\n"
    "v_dot2c_f32_bf16 v6, s66, v110\n"
    "v_dot2c_f32_bf16 v7, s67, v110\n"
    "v_dot2c_f32_bf16 v8, s66, v111\n"
    "v_dot2c_f32_bf16 v9, s67, v111\n"
    "buffer_load_dwordx4 v[140:143], v75, s[72:75], s69 offen\n"
    "s_add_u32 s69, s69, 57344\n"
    "buffer_store_dwordx4 v[10:13], v230, s[52:55], s76 offen sc1\n"
    "s_add_u32 s76, s76, 4096\n"
    "s_waitcnt vmcnt(10)\n"
    "v_dot2c_f32_bf16 v2, s66, v112\n"
    "v_dot2c_f32_bf16 v3, s67, v112\n"
    "v_dot2c_f32_bf16 v4, s66, v113\n"
    "v_dot2c_f32_bf16 v5, s67, v113\n"
    "v_dot2c_f32_bf16 v6, s66, v114\n"
    "v_dot2c_f32_bf16 v7, s67, v114\n"
    "v_dot2c_f32_bf16 v8, s66, v115\n"
    "v_dot2c_f32_bf16 v9, s67, v115\n"
    "buffer_load_dwordx4 v[144:147], v75, s[72:75], s69 offen\n"
    "s_add_u32 s69, s69, 57344\n"
    "buffer_store_dwordx4 v[10:13], v230, s[52:55], s76 offen sc1\n"
    "s_add_u32 s76, s76, 4096\n"
    "s_waitcnt vmcnt(11)\n"
    "v_dot2c_f32_bf16 v2, s66, v116\n"
    "v_dot2c_f32_bf16 v3, s67, v116\n"
    "v_dot2c_f32_bf16 v4, s66, v117\n"
    "v_dot2c_f32_bf16 v5, s67, v117\n"
    "v_dot2c_f32_bf16 v6, s66, v118\n"
    "v_dot2c_f32_bf16 v7, s67, v118\n"
    "v_dot2c_f32_bf16 v8, s66, v119\n"
    "v_dot2c_f32_bf16 v9, s67, v119\n"
    "s_lshl_b32 s64, s68, 7\n"
    "s_add_u32 s64, s64, 4096\n"
    "s_barrier\n"
    "s_mov_b64 exec, 1\n"
    "buffer_store_dword v231, off, s[52:55], s64 offset:0 sc1\n"
    "s_mov_b64 exec, -1\n"
    "buffer_load_dwordx4 v[148:151], v75, s[72:75], s69 offen\n"
    "s_add_u32 s69, s69, 57344\n"
    "buffer_store_dwordx4 v[10:13], v230, s[52:55], s76 offen sc1\n"
    "s_add_u32 s76, s76, 4096\n"
    "s_waitcnt vmcnt(13)\n"
    "v_dot2c_f32_bf16 v2, s66, v120\n"
    "v_dot2c_f32_bf16 v3, s67, v120\n"
    "v_dot2c_f32_bf16 v4, s66, v121\n"
    "v_dot2c_f32_bf16 v5, s67, v121\n"
    "v_dot2c_f32_bf16 v6, s66, v122\n"
    "v_dot2c_f32_bf16 v7, s67, v122\n"
    "v_dot2c_f32_bf16 v8, s66, v123\n"
    "v_dot2c_f32_bf16 v9, s67, v123\n"
    "buffer_load_dwordx4 v[152:155], v75, s[72:75], s69 offen\n"
    "s_add_u32 s69, s69, 57344\n"
    "buffer_store_dwordx4 v[10:13], v230, s[52:55], s76 offen sc1\n"
    "s_add_u32 s76, s76, 4096\n"
    "s_waitcnt vmcnt(14)\n"
    "v_dot2c_f32_bf16 v2, s66, v124\n"
    "v_dot2c_f32_bf16 v3, s67, v124\n"
    "v_dot2c_f32_bf16 v4, s66, v125\n"
    "v_dot2c_f32_bf16 v5, s67, v125\n"
    "v_dot2c_f32_bf16 v6, s66, v126\n"
    "v_dot2c_f32_bf16 v7, s67, v126\n"
    "v_dot2c_f32_bf16 v8, s66, v127\n"
    "v_dot2c_f32_bf16 v9, s67, v127\n"
    "buffer_load_dwordx4 v[156:159], v75, s[72:75], s69 offen\n"
    "s_add_u32 s69, s69, 57344\n"
    "buffer_store_dwordx4 v[10:13], v230, s[52:55], s76 offen sc1\n"
    "s_add_u32 s76, s76, 4096\n"
    "s_waitcnt vmcnt(15)\n"
    "v_dot2c_f32_bf16 v2, s66, v128\n"
    "v_dot2c_f32_bf16 v3, s67, v128\n"
    "v_dot2c_f32_bf16 v4, s66, v129\n"
    "v_dot2c_f32_bf16 v5, s67, v129\n"
    "v_dot2c_f32_bf16 v6, s66, v130\n"
    "v_dot2c_f32_bf16 v7, s67, v130\n"
    "v_dot2c_f32_bf16 v8, s66, v131\n"
    "v_dot2c_f32_bf16 v9, s67, v131\n"
    "buffer_load_dwordx4 v[160:163], v75, s[72:75], s69 offen\n"
    "s_add_u32 s69, s69, 57344\n"
    "buffer_store_dwordx4 v[10:13], v230, s[52:55], s76 offen sc1\n"
    "s_add_u32 s76, s76, 4096\n"
    "s_waitcnt vmcnt(16)\n"
    "v_dot2c_f32_bf16 v2, s66, v132\n"
    "v_dot2c_f32_bf16 v3, s67, v132\n"
    "v_dot2c_f32_bf16 v4, s66, v133\n"
    "v_dot2c_f32_bf16 v5, s67, v133\n"
    "v_dot2c_f32_bf16 v6, s66, v134\n"
    "v_dot2c_f32_bf16 v7, s67, v134\n"
    "v_dot2c_f32_bf16 v8, s66, v135\n"
    "v_dot2c_f32_bf16 v9, s67, v135\n"
    "buffer_load_dwordx4 v[164:167], v75, s[72:75], s69 offen\n"
    "s_add_u32 s69, s69, 57344\n"
    "buffer_store_dwordx4 v[10:13], v230, s[52:55], s76 offen sc1\n"
    "s_add_u32 s76, s76, 4096\n"
    "s_waitcnt vmcnt(16)\n"
    "v_dot2c_f32_bf16 v2, s66, v136\n"
    "v_dot2c_f32_bf16 v3, s67, v136\n"
    "v_dot2c_f32_bf16 v4, s66, v137\n"
    "v_dot2c_f32_bf16 v5, s67, v137\n"
    "v_dot2c_f32_bf16 v6, s66, v138\n"
    "v_dot2c_f32_bf16 v7, s67, v138\n"
    "v_dot2c_f32_bf16 v8, s66, v139\n"
    "v_dot2c_f32_bf16 v9, s67, v139\n"
    "buffer_load_dwordx4 v[168:171], v75, s[72:75], s69 offen\n"
    "s_add_u32 s69, s69, 57344\n"
    "buffer_store_dwordx4 v[10:13], v230, s[52:55], s76 offen sc1\n"
    "s_add_u32 s76, s76, 4096\n"
    "s_waitcnt vmcnt(16)\n"
    "v_dot2c_f32_bf16 v2, s66, v140\n"
    "v_dot2c_f32_bf16 v3, s67, v140\n"
    "v_dot2c_f32_bf16 v4, s66, v141\n"
    "v_dot2c_f32_bf16 v5, s67, v141\n"
    "v_dot2c_f32_bf16 v6, s66, v142\n"
    "v_dot2c_f32_bf16 v7, s67, v142\n"
    "v_dot2c_f32_bf16 v8, s66, v143\n"
    "v_dot2c_f32_bf16 v9, s67, v143\n"
    "buffer_load_dwordx4 v[172:175], v75, s[72:75], s69 offen\n"
    "s_add_u32 s69, s69, 57344\n"
    "buffer_store_dwordx4 v[10:13], v230, s[52:55], s76 offen sc1\n"
    "s_add_u32 s76, s76, 4096\n"
    "s_waitcnt vmcnt(16)\n"
    "v_dot2c_f32_bf16 v2, s66, v144\n"
    "v_dot2c_f32_bf16 v3, s67, v144\n"
    "v_dot2c_f32_bf16 v4, s66, v145\n"
    "v_dot2c_f32_bf16 v5, s67, v145\n"
    "v_dot2c_f32_bf16 v6, s66, v146\n"
    "v_dot2c_f32_bf16 v7, s67, v146\n"
    "v_dot2c_f32_bf16 v8, s66, v147\n"
    "v_dot2c_f32_bf16 v9, s67, v147\n"
    "buffer_load_dwordx4 v[176:179], v75, s[72:75], s69 offen\n"
    "s_add_u32 s69, s69, 57344\n"
    "buffer_store_dwordx4 v[10:13], v230, s[52:55], s76 offen sc1\n"
    "s_add_u32 s76, s76, 4096\n"
    "s_waitcnt vmcnt(15)\n"
    "v_dot2c_f32_bf16 v2, s66, v148\n"
    "v_dot2c_f32_bf16 v3, s67, v148\n"
    "v_dot2c_f32_bf16 v4, s66, v149\n"
    "v_dot2c_f32_bf16 v5, s67, v149\n"
    "v_dot2c_f32_bf16 v6, s66, v150\n"
    "v_dot2c_f32_bf16 v7, s67, v150\n"
    "v_dot2c_f32_bf16 v8, s66, v151\n"
    "v_dot2c_f32_bf16 v9, s67, v151\n"
    "buffer_load_dwordx4 v[180:183], v75, s[72:75], s69 offen\n"
    "s_add_u32 s69, s69, 57344\n"
    "buffer_store_dwordx4 v[10:13], v230, s[52:55], s76 offen sc1\n"
    "s_add_u32 s76, s76, 4096\n"
    "s_waitcnt vmcnt(15)\n"
    "v_dot2c_f32_bf16 v2, s66, v152\n"
    "v_dot2c_f32_bf16 v3, s67, v152\n"
    "v_dot2c_f32_bf16 v4, s66, v153\n"
    "v_dot2c_f32_bf16 v5, s67, v153\n"
    "v_dot2c_f32_bf16 v6, s66, v154\n"
    "v_dot2c_f32_bf16 v7, s67, v154\n"
    "v_dot2c_f32_bf16 v8, s66, v155\n"
    "v_dot2c_f32_bf16 v9, s67, v155\n"
    "buffer_load_dwordx4 v[184:187], v75, s[72:75], s69 offen\n"
    "buffer_store_dwordx4 v[10:13], v230, s[52:55], s76 offen sc1\n"
    "s_add_u32 s76, s76, 4096\n"
    "s_waitcnt vmcnt(15)\n"
    "v_dot2c_f32_bf16 v2, s66, v156\n"
    "v_dot2c_f32_bf16 v3, s67, v156\n"
    "v_dot2c_f32_bf16 v4, s66, v157\n"
    "v_dot2c_f32_bf16 v5, s67, v157\n"
    "v_dot2c_f32_bf16 v6, s66, v158\n"
    "v_dot2c_f32_bf16 v7, s67, v158\n"
    "v_dot2c_f32_bf16 v8, s66, v159\n"
    "v_dot2c_f32_bf16 v9, s67, v159\n"
    "buffer_store_dwordx4 v[10:13], v230, s[52:55], s76 offen sc1\n"
    "s_add_u32 s76, s76, 4096\n"
    "s_waitcnt vmcnt(14)\n"
    "v_dot2c_f32_bf16 v2, s66, v160\n"
    "v_dot2c_f32_bf16 v3, s67, v160\n"
    "v_dot2c_f32_bf16 v4, s66, v161\n"
    "v_dot2c_f32_bf16 v5, s67, v161\n"
    "v_dot2c_f32_bf16 v6, s66, v162\n"
    "v_dot2c_f32_bf16 v7, s67, v162\n"
    "v_dot2c_f32_bf16 v8, s66, v163\n"
    "v_dot2c_f32_bf16 v9, s67, v163\n"
    "buffer_store_dwordx4 v[10:13], v230, s[52:55], s76 offen sc1\n"
    "s_add_u32 s76, s76, 4096\n"
    "s_waitcnt vmcnt(13)\n"
    "v_dot2c_f32_bf16 v2, s66, v164\n"
    "v_dot2c_f32_bf16 v3, s67, v164\n"
    "v_dot2c_f32_bf16 v4, s66, v165\n"
    "v_dot2c_f32_bf16 v5, s67, v165\n"
    "v_dot2c_f32_bf16 v6, s66, v166\n"
    "v_dot2c_f32_bf16 v7, s67, v166\n"
    "v_dot2c_f32_bf16 v8, s66, v167\n"
    "v_dot2c_f32_bf16 v9, s67, v167\n"
    "buffer_store_dwordx4 v[10:13], v230, s[52:55], s76 offen sc1\n"
    "s_add_u32 s76, s76, 4096\n"
    "s_waitcnt vmcnt(12)\n"
    "v_dot2c_f32_bf16 v2, s66, v168\n"
    "v_dot2c_f32_bf16 v3, s67, v168\n"
    "v_dot2c_f32_bf16 v4, s66, v169\n"
    "v_dot2c_f32_bf16 v5, s67, v169\n"
    "v_dot2c_f32_bf16 v6, s66, v170\n"
    "v_dot2c_f32_bf16 v7, s67, v170\n"
    "v_dot2c_f32_bf16 v8, s66, v171\n"
    "v_dot2c_f32_bf16 v9, s67, v171\n"
    "buffer_store_dwordx4 v[10:13], v230, s[52:55], s76 offen sc1\n"
    "s_add_u32 s76, s76, 4096\n"
    "s_waitcnt vmcnt(11)\n"
    "v_dot2c_f32_bf16 v2, s66, v172\n"
    "v_dot2c_f32_bf16 v3, s67, v172\n"
    "v_dot2c_f32_bf16 v4, s66, v173\n"
    "v_dot2c_f32_bf16 v5, s67, v173\n"
    "v_dot2c_f32_bf16 v6, s66, v174\n"
    "v_dot2c_f32_bf16 v7, s67, v174\n"
    "v_dot2c_f32_bf16 v8, s66, v175\n"
    "v_dot2c_f32_bf16 v9, s67, v175\n"
    "buffer_store_dwordx4 v[10:13], v230, s[52:55], s76 offen sc1\n"
    "s_add_u32 s76, s76, 4096\n"
    "s_waitcnt vmcnt(10)\n"
    "v_dot2c_f32_bf16 v2, s66, v176\n"
    "v_dot2c_f32_bf16 v3, s67, v176\n"
    "v_dot2c_f32_bf16 v4, s66, v177\n"
    "v_dot2c_f32_bf16 v5, s67, v177\n"
    "v_dot2c_f32_bf16 v6, s66, v178\n"
    "v_dot2c_f32_bf16 v7, s67, v178\n"
    "v_dot2c_f32_bf16 v8, s66, v179\n"
    "v_dot2c_f32_bf16 v9, s67, v179\n"
    "buffer_store_dwordx4 v[10:13], v230, s[52:55], s76 offen sc1\n"
    "s_add_u32 s76, s76, 4096\n"
    "s_waitcnt vmcnt(9)\n"
    "v_dot2c_f32_bf16 v2, s66, v180\n"
    "v_dot2c_f32_bf16 v3, s67, v180\n"
    "v_dot2c_f32_bf16 v4, s66, v181\n"
    "v_dot2c_f32_bf16 v5, s67, v181\n"
    "v_dot2c_f32_bf16 v6, s66, v182\n"
    "v_dot2c_f32_bf16 v7, s67, v182\n"
    "v_dot2c_f32_bf16 v8, s66, v183\n"
    "v_dot2c_f32_bf16 v9, s67, v183\n"
    "buffer_store_dwordx4 v[10:13], v230, s[52:55], s76 offen sc1\n"
    "s_add_u32 s76, s76, 4096\n"
    "s_waitcnt vmcnt(8)\n"
    "v_dot2c_f32_bf16 v2, s66, v184\n"
    "v_dot2c_f32_bf16 v3, s67, v184\n"
    "v_dot2c_f32_bf16 v4, s66, v185\n"
    "v_dot2c_f32_bf16 v5, s67, v185\n"
    "v_dot2c_f32_bf16 v6, s66, v186\n"
    "v_dot2c_f32_bf16 v7, s67, v186\n"
    "v_dot2c_f32_bf16 v8, s66, v187\n"
    "v_dot2c_f32_bf16 v9, s67, v187\n"
    "v_lshlrev_b32_e64 v226, 5, v0\n"
    "v_add_u32 v226, 90112, v226\n"
    "v_and_b32 v227, 127, v0\n"
    "v_lshlrev_b32_e64 v228, 2, v227\n"
    "v_add_u32 v228, 90112, v228\n"
    "ds_write_b128 v226, v[2:5]\n"
    "ds_write_b128 v226, v[6:9] offset:16\n"
    "s_waitcnt lgkmcnt(0)\n"
    "s_barrier\n"
    "ds_read2st64_b32 v[76:77], v228 offset0:0 offset1:2\n"
    "ds_read2st64_b32 v[78:79], v228 offset0:4 offset1:6\n"
    "ds_read2st64_b32 v[80:81], v228 offset0:8 offset1:10\n"
    "ds_read2st64_b32 v[82:83], v228 offset0:12 offset1:14\n"
    "ds_read2st64_b32 v[84:85], v228 offset0:16 offset1:18\n"
    "ds_read2st64_b32 v[86:87], v228 offset0:20 offset1:22\n"
    "ds_read2st64_b32 v[88:89], v228 offset0:24 offset1:26\n"
    "ds_read2st64_b32 v[90:91], v228 offset0:28 offset1:30\n"
    "s_waitcnt lgkmcnt(3)\n"
    "v_add_f32 v76, v76, v84\n"
    "v_add_f32 v77, v77, v85\n"
    "s_waitcnt lgkmcnt(2)\n"
    "v_add_f32 v78, v78, v86\n"
    "v_add_f32 v79, v79, v87\n"
    "s_waitcnt lgkmcnt(1)\n"
    "v_add_f32 v80, v80, v88\n"
    "v_add_f32 v81, v81, v89\n"
    "s_waitcnt lgkmcnt(0)\n"
    "v_add_f32 v82, v82, v90\n"
    "v_add_f32 v83, v83, v91\n"
    "v_add_f32 v76, v76, v80\n"
    "v_add_f32 v77, v77, v81\n"
    "v_add_f32 v78, v78, v82\n"
    "v_add_f32 v79, v79, v83\n"
    "v_add_f32 v76, v76, v78\n"
    "v_add_f32 v77, v77, v79\n"
    "v_add_f32 v76, v76, v77\n"
    "v_mov_b32_e64 v77, s60\n"
    "s_cmp_lt_u32 s68, 28\n"
    "s_cselect_b32 s66, 128, 0\n"
    "v_cmp_gt_u32 vcc, s66, v0\n"
    "v_lshlrev_b32 v227, 3, v227\n"
    "v_mov_b32 v224, 1073741824\n"
    "v_cndmask_b32_e64 v227, v224, v227, vcc\n"
    "s_and_b32 s64, s68, 1\n"
    "s_mul_i32 s64, s64, 14336\n"
    "s_lshr_b32 s65, s68, 1\n"
    "s_lshl_b32 s65, s65, 10\n"
    "s_add_u32 s64, s64, s65\n"
    "s_nop 0\n"
    "s_add_u32 s64, s64, 8585216\n"
    "buffer_store_dwordx2 v[76:77], v227, s[52:55], s64 offen sc1\n"
    "buffer_store_dwordx4 v[10:13], v230, s[52:55], s76 offen sc1\n"
    "s_add_u32 s76, s76, 4096\n"
    "buffer_store_dwordx4 v[10:13], v230, s[52:55], s76 offen sc1\n"
    "s_add_u32 s76, s76, 4096\n"
    "buffer_store_dwordx4 v[10:13], v230, s[52:55], s76 offen sc1\n"
    "s_add_u32 s76, s76, 4096\n"
    "buffer_store_dwordx4 v[10:13], v230, s[52:55], s76 offen sc1\n"
    "s_add_u32 s76, s76, 4096\n"
    "buffer_store_dwordx4 v[10:13], v230, s[52:55], s76 offen sc1\n"
    "s_add_u32 s76, s76, 4096\n"
    "buffer_store_dwordx4 v[10:13], v230, s[52:55], s76 offen sc1\n"
    "s_add_u32 s76, s76, 4096\n"
    "buffer_store_dwordx4 v[10:13], v230, s[52:55], s76 offen sc1\n"
    "s_add_u32 s76, s76, 4096\n"
    "s_mov_b32 s64, 32768\n"
    "buffer_load_dwordx4 v[92:95], v230, s[52:55], s64 offen sc1\n"
    "s_mov_b32 s64, 36864\n"
    "buffer_load_dwordx4 v[96:99], v230, s[52:55], s64 offen sc1\n"
    "s_mov_b32 s64, 40960\n"
    "buffer_load_dwordx4 v[100:103], v230, s[52:55], s64 offen sc1\n"
    "s_mov_b32 s64, 45056\n"
    "buffer_load_dwordx4 v[104:107], v230, s[52:55], s64 offen sc1\n"
    "s_mov_b32 s64, 49152\n"
    "buffer_load_dwordx4 v[108:111], v230, s[52:55], s64 offen sc1\n"
    "s_mov_b32 s64, 53248\n"
    "buffer_load_dwordx4 v[112:115], v230, s[52:55], s64 offen sc1\n"
    "s_mov_b32 s64, 57344\n"
    "buffer_load_dwordx4 v[116:119], v230, s[52:55], s64 offen sc1\n"
    "s_mov_b32 s64, 61440\n"
    "buffer_load_dwordx4 v[120:123], v230, s[52:55], s64 offen sc1\n"
    "s_waitcnt vmcnt(6)\n"
    "v_min3_u32 v124, v93, v95, v97\n"
    "v_max3_u32 v125, v93, v95, v97\n"
    "s_waitcnt vmcnt(5)\n"
    "v_min3_u32 v124, v124, v99, v101\n"
    "v_max3_u32 v125, v125, v99, v101\n"
    "s_waitcnt vmcnt(4)\n"
    "v_min3_u32 v124, v124, v103, v105\n"
    "v_max3_u32 v125, v125, v103, v105\n"
    "s_waitcnt vmcnt(3)\n"
    "v_min3_u32 v124, v124, v107, v109\n"
    "v_max3_u32 v125, v125, v107, v109\n"
    "s_waitcnt vmcnt(2)\n"
    "v_min3_u32 v124, v124, v111, v113\n"
    "v_max3_u32 v125, v125, v111, v113\n"
    "s_waitcnt vmcnt(1)\n"
    "v_min3_u32 v124, v124, v115, v117\n"
    "v_max3_u32 v125, v125, v115, v117\n"
    "s_waitcnt vmcnt(0)\n"
    "v_min3_u32 v124, v124, v119, v121\n"
    "v_max3_u32 v125, v125, v119, v121\n"
    "v_min_u32 v124, v124, v123\n"
    "v_max_u32 v125, v125, v123\n"
    "v_xor_b32 v124, s60, v124\n"
    "v_xor_b32 v125, s60, v125\n"
    "v_or_b32 v124, v124, v125\n"
    "v_cmp_ne_u32_e64 vcc, 0, v124\n"
    "s_cbranch_vccnz Lslow_sprs_%=\n"
    "Lback_sprs_%=:\n"
    "s_lshl_b32 s64, s68, 7\n"
    "s_add_u32 s64, s64, 8192\n"
    "s_waitcnt vmcnt(0)\n"
    "s_barrier\n"
    "s_mov_b64 exec, 1\n"
    "buffer_store_dword v231, off, s[52:55], s64 offset:0 sc1\n"
    "s_mov_b64 exec, -1\n"
    "s_and_b32 s64, s58, 1\n"
    "s_lshl_b32 s64, s64, 4\n"
    "s_and_b32 s65, s58, 6\n"
    "s_lshl1_add_u32 s64, s65, s64\n"
    "s_sub_u32 s65, s59, 28\n"
    "s_add_u32 s64, s64, s65\n"
    "s_lshl_b32 s70, s64, 9\n"
    "s_add_u32 s70, s70, 65536\n"
    "s_mul_i32 s69, s64, 28672\n"
    "s_mul_i32 s65, s57, 7168\n"
    "s_add_u32 s69, s69, s65\n"
    "s_add_u32 s69, s69, 8613888\n"
    "v_add_u32 v140, 4096, v74\n"
    "v_add_u32 v141, s65, v74\n"
    "v_lshrrev_b32 v226, 5, v222\n"
    "v_bfe_u32 v227, v222, 2, 2\n"
    "v_lshl_add_u32 v226, v226, 3, v227\n"
    "v_bfe_u32 v227, v222, 4, 1\n"
    "v_lshlrev_b32_e64 v227, 5, v227\n"
    "v_lshl_add_u32 v226, v226, 6, v227\n"
    "v_and_b32_e64 v227, 3, v222\n"
    "v_lshl_add_u32 v226, v227, 3, v226\n"
    "v_add_u32 v142, s65, v226\n"
    "v_lshlrev_b32 v144, 2, v222\n"
    "v_add_u32 v144, 32768, v144\n"
    "s_lshl_b32 s65, s57, 8\n"
    "v_add_u32 v143, s65, v144\n"
    "v_cmp_gt_u32 vcc, 32, v0\n"
    "v_mov_b32 v224, 1073741824\n"
    "v_mov_b32 v149, s60\n"
    "v_mov_b32 v151, s60\n"
    "v_cndmask_b32_e64 v145, v224, v230, vcc\n"
    "buffer_load_dwordx4 v[76:79], v74, s[52:55], s69 offen offset:0 sc1\n"
    "buffer_load_dwordx4 v[80:83], v74, s[52:55], s69 offen offset:1024 sc1\n"
    "buffer_load_dwordx4 v[84:87], v74, s[52:55], s69 offen offset:2048 sc1\n"
    "buffer_load_dwordx4 v[88:91], v74, s[52:55], s69 offen offset:3072 sc1\n"
    "buffer_load_dwordx4 v[92:95], v140, s[52:55], s69 offen offset:0 sc1\n"
    "buffer_load_dwordx4 v[96:99], v140, s[52:55], s69 offen offset:1024 sc1\n"
    "buffer_load_dwordx4 v[100:103], v140, s[52:55], s69 offen offset:2048 sc1\n"
    "s_waitcnt vmcnt(6)\n"
    "v_max3_u32 v132, v76, v77, v78\n"
    "s_waitcnt vmcnt(5)\n"
    "v_max3_u32 v132, v132, v79, v80\n"
    "v_max3_u32 v132, v132, v81, v82\n"
    "s_waitcnt vmcnt(4)\n"
    "v_max3_u32 v132, v132, v83, v84\n"
    "v_max3_u32 v132, v132, v85, v86\n"
    "s_waitcnt vmcnt(3)\n"
    "v_max3_u32 v132, v132, v87, v88\n"
    "v_max3_u32 v132, v132, v89, v90\n"
    "s_waitcnt vmcnt(2)\n"
    "v_max3_u32 v132, v132, v91, v92\n"
    "v_max3_u32 v132, v132, v93, v94\n"
    "s_waitcnt vmcnt(1)\n"
    "v_max3_u32 v132, v132, v95, v96\n"
    "v_max3_u32 v132, v132, v97, v98\n"
    "s_waitcnt vmcnt(0)\n"
    "v_max3_u32 v132, v132, v99, v100\n"
    "v_max3_u32 v132, v132, v101, v102\n"
    "v_max_u32 v132, v132, v103\n"
    "v_cmp_eq_u32 vcc, -1, v132\n"
    "s_cbranch_vccnz Lslow_sptile0_%=\n"
    "Lback_sptile0_%=:\n"
    "ds_write_b128 v141, v[76:79] offset:0\n"
    "ds_write_b128 v141, v[80:83] offset:1024\n"
    "ds_write_b128 v141, v[84:87] offset:2048\n"
    "ds_write_b128 v141, v[88:91] offset:3072\n"
    "ds_write_b128 v141, v[92:95] offset:4096\n"
    "ds_write_b128 v141, v[96:99] offset:5120\n"
    "ds_write_b128 v141, v[100:103] offset:6144\n"
    "ds_read_b64_tr_b16 v[104:105], v142 offset:0\n"
    "ds_read_b64_tr_b16 v[106:107], v142 offset:256\n"
    "ds_read_b64_tr_b16 v[108:109], v142 offset:1024\n"
    "ds_read_b64_tr_b16 v[110:111], v142 offset:1280\n"
    "ds_read_b64_tr_b16 v[112:113], v142 offset:2048\n"
    "ds_read_b64_tr_b16 v[114:115], v142 offset:2304\n"
    "ds_read_b64_tr_b16 v[116:117], v142 offset:3072\n"
    "ds_read_b64_tr_b16 v[118:119], v142 offset:3328\n"
    "ds_read_b64_tr_b16 v[120:121], v142 offset:4096\n"
    "ds_read_b64_tr_b16 v[122:123], v142 offset:4352\n"
    "ds_read_b64_tr_b16 v[124:125], v142 offset:5120\n"
    "ds_read_b64_tr_b16 v[126:127], v142 offset:5376\n"
    "ds_read_b64_tr_b16 v[128:129], v142 offset:6144\n"
    "ds_read_b64_tr_b16 v[130:131], v142 offset:6400\n"
    "v_mov_b32 v134, 0\n"
    "v_mov_b32_e64 v135, 0\n"
    "s_waitcnt lgkmcnt(13)\n"
    "v_dot2c_f32_bf16 v134, 0x3f803f80, v104\n"
    "v_dot2c_f32_bf16 v135, v104, v104\n"
    "v_dot2c_f32_bf16 v134, 0x3f803f80, v105\n"
    "v_dot2c_f32_bf16 v135, v105, v105\n"
    "s_waitcnt lgkmcnt(12)\n"
    "v_dot2c_f32_bf16 v134, 0x3f803f80, v106\n"
    "v_dot2c_f32_bf16 v135, v106, v106\n"
    "v_dot2c_f32_bf16 v134, 0x3f803f80, v107\n"
    "v_dot2c_f32_bf16 v135, v107, v107\n"
    "s_waitcnt lgkmcnt(11)\n"
    "v_dot2c_f32_bf16 v134, 0x3f803f80, v108\n"
    "v_dot2c_f32_bf16 v135, v108, v108\n"
    "v_dot2c_f32_bf16 v134, 0x3f803f80, v109\n"
    "v_dot2c_f32_bf16 v135, v109, v109\n"
    "s_waitcnt lgkmcnt(10)\n"
    "v_dot2c_f32_bf16 v134, 0x3f803f80, v110\n"
    "v_dot2c_f32_bf16 v135, v110, v110\n"
    "v_dot2c_f32_bf16 v134, 0x3f803f80, v111\n"
    "v_dot2c_f32_bf16 v135, v111, v111\n"
    "s_waitcnt lgkmcnt(9)\n"
    "v_dot2c_f32_bf16 v134, 0x3f803f80, v112\n"
    "v_dot2c_f32_bf16 v135, v112, v112\n"
    "v_dot2c_f32_bf16 v134, 0x3f803f80, v113\n"
    "v_dot2c_f32_bf16 v135, v113, v113\n"
    "s_waitcnt lgkmcnt(8)\n"
    "v_dot2c_f32_bf16 v134, 0x3f803f80, v114\n"
    "v_dot2c_f32_bf16 v135, v114, v114\n"
    "v_dot2c_f32_bf16 v134, 0x3f803f80, v115\n"
    "v_dot2c_f32_bf16 v135, v115, v115\n"
    "s_waitcnt lgkmcnt(7)\n"
    "v_dot2c_f32_bf16 v134, 0x3f803f80, v116\n"
    "v_dot2c_f32_bf16 v135, v116, v116\n"
    "v_dot2c_f32_bf16 v134, 0x3f803f80, v117\n"
    "v_dot2c_f32_bf16 v135, v117, v117\n"
    "s_waitcnt lgkmcnt(6)\n"
    "v_dot2c_f32_bf16 v134, 0x3f803f80, v118\n"
    "v_dot2c_f32_bf16 v135, v118, v118\n"
    "v_dot2c_f32_bf16 v134, 0x3f803f80, v119\n"
    "v_dot2c_f32_bf16 v135, v119, v119\n"
    "s_waitcnt lgkmcnt(5)\n"
    "v_dot2c_f32_bf16 v134, 0x3f803f80, v120\n"
    "v_dot2c_f32_bf16 v135, v120, v120\n"
    "v_dot2c_f32_bf16 v134, 0x3f803f80, v121\n"
    "v_dot2c_f32_bf16 v135, v121, v121\n"
    "s_waitcnt lgkmcnt(4)\n"
    "v_dot2c_f32_bf16 v134, 0x3f803f80, v122\n"
    "v_dot2c_f32_bf16 v135, v122, v122\n"
    "v_dot2c_f32_bf16 v134, 0x3f803f80, v123\n"
    "v_dot2c_f32_bf16 v135, v123, v123\n"
    "s_waitcnt lgkmcnt(3)\n"
    "v_dot2c_f32_bf16 v134, 0x3f803f80, v124\n"
    "v_dot2c_f32_bf16 v135, v124, v124\n"
    "v_dot2c_f32_bf16 v134, 0x3f803f80, v125\n"
    "v_dot2c_f32_bf16 v135, v125, v125\n"
    "s_waitcnt lgkmcnt(2)\n"
    "v_dot2c_f32_bf16 v134, 0x3f803f80, v126\n"
    "v_dot2c_f32_bf16 v135, v126, v126\n"
    "v_dot2c_f32_bf16 v134, 0x3f803f80, v127\n"
    "v_dot2c_f32_bf16 v135, v127, v127\n"
    "s_waitcnt lgkmcnt(1)\n"
    "v_dot2c_f32_bf16 v134, 0x3f803f80, v128\n"
    "v_dot2c_f32_bf16 v135, v128, v128\n"
    "v_dot2c_f32_bf16 v134, 0x3f803f80, v129\n"
    "v_dot2c_f32_bf16 v135, v129, v129\n"
    "s_waitcnt lgkmcnt(0)\n"
    "v_dot2c_f32_bf16 v134, 0x3f803f80, v130\n"
    "v_dot2c_f32_bf16 v135, v130, v130\n"
    "v_dot2c_f32_bf16 v134, 0x3f803f80, v131\n"
    "v_dot2c_f32_bf16 v135, v131, v131\n"
    "s_nop 2\n"
    "v_permlane32_swap_b32 v134, v135\n"
    "v_add_f32 v134, v134, v135\n"
    "ds_write_b32 v143, v134 offset:0\n"
    "s_waitcnt lgkmcnt(0)\n"
    "s_barrier\n"
    "ds_read2st64_b32 v[136:137], v144 offset0:0 offset1:1\n"
    "ds_read2st64_b32 v[138:139], v144 offset0:2 offset1:3\n"
    "v_mov_b32 v150, 0x3727c5ac\n"
    "s_waitcnt lgkmcnt(1)\n"
    "v_add_f32 v136, v136, v137\n"
    "s_waitcnt lgkmcnt(0)\n"
    "v_add_f32 v138, v138, v139\n"
    "v_add_f32 v136, v136, v138\n"
    "v_mov_b32 v137, v136\n"
    "s_nop 1\n"
    "v_permlane32_swap_b32 v136, v137\n"
    "v_mul_f32 v148, 0xbb124925, v136\n"
    "v_fmac_f32 v150, 0x3b124925, v137\n"
    "v_fma_f32 v150, -v148, v148, v150\n"
    "v_max_f32 v150, 0x3727c5ac, v150\n"
    "buffer_store_dwordx4 v[148:151], v145, s[52:55], s70 offen sc1\n"
    "s_add_u32 s70, s70, 16384\n"
    "s_add_u32 s69, s69, 917504\n"
    "buffer_load_dwordx4 v[76:79], v74, s[52:55], s69 offen offset:0 sc1\n"
    "buffer_load_dwordx4 v[80:83], v74, s[52:55], s69 offen offset:1024 sc1\n"
    "buffer_load_dwordx4 v[84:87], v74, s[52:55], s69 offen offset:2048 sc1\n"
    "buffer_load_dwordx4 v[88:91], v74, s[52:55], s69 offen offset:3072 sc1\n"
    "buffer_load_dwordx4 v[92:95], v140, s[52:55], s69 offen offset:0 sc1\n"
    "buffer_load_dwordx4 v[96:99], v140, s[52:55], s69 offen offset:1024 sc1\n"
    "buffer_load_dwordx4 v[100:103], v140, s[52:55], s69 offen offset:2048 sc1\n"
    "s_waitcnt vmcnt(6)\n"
    "v_max3_u32 v132, v76, v77, v78\n"
    "s_waitcnt vmcnt(5)\n"
    "v_max3_u32 v132, v132, v79, v80\n"
    "v_max3_u32 v132, v132, v81, v82\n"
    "s_waitcnt vmcnt(4)\n"
    "v_max3_u32 v132, v132, v83, v84\n"
    "v_max3_u32 v132, v132, v85, v86\n"
    "s_waitcnt vmcnt(3)\n"
    "v_max3_u32 v132, v132, v87, v88\n"
    "v_max3_u32 v132, v132, v89, v90\n"
    "s_waitcnt vmcnt(2)\n"
    "v_max3_u32 v132, v132, v91, v92\n"
    "v_max3_u32 v132, v132, v93, v94\n"
    "s_waitcnt vmcnt(1)\n"
    "v_max3_u32 v132, v132, v95, v96\n"
    "v_max3_u32 v132, v132, v97, v98\n"
    "s_waitcnt vmcnt(0)\n"
    "v_max3_u32 v132, v132, v99, v100\n"
    "v_max3_u32 v132, v132, v101, v102\n"
    "v_max_u32 v132, v132, v103\n"
    "v_cmp_eq_u32 vcc, -1, v132\n"
    "s_cbranch_vccnz Lslow_sptile1_%=\n"
    "Lback_sptile1_%=:\n"
    "ds_write_b128 v141, v[76:79] offset:0\n"
    "ds_write_b128 v141, v[80:83] offset:1024\n"
    "ds_write_b128 v141, v[84:87] offset:2048\n"
    "ds_write_b128 v141, v[88:91] offset:3072\n"
    "ds_write_b128 v141, v[92:95] offset:4096\n"
    "ds_write_b128 v141, v[96:99] offset:5120\n"
    "ds_write_b128 v141, v[100:103] offset:6144\n"
    "ds_read_b64_tr_b16 v[104:105], v142 offset:0\n"
    "ds_read_b64_tr_b16 v[106:107], v142 offset:256\n"
    "ds_read_b64_tr_b16 v[108:109], v142 offset:1024\n"
    "ds_read_b64_tr_b16 v[110:111], v142 offset:1280\n"
    "ds_read_b64_tr_b16 v[112:113], v142 offset:2048\n"
    "ds_read_b64_tr_b16 v[114:115], v142 offset:2304\n"
    "ds_read_b64_tr_b16 v[116:117], v142 offset:3072\n"
    "ds_read_b64_tr_b16 v[118:119], v142 offset:3328\n"
    "ds_read_b64_tr_b16 v[120:121], v142 offset:4096\n"
    "ds_read_b64_tr_b16 v[122:123], v142 offset:4352\n"
    "ds_read_b64_tr_b16 v[124:125], v142 offset:5120\n"
    "ds_read_b64_tr_b16 v[126:127], v142 offset:5376\n"
    "ds_read_b64_tr_b16 v[128:129], v142 offset:6144\n"
    "ds_read_b64_tr_b16 v[130:131], v142 offset:6400\n"
    "v_mov_b32 v134, 0\n"
    "v_mov_b32_e64 v135, 0\n"
    "s_waitcnt lgkmcnt(13)\n"
    "v_dot2c_f32_bf16 v134, 0x3f803f80, v104\n"
    "v_dot2c_f32_bf16 v135, v104, v104\n"
    "v_dot2c_f32_bf16 v134, 0x3f803f80, v105\n"
    "v_dot2c_f32_bf16 v135, v105, v105\n"
    "s_waitcnt lgkmcnt(12)\n"
    "v_dot2c_f32_bf16 v134, 0x3f803f80, v106\n"
    "v_dot2c_f32_bf16 v135, v106, v106\n"
    "v_dot2c_f32_bf16 v134, 0x3f803f80, v107\n"
    "v_dot2c_f32_bf16 v135, v107, v107\n"
    "s_waitcnt lgkmcnt(11)\n"
    "v_dot2c_f32_bf16 v134, 0x3f803f80, v108\n"
    "v_dot2c_f32_bf16 v135, v108, v108\n"
    "v_dot2c_f32_bf16 v134, 0x3f803f80, v109\n"
    "v_dot2c_f32_bf16 v135, v109, v109\n"
    "s_waitcnt lgkmcnt(10)\n"
    "v_dot2c_f32_bf16 v134, 0x3f803f80, v110\n"
    "v_dot2c_f32_bf16 v135, v110, v110\n"
    "v_dot2c_f32_bf16 v134, 0x3f803f80, v111\n"
    "v_dot2c_f32_bf16 v135, v111, v111\n"
    "s_waitcnt lgkmcnt(9)\n"
    "v_dot2c_f32_bf16 v134, 0x3f803f80, v112\n"
    "v_dot2c_f32_bf16 v135, v112, v112\n"
    "v_dot2c_f32_bf16 v134, 0x3f803f80, v113\n"
    "v_dot2c_f32_bf16 v135, v113, v113\n"
    "s_waitcnt lgkmcnt(8)\n"
    "v_dot2c_f32_bf16 v134, 0x3f803f80, v114\n"
    "v_dot2c_f32_bf16 v135, v114, v114\n"
    "v_dot2c_f32_bf16 v134, 0x3f803f80, v115\n"
    "v_dot2c_f32_bf16 v135, v115, v115\n"
    "s_waitcnt lgkmcnt(7)\n"
    "v_dot2c_f32_bf16 v134, 0x3f803f80, v116\n"
    "v_dot2c_f32_bf16 v135, v116, v116\n"
    "v_dot2c_f32_bf16 v134, 0x3f803f80, v117\n"
    "v_dot2c_f32_bf16 v135, v117, v117\n"
    "s_waitcnt lgkmcnt(6)\n"
    "v_dot2c_f32_bf16 v134, 0x3f803f80, v118\n"
    "v_dot2c_f32_bf16 v135, v118, v118\n"
    "v_dot2c_f32_bf16 v134, 0x3f803f80, v119\n"
    "v_dot2c_f32_bf16 v135, v119, v119\n"
    "s_waitcnt lgkmcnt(5)\n"
    "v_dot2c_f32_bf16 v134, 0x3f803f80, v120\n"
    "v_dot2c_f32_bf16 v135, v120, v120\n"
    "v_dot2c_f32_bf16 v134, 0x3f803f80, v121\n"
    "v_dot2c_f32_bf16 v135, v121, v121\n"
    "s_waitcnt lgkmcnt(4)\n"
    "v_dot2c_f32_bf16 v134, 0x3f803f80, v122\n"
    "v_dot2c_f32_bf16 v135, v122, v122\n"
    "v_dot2c_f32_bf16 v134, 0x3f803f80, v123\n"
    "v_dot2c_f32_bf16 v135, v123, v123\n"
    "s_waitcnt lgkmcnt(3)\n"
    "v_dot2c_f32_bf16 v134, 0x3f803f80, v124\n"
    "v_dot2c_f32_bf16 v135, v124, v124\n"
    "v_dot2c_f32_bf16 v134, 0x3f803f80, v125\n"
    "v_dot2c_f32_bf16 v135, v125, v125\n"
    "s_waitcnt lgkmcnt(2)\n"
    "v_dot2c_f32_bf16 v134, 0x3f803f80, v126\n"
    "v_dot2c_f32_bf16 v135, v126, v126\n"
    "v_dot2c_f32_bf16 v134, 0x3f803f80, v127\n"
    "v_dot2c_f32_bf16 v135, v127, v127\n"
    "s_waitcnt lgkmcnt(1)\n"
    "v_dot2c_f32_bf16 v134, 0x3f803f80, v128\n"
    "v_dot2c_f32_bf16 v135, v128, v128\n"
    "v_dot2c_f32_bf16 v134, 0x3f803f80, v129\n"
    "v_dot2c_f32_bf16 v135, v129, v129\n"
    "s_waitcnt lgkmcnt(0)\n"
    "v_dot2c_f32_bf16 v134, 0x3f803f80, v130\n"
    "v_dot2c_f32_bf16 v135, v130, v130\n"
    "v_dot2c_f32_bf16 v134, 0x3f803f80, v131\n"
    "v_dot2c_f32_bf16 v135, v131, v131\n"
    "s_nop 2\n"
    "v_permlane32_swap_b32 v134, v135\n"
    "v_add_f32 v134, v134, v135\n"
    "ds_write_b32 v143, v134 offset:1024\n"
    "s_waitcnt lgkmcnt(0)\n"
    "s_barrier\n"
    "ds_read2st64_b32 v[136:137], v144 offset0:4 offset1:5\n"
    "ds_read2st64_b32 v[138:139], v144 offset0:6 offset1:7\n"
    "v_mov_b32 v150, 0x3727c5ac\n"
    "s_waitcnt lgkmcnt(1)\n"
    "v_add_f32 v136, v136, v137\n"
    "s_waitcnt lgkmcnt(0)\n"
    "v_add_f32 v138, v138, v139\n"
    "v_add_f32 v136, v136, v138\n"
    "v_mov_b32 v137, v136\n"
    "s_nop 1\n"
    "v_permlane32_swap_b32 v136, v137\n"
    "v_mul_f32 v148, 0xbb124925, v136\n"
    "v_fmac_f32 v150, 0x3b124925, v137\n"
    "v_fma_f32 v150, -v148, v148, v150\n"
    "v_max_f32 v150, 0x3727c5ac, v150\n"
    "buffer_store_dwordx4 v[148:151], v145, s[52:55], s70 offen sc1\n"
    "s_add_u32 s70, s70, 16384\n"
    "s_add_u32 s69, s69, 917504\n"
    "s_branch Lend_%=\n"
    "Lslow_ux0_%=:\n"
    "s_waitcnt vmcnt(0) lgkmcnt(0)\n"
    "s_nop 11\n"
    "Lslowl_ux0_%=:\n"
    "s_sub_u32 s61, s61, 1\n"
    "s_cbranch_scc1 Lslowx_ux0_%=\n"
    "s_lshl_b32 s16, s68, 6\n"
    "s_add_u32 s16, s16, 98304\n"
    "buffer_load_dwordx4 v[206:209], v184, s[52:55], s16 offen sc1\n"
    "s_add_u32 s16, s16, 917504\n"
    "buffer_load_dwordx4 v[210:213], v184, s[52:55], s16 offen sc1\n"
    "s_add_u32 s16, s16, 917504\n"
    "buffer_load_dwordx4 v[214:217], v184, s[52:55], s16 offen sc1\n"
    "s_add_u32 s16, s16, 917504\n"
    "buffer_load_dwordx4 v[218:221], v184, s[52:55], s16 offen sc1\n"
    "s_waitcnt vmcnt(2)\n"
    "v_min3_u32 v170, v207, v209, v211\n"
    "v_max3_u32 v171, v207, v209, v211\n"
    "s_waitcnt vmcnt(1)\n"
    "v_min3_u32 v170, v170, v213, v215\n"
    "v_max3_u32 v171, v171, v213, v215\n"
    "s_waitcnt vmcnt(0)\n"
    "v_min3_u32 v170, v170, v217, v219\n"
    "v_max3_u32 v171, v171, v217, v219\n"
    "v_min_u32 v170, v170, v221\n"
    "v_max_u32 v171, v171, v221\n"
    "v_xor_b32 v170, s60, v170\n"
    "v_xor_b32 v171, s60, v171\n"
    "v_or_b32 v170, v170, v171\n"
    "v_cmp_ne_u32_e64 vcc, 0, v170\n"
    "s_cbranch_vccnz Lslowl_ux0_%=\n"
    "s_branch Lslowy_ux0_%=\n"
    "Lslowx_ux0_%=:\n"
    "s_mov_b32 s61, 0\n"
    "Lslowy_ux0_%=:\n"
    "s_waitcnt vmcnt(0) lgkmcnt(0)\n"
    "s_nop 11\n"
    "s_branch Lback_ux0_%=\n"
    "Lslow_ux1_%=:\n"
    "s_waitcnt vmcnt(0) lgkmcnt(0)\n"
    "s_nop 11\n"
    "Lslowl_ux1_%=:\n"
    "s_sub_u32 s61, s61, 1\n"
    "s_cbranch_scc1 Lslowx_ux1_%=\n"
    "s_mov_b32 s16, 32768\n"
    "buffer_load_dwordx4 v[98:101], v230, s[52:55], s16 offen sc1\n"
    "s_mov_b32 s16, 36864\n"
    "buffer_load_dwordx4 v[102:105], v230, s[52:55], s16 offen sc1\n"
    "s_mov_b32 s16, 40960\n"
    "buffer_load_dwordx4 v[106:109], v230, s[52:55], s16 offen sc1\n"
    "s_mov_b32 s16, 45056\n"
    "buffer_load_dwordx4 v[110:113], v230, s[52:55], s16 offen sc1\n"
    "s_mov_b32 s16, 49152\n"
    "buffer_load_dwordx4 v[114:117], v230, s[52:55], s16 offen sc1\n"
    "s_mov_b32 s16, 53248\n"
    "buffer_load_dwordx4 v[118:121], v230, s[52:55], s16 offen sc1\n"
    "s_mov_b32 s16, 57344\n"
    "buffer_load_dwordx4 v[122:125], v230, s[52:55], s16 offen sc1\n"
    "s_mov_b32 s16, 61440\n"
    "buffer_load_dwordx4 v[126:129], v230, s[52:55], s16 offen sc1\n"
    "s_waitcnt vmcnt(6)\n"
    "v_min3_u32 v170, v99, v101, v103\n"
    "v_max3_u32 v171, v99, v101, v103\n"
    "s_waitcnt vmcnt(5)\n"
    "v_min3_u32 v170, v170, v105, v107\n"
    "v_max3_u32 v171, v171, v105, v107\n"
    "s_waitcnt vmcnt(4)\n"
    "v_min3_u32 v170, v170, v109, v111\n"
    "v_max3_u32 v171, v171, v109, v111\n"
    "s_waitcnt vmcnt(3)\n"
    "v_min3_u32 v170, v170, v113, v115\n"
    "v_max3_u32 v171, v171, v113, v115\n"
    "s_waitcnt vmcnt(2)\n"
    "v_min3_u32 v170, v170, v117, v119\n"
    "v_max3_u32 v171, v171, v117, v119\n"
    "s_waitcnt vmcnt(1)\n"
    "v_min3_u32 v170, v170, v121, v123\n"
    "v_max3_u32 v171, v171, v121, v123\n"
    "s_waitcnt vmcnt(0)\n"
    "v_min3_u32 v170, v170, v125, v127\n"
    "v_max3_u32 v171, v171, v125, v127\n"
    "v_min_u32 v170, v170, v129\n"
    "v_max_u32 v171, v171, v129\n"
    "v_xor_b32 v170, s60, v170\n"
    "v_xor_b32 v171, s60, v171\n"
    "v_or_b32 v170, v170, v171\n"
    "v_cmp_ne_u32 vcc, 0, v170\n"
    "s_cbranch_vccnz Lslowl_ux1_%=\n"
    "s_branch Lslowy_ux1_%=\n"
    "Lslowx_ux1_%=:\n"
    "s_mov_b32 s61, 0\n"
    "Lslowy_ux1_%=:\n"
    "s_waitcnt vmcnt(0) lgkmcnt(0)\n"
    "s_nop 11\n"
    "s_branch Lback_ux1_%=\n"
    "Lslow_f13_%=:\n"
    "s_waitcnt vmcnt(0) lgkmcnt(0)\n"
    "s_nop 11\n"
    "Lslowl_f13_%=:\n"
    "s_sub_u32 s61, s61, 1\n"
    "s_cbranch_scc1 Lslowx_f13_%=\n"
    "s_sleep 2\n"
    "buffer_load_dword v240, v185, s[52:55], s14 offen sc1\n"
    "s_waitcnt vmcnt(0)\n"
    "v_cmp_ne_u32 vcc, s60, v240\n"
    "s_cbranch_vccnz Lslowl_f13_%=\n"
    "s_branch Lslowy_f13_%=\n"
    "Lslowx_f13_%=:\n"
    "s_mov_b32 s61, 0\n"
    "Lslowy_f13_%=:\n"
    "s_waitcnt vmcnt(0) lgkmcnt(0)\n"
    "s_nop 11\n"
    "s_branch Lback_f13_%=\n"
    "Lslow_tile0_%=:\n"
    "s_waitcnt vmcnt(0) lgkmcnt(0)\n"
    "s_nop 11\n"
    "Lslowl_tile0_%=:\n"
    "s_sub_u32 s61, s61, 1\n"
    "s_cbranch_scc1 Lslowx_tile0_%=\n"
    "buffer_load_dwordx4 v[102:105], v233, s[52:55], s15 offen offset:0 sc1\n"
    "buffer_load_dwordx4 v[106:109], v233, s[52:55], s15 offen offset:1024 sc1\n"
    "buffer_load_dwordx4 v[110:113], v233, s[52:55], s15 offen offset:2048 sc1\n"
    "buffer_load_dwordx4 v[114:117], v233, s[52:55], s15 offen offset:3072 sc1\n"
    "buffer_load_dwordx4 v[118:121], v241, s[52:55], s15 offen offset:0 sc1\n"
    "buffer_load_dwordx4 v[122:125], v241, s[52:55], s15 offen offset:1024 sc1\n"
    "buffer_load_dwordx4 v[126:129], v241, s[52:55], s15 offen offset:2048 sc1\n"
    "s_waitcnt vmcnt(6)\n"
    "v_max3_u32 v234, v102, v103, v104\n"
    "s_waitcnt vmcnt(5)\n"
    "v_max3_u32 v234, v234, v105, v106\n"
    "v_max3_u32 v234, v234, v107, v108\n"
    "s_waitcnt vmcnt(4)\n"
    "v_max3_u32 v234, v234, v109, v110\n"
    "v_max3_u32 v234, v234, v111, v112\n"
    "s_waitcnt vmcnt(3)\n"
    "v_max3_u32 v234, v234, v113, v114\n"
    "v_max3_u32 v234, v234, v115, v116\n"
    "s_waitcnt vmcnt(2)\n"
    "v_max3_u32 v234, v234, v117, v118\n"
    "v_max3_u32 v234, v234, v119, v120\n"
    "s_waitcnt vmcnt(1)\n"
    "v_max3_u32 v234, v234, v121, v122\n"
    "v_max3_u32 v234, v234, v123, v124\n"
    "s_waitcnt vmcnt(0)\n"
    "v_max3_u32 v234, v234, v125, v126\n"
    "v_max3_u32 v234, v234, v127, v128\n"
    "v_max_u32 v234, v234, v129\n"
    "v_cmp_eq_u32_e64 vcc, -1, v234\n"
    "s_cbranch_vccnz Lslowl_tile0_%=\n"
    "s_branch Lslowy_tile0_%=\n"
    "Lslowx_tile0_%=:\n"
    "s_mov_b32 s61, 0\n"
    "Lslowy_tile0_%=:\n"
    "s_waitcnt vmcnt(0) lgkmcnt(0)\n"
    "s_nop 11\n"
    "s_branch Lback_tile0_%=\n"
    "Lslow_sg_%=:\n"
    "s_waitcnt vmcnt(0) lgkmcnt(0)\n"
    "s_nop 11\n"
    "Lslowl_sg_%=:\n"
    "s_sub_u32 s61, s61, 1\n"
    "s_cbranch_scc1 Lslowx_sg_%=\n"
    "s_add_u32 s67, s27, 14336\n"
    "buffer_load_dwordx2 v[214:215], v218, s[52:55], s27 offen sc1\n"
    "buffer_load_dwordx2 v[216:217], v218, s[52:55], s67 offen sc1\n"
    "s_waitcnt vmcnt(1)\n"
    "v_xor_b32 v252, s60, v215\n"
    "s_waitcnt vmcnt(0)\n"
    "v_xor_b32 v253, s60, v217\n"
    "v_or_b32 v252, v252, v253\n"
    "v_cmp_ne_u32 vcc, 0, v252\n"
    "s_cbranch_vccnz Lslowl_sg_%=\n"
    "s_branch Lslowy_sg_%=\n"
    "Lslowx_sg_%=:\n"
    "s_mov_b32 s61, 0\n"
    "Lslowy_sg_%=:\n"
    "s_waitcnt vmcnt(0) lgkmcnt(0)\n"
    "s_nop 11\n"
    "s_branch Lback_sg_%=\n"
    "Lslow_st0_%=:\n"
    "s_waitcnt vmcnt(0) lgkmcnt(0)\n"
    "s_nop 11\n"
    "Lslowl_st0_%=:\n"
    "s_sub_u32 s61, s61, 1\n"
    "s_cbranch_scc1 Lslowx_st0_%=\n"
    "buffer_load_dwordx4 v[252:255], v1, s[52:55], s36 offen sc1\n"
    "s_waitcnt vmcnt(0)\n"
    "v_xor_b32 v243, s60, v253\n"
    "v_xor_b32 v228, s60, v255\n"
    "v_or_b32 v243, v243, v228\n"
    "v_cmp_ne_u32 vcc, 0, v243\n"
    "s_cbranch_vccnz Lslowl_st0_%=\n"
    "s_branch Lslowy_st0_%=\n"
    "Lslowx_st0_%=:\n"
    "s_mov_b32 s61, 0\n"
    "Lslowy_st0_%=:\n"
    "s_waitcnt vmcnt(0) lgkmcnt(0)\n"
    "s_nop 11\n"
    "s_branch Lback_st0_%=\n"
    "Lslow_f2_%=:\n"
    "s_waitcnt vmcnt(0) lgkmcnt(0)\n"
    "s_nop 11\n"
    "Lslowl_f2_%=:\n"
    "s_sub_u32 s61, s61, 1\n"
    "s_cbranch_scc1 Lslowx_f2_%=\n"
    "s_sleep 2\n"
    "buffer_load_dword v240, off, s[52:55], s26 sc1\n"
    "s_waitcnt vmcnt(0)\n"
    "v_cmp_ne_u32 vcc, s60, v240\n"
    "s_cbranch_vccnz Lslowl_f2_%=\n"
    "s_branch Lslowy_f2_%=\n"
    "Lslowx_f2_%=:\n"
    "s_mov_b32 s61, 0\n"
    "Lslowy_f2_%=:\n"
    "s_waitcnt vmcnt(0) lgkmcnt(0)\n"
    "s_nop 11\n"
    "s_branch Lback_f2_%=\n"
    "Lslow_tile1_%=:\n"
    "s_waitcnt vmcnt(0) lgkmcnt(0)\n"
    "s_nop 11\n"
    "Lslowl_tile1_%=:\n"
    "s_sub_u32 s61, s61, 1\n"
    "s_cbranch_scc1 Lslowx_tile1_%=\n"
    "buffer_load_dwordx4 v[102:105], v233, s[52:55], s15 offen offset:0 sc1\n"
    "buffer_load_dwordx4 v[106:109], v233, s[52:55], s15 offen offset:1024 sc1\n"
    "buffer_load_dwordx4 v[110:113], v233, s[52:55], s15 offen offset:2048 sc1\n"
    "buffer_load_dwordx4 v[114:117], v233, s[52:55], s15 offen offset:3072 sc1\n"
    "buffer_load_dwordx4 v[118:121], v241, s[52:55], s15 offen offset:0 sc1\n"
    "buffer_load_dwordx4 v[122:125], v241, s[52:55], s15 offen offset:1024 sc1\n"
    "buffer_load_dwordx4 v[126:129], v241, s[52:55], s15 offen offset:2048 sc1\n"
    "s_waitcnt vmcnt(6)\n"
    "v_max3_u32 v234, v102, v103, v104\n"
    "s_waitcnt vmcnt(5)\n"
    "v_max3_u32 v234, v234, v105, v106\n"
    "v_max3_u32 v234, v234, v107, v108\n"
    "s_waitcnt vmcnt(4)\n"
    "v_max3_u32 v234, v234, v109, v110\n"
    "v_max3_u32 v234, v234, v111, v112\n"
    "s_waitcnt vmcnt(3)\n"
    "v_max3_u32 v234, v234, v113, v114\n"
    "v_max3_u32 v234, v234, v115, v116\n"
    "s_waitcnt vmcnt(2)\n"
    "v_max3_u32 v234, v234, v117, v118\n"
    "v_max3_u32 v234, v234, v119, v120\n"
    "s_waitcnt vmcnt(1)\n"
    "v_max3_u32 v234, v234, v121, v122\n"
    "v_max3_u32 v234, v234, v123, v124\n"
    "s_waitcnt vmcnt(0)\n"
    "v_max3_u32 v234, v234, v125, v126\n"
    "v_max3_u32 v234, v234, v127, v128\n"
    "v_max_u32 v234, v234, v129\n"
    "v_cmp_eq_u32_e64 vcc, -1, v234\n"
    "s_cbranch_vccnz Lslowl_tile1_%=\n"
    "s_branch Lslowy_tile1_%=\n"
    "Lslowx_tile1_%=:\n"
    "s_mov_b32 s61, 0\n"
    "Lslowy_tile1_%=:\n"
    "s_waitcnt vmcnt(0) lgkmcnt(0)\n"
    "s_nop 11\n"
    "s_branch Lback_tile1_%=\n"
    "Lslow_st1_%=:\n"
    "s_waitcnt vmcnt(0) lgkmcnt(0)\n"
    "s_nop 11\n"
    "Lslowl_st1_%=:\n"
    "s_sub_u32 s61, s61, 1\n"
    "s_cbranch_scc1 Lslowx_st1_%=\n"
    "buffer_load_dwordx4 v[252:255], v1, s[52:55], s36 offen sc1\n"
    "s_waitcnt vmcnt(0)\n"
    "v_xor_b32 v244, s60, v253\n"
    "v_xor_b32 v229, s60, v255\n"
    "v_or_b32 v244, v244, v229\n"
    "v_cmp_ne_u32_e64 vcc, 0, v244\n"
    "s_cbranch_vccnz Lslowl_st1_%=\n"
    "s_branch Lslowy_st1_%=\n"
    "Lslowx_st1_%=:\n"
    "s_mov_b32 s61, 0\n"
    "Lslowy_st1_%=:\n"
    "s_waitcnt vmcnt(0) lgkmcnt(0)\n"
    "s_nop 11\n"
    "s_branch Lback_st1_%=\n"
    "Lslow_tile2_%=:\n"
    "s_waitcnt vmcnt(0) lgkmcnt(0)\n"
    "s_nop 11\n"
    "Lslowl_tile2_%=:\n"
    "s_sub_u32 s61, s61, 1\n"
    "s_cbranch_scc1 Lslowx_tile2_%=\n"
    "buffer_load_dwordx4 v[102:105], v233, s[52:55], s15 offen offset:0 sc1\n"
    "buffer_load_dwordx4 v[106:109], v233, s[52:55], s15 offen offset:1024 sc1\n"
    "buffer_load_dwordx4 v[110:113], v233, s[52:55], s15 offen offset:2048 sc1\n"
    "buffer_load_dwordx4 v[114:117], v233, s[52:55], s15 offen offset:3072 sc1\n"
    "buffer_load_dwordx4 v[118:121], v241, s[52:55], s15 offen offset:0 sc1\n"
    "buffer_load_dwordx4 v[122:125], v241, s[52:55], s15 offen offset:1024 sc1\n"
    "buffer_load_dwordx4 v[126:129], v241, s[52:55], s15 offen offset:2048 sc1\n"
    "s_waitcnt vmcnt(6)\n"
    "v_max3_u32 v234, v102, v103, v104\n"
    "s_waitcnt vmcnt(5)\n"
    "v_max3_u32 v234, v234, v105, v106\n"
    "v_max3_u32 v234, v234, v107, v108\n"
    "s_waitcnt vmcnt(4)\n"
    "v_max3_u32 v234, v234, v109, v110\n"
    "v_max3_u32 v234, v234, v111, v112\n"
    "s_waitcnt vmcnt(3)\n"
    "v_max3_u32 v234, v234, v113, v114\n"
    "v_max3_u32 v234, v234, v115, v116\n"
    "s_waitcnt vmcnt(2)\n"
    "v_max3_u32 v234, v234, v117, v118\n"
    "v_max3_u32 v234, v234, v119, v120\n"
    "s_waitcnt vmcnt(1)\n"
    "v_max3_u32 v234, v234, v121, v122\n"
    "v_max3_u32 v234, v234, v123, v124\n"
    "s_waitcnt vmcnt(0)\n"
    "v_max3_u32 v234, v234, v125, v126\n"
    "v_max3_u32 v234, v234, v127, v128\n"
    "v_max_u32 v234, v234, v129\n"
    "v_cmp_eq_u32_e64 vcc, -1, v234\n"
    "s_cbranch_vccnz Lslowl_tile2_%=\n"
    "s_branch Lslowy_tile2_%=\n"
    "Lslowx_tile2_%=:\n"
    "s_mov_b32 s61, 0\n"
    "Lslowy_tile2_%=:\n"
    "s_waitcnt vmcnt(0) lgkmcnt(0)\n"
    "s_nop 11\n"
    "s_branch Lback_tile2_%=\n"
    "Lslow_st2_%=:\n"
    "s_waitcnt vmcnt(0) lgkmcnt(0)\n"
    "s_nop 11\n"
    "Lslowl_st2_%=:\n"
    "s_sub_u32 s61, s61, 1\n"
    "s_cbranch_scc1 Lslowx_st2_%=\n"
    "buffer_load_dwordx4 v[252:255], v1, s[52:55], s36 offen sc1\n"
    "s_waitcnt vmcnt(0)\n"
    "v_xor_b32 v243, s60, v253\n"
    "v_xor_b32 v228, s60, v255\n"
    "v_or_b32 v243, v243, v228\n"
    "v_cmp_ne_u32_e64 vcc, 0, v243\n"
    "s_cbranch_vccnz Lslowl_st2_%=\n"
    "s_branch Lslowy_st2_%=\n"
    "Lslowx_st2_%=:\n"
    "s_mov_b32 s61, 0\n"
    "Lslowy_st2_%=:\n"
    "s_waitcnt vmcnt(0) lgkmcnt(0)\n"
    "s_nop 11\n"
    "s_branch Lback_st2_%=\n"
    "Lslow_tile3_%=:\n"
    "s_waitcnt vmcnt(0) lgkmcnt(0)\n"
    "s_nop 11\n"
    "Lslowl_tile3_%=:\n"
    "s_sub_u32 s61, s61, 1\n"
    "s_cbranch_scc1 Lslowx_tile3_%=\n"
    "buffer_load_dwordx4 v[102:105], v233, s[52:55], s15 offen offset:0 sc1\n"
    "buffer_load_dwordx4 v[106:109], v233, s[52:55], s15 offen offset:1024 sc1\n"
    "buffer_load_dwordx4 v[110:113], v233, s[52:55], s15 offen offset:2048 sc1\n"
    "buffer_load_dwordx4 v[114:117], v233, s[52:55], s15 offen offset:3072 sc1\n"
    "buffer_load_dwordx4 v[118:121], v241, s[52:55], s15 offen offset:0 sc1\n"
    "buffer_load_dwordx4 v[122:125], v241, s[52:55], s15 offen offset:1024 sc1\n"
    "buffer_load_dwordx4 v[126:129], v241, s[52:55], s15 offen offset:2048 sc1\n"
    "s_waitcnt vmcnt(6)\n"
    "v_max3_u32 v234, v102, v103, v104\n"
    "s_waitcnt vmcnt(5)\n"
    "v_max3_u32 v234, v234, v105, v106\n"
    "v_max3_u32 v234, v234, v107, v108\n"
    "s_waitcnt vmcnt(4)\n"
    "v_max3_u32 v234, v234, v109, v110\n"
    "v_max3_u32 v234, v234, v111, v112\n"
    "s_waitcnt vmcnt(3)\n"
    "v_max3_u32 v234, v234, v113, v114\n"
    "v_max3_u32 v234, v234, v115, v116\n"
    "s_waitcnt vmcnt(2)\n"
    "v_max3_u32 v234, v234, v117, v118\n"
    "v_max3_u32 v234, v234, v119, v120\n"
    "s_waitcnt vmcnt(1)\n"
    "v_max3_u32 v234, v234, v121, v122\n"
    "v_max3_u32 v234, v234, v123, v124\n"
    "s_waitcnt vmcnt(0)\n"
    "v_max3_u32 v234, v234, v125, v126\n"
    "v_max3_u32 v234, v234, v127, v128\n"
    "v_max_u32 v234, v234, v129\n"
    "v_cmp_eq_u32 vcc, -1, v234\n"
    "s_cbranch_vccnz Lslowl_tile3_%=\n"
    "s_branch Lslowy_tile3_%=\n"
    "Lslowx_tile3_%=:\n"
    "s_mov_b32 s61, 0\n"
    "Lslowy_tile3_%=:\n"
    "s_waitcnt vmcnt(0) lgkmcnt(0)\n"
    "s_nop 11\n"
    "s_branch Lback_tile3_%=\n"
    "Lslow_fc_%=:\n"
    "s_waitcnt vmcnt(0) lgkmcnt(0)\n"
    "s_nop 11\n"
    "Lslowl_fc_%=:\n"
    "s_sub_u32 s61, s61, 1\n"
    "s_cbranch_scc1 Lslowx_fc_%=\n"
    "s_sleep 2\n"
    "buffer_load_dword v240, v234, s[52:55], s67 offen sc1\n"
    "s_waitcnt vmcnt(0)\n"
    "v_cmp_ne_u32 vcc, s60, v240\n"
    "s_cbranch_vccnz Lslowl_fc_%=\n"
    "s_branch Lslowy_fc_%=\n"
    "Lslowx_fc_%=:\n"
    "s_mov_b32 s61, 0\n"
    "Lslowy_fc_%=:\n"
    "s_waitcnt vmcnt(0) lgkmcnt(0)\n"
    "s_nop 11\n"
    "s_branch Lback_fc_%=\n"
    "Lslow_st3_%=:\n"
    "s_waitcnt vmcnt(0) lgkmcnt(0)\n"
    "s_nop 11\n"
    "Lslowl_st3_%=:\n"
    "s_sub_u32 s61, s61, 1\n"
    "s_cbranch_scc1 Lslowx_st3_%=\n"
    "buffer_load_dwordx4 v[252:255], v1, s[52:55], s36 offen sc1\n"
    "s_waitcnt vmcnt(0)\n"
    "v_xor_b32 v244, s60, v253\n"
    "v_xor_b32 v229, s60, v255\n"
    "v_or_b32 v244, v244, v229\n"
    "v_cmp_ne_u32 vcc, 0, v244\n"
    "s_cbranch_vccnz Lslowl_st3_%=\n"
    "s_branch Lslowy_st3_%=\n"
    "Lslowx_st3_%=:\n"
    "s_mov_b32 s61, 0\n"
    "Lslowy_st3_%=:\n"
    "s_waitcnt vmcnt(0) lgkmcnt(0)\n"
    "s_nop 11\n"
    "s_branch Lback_st3_%=\n"
    "Lslow_res_%=:\n"
    "s_waitcnt vmcnt(0) lgkmcnt(0)\n"
    "s_nop 11\n"
    "Lslowl_res_%=:\n"
    "s_sub_u32 s61, s61, 1\n"
    "s_cbranch_scc1 Lslowx_res_%=\n"
    "s_mul_i32 s39, s11, 7\n"
    "s_add_u32 s39, s39, s10\n"
    "s_lshl_b32 s39, s39, 12\n"
    "s_add_u32 s39, s39, 8613888\n"
    "buffer_load_dwordx4 v[102:105], v230, s[52:55], s39 offen sc1\n"
    "s_mul_i32 s39, s12, 7\n"
    "s_add_u32 s39, s39, s10\n"
    "s_lshl_b32 s39, s39, 12\n"
    "s_add_u32 s39, s39, 8613888\n"
    "buffer_load_dwordx4 v[106:109], v230, s[52:55], s39 offen sc1\n"
    "s_waitcnt vmcnt(1)\n"
    "v_max3_u32 v234, v102, v103, v104\n"
    "s_waitcnt vmcnt(0)\n"
    "v_max3_u32 v234, v234, v105, v106\n"
    "v_max3_u32 v234, v234, v107, v108\n"
    "v_max_u32 v234, v234, v109\n"
    "v_cmp_eq_u32 vcc, -1, v234\n"
    "s_cbranch_vccnz Lslowl_res_%=\n"
    "s_branch Lslowy_res_%=\n"
    "Lslowx_res_%=:\n"
    "s_mov_b32 s61, 0\n"
    "Lslowy_res_%=:\n"
    "s_waitcnt vmcnt(0) lgkmcnt(0)\n"
    "s_nop 11\n"
    "s_branch Lback_res_%=\n"
    "Lslow_fh_%=:\n"
    "s_waitcnt vmcnt(0) lgkmcnt(0)\n"
    "s_nop 11\n"
    "Lslowl_fh_%=:\n"
    "s_sub_u32 s61, s61, 1\n"
    "s_cbranch_scc1 Lslowx_fh_%=\n"
    "s_sleep 2\n"
    "buffer_load_dword v240, v232, s[52:55], s67 offen sc1\n"
    "s_waitcnt vmcnt(0)\n"
    "v_cmp_ne_u32 vcc, s60, v240\n"
    "s_cbranch_vccnz Lslowl_fh_%=\n"
    "s_branch Lslowy_fh_%=\n"
    "Lslowx_fh_%=:\n"
    "s_mov_b32 s61, 0\n"
    "Lslowy_fh_%=:\n"
    "s_waitcnt vmcnt(0) lgkmcnt(0)\n"
    "s_nop 11\n"
    "s_branch Lback_fh_%=\n"
    "Lslow_probe_%=:\n"
    "s_waitcnt vmcnt(0) lgkmcnt(0)\n"
    "s_nop 11\n"
    "Lslowl_probe_%=:\n"
    "s_sub_u32 s61, s61, 1\n"
    "s_cbranch_scc1 Lslowx_probe_%=\n"
    "buffer_load_dword v240, v125, s[52:55], s15 offen sc1\n"
    "s_waitcnt vmcnt(0)\n"
    "v_cmp_eq_u32_e64 vcc, -1, v240\n"
    "s_cbranch_vccnz Lslowl_probe_%=\n"
    "s_branch Lslowy_probe_%=\n"
    "Lslowx_probe_%=:\n"
    "s_mov_b32 s61, 0\n"
    "Lslowy_probe_%=:\n"
    "s_waitcnt vmcnt(0) lgkmcnt(0)\n"
    "s_nop 11\n"
    "s_branch Lback_probe_%=\n"
    "Lslow_late_%=:\n"
    "s_waitcnt vmcnt(0) lgkmcnt(0)\n"
    "s_nop 11\n"
    "s_sub_u32 s61, s61, 1\n"
    "s_cbranch_scc1 Lslowx_late_%=\n"
    "s_add_u32 s37, s38, s28\n"
    "buffer_load_dwordx4 a[60:63], v233, s[52:55], s37 offen offset:0 sc1\n"
    "buffer_load_dwordx4 a[64:67], v233, s[52:55], s37 offen offset:1024 sc1\n"
    "buffer_load_dwordx4 a[68:71], v233, s[52:55], s37 offen offset:2048 sc1\n"
    "buffer_load_dwordx4 a[72:75], v233, s[52:55], s37 offen offset:3072 sc1\n"
    "s_add_u32 s37, s38, s29\n"
    "buffer_load_dwordx4 a[76:79], v233, s[52:55], s37 offen offset:0 sc1\n"
    "buffer_load_dwordx4 a[80:83], v233, s[52:55], s37 offen offset:1024 sc1\n"
    "buffer_load_dwordx4 a[84:87], v233, s[52:55], s37 offen offset:2048 sc1\n"
    "buffer_load_dwordx4 a[88:91], v233, s[52:55], s37 offen offset:3072 sc1\n"
    "s_add_u32 s37, s38, s30\n"
    "buffer_load_dwordx4 a[92:95], v233, s[52:55], s37 offen offset:0 sc1\n"
    "buffer_load_dwordx4 a[96:99], v233, s[52:55], s37 offen offset:1024 sc1\n"
    "buffer_load_dwordx4 a[100:103], v233, s[52:55], s37 offen offset:2048 sc1\n"
    "buffer_load_dwordx4 a[104:107], v233, s[52:55], s37 offen offset:3072 sc1\n"
    "s_add_u32 s37, s38, s31\n"
    "buffer_load_dwordx4 a[108:111], v233, s[52:55], s37 offen offset:0 sc1\n"
    "buffer_load_dwordx4 a[112:115], v233, s[52:55], s37 offen offset:1024 sc1\n"
    "buffer_load_dwordx4 a[116:119], v233, s[52:55], s37 offen offset:2048 sc1\n"
    "buffer_load_dwordx4 a[120:123], v233, s[52:55], s37 offen offset:3072 sc1\n"
    "s_add_u32 s37, s38, s32\n"
    "buffer_load_dwordx4 a[124:127], v233, s[52:55], s37 offen offset:0 sc1\n"
    "buffer_load_dwordx4 a[128:131], v233, s[52:55], s37 offen offset:1024 sc1\n"
    "buffer_load_dwordx4 a[132:135], v233, s[52:55], s37 offen offset:2048 sc1\n"
    "buffer_load_dwordx4 a[136:139], v233, s[52:55], s37 offen offset:3072 sc1\n"
    "s_add_u32 s37, s38, s33\n"
    "buffer_load_dwordx4 a[140:143], v233, s[52:55], s37 offen offset:0 sc1\n"
    "buffer_load_dwordx4 a[144:147], v233, s[52:55], s37 offen offset:1024 sc1\n"
    "buffer_load_dwordx4 a[148:151], v233, s[52:55], s37 offen offset:2048 sc1\n"
    "buffer_load_dwordx4 a[152:155], v233, s[52:55], s37 offen offset:3072 sc1\n"
    "s_add_u32 s37, s38, s34\n"
    "buffer_load_dwordx4 a[156:159], v233, s[52:55], s37 offen offset:0 sc1\n"
    "buffer_load_dwordx4 a[160:163], v233, s[52:55], s37 offen offset:1024 sc1\n"
    "buffer_load_dwordx4 a[164:167], v233, s[52:55], s37 offen offset:2048 sc1\n"
    "buffer_load_dwordx4 a[168:171], v233, s[52:55], s37 offen offset:3072 sc1\n"
    "s_waitcnt vmcnt(27)\n"
    "v_mfma_f32_32x32x16_bf16 v[162:177], v[2:5], a[60:63], 0\n"
    "v_mfma_f32_32x32x16_bf16 v[178:193], v[6:9], a[60:63], 0\n"
    "s_waitcnt vmcnt(26)\n"
    "v_mfma_f32_32x32x16_bf16 v[162:177], v[10:13], a[64:67], v[162:177]\n"
    "v_mfma_f32_32x32x16_bf16 v[178:193], v[14:17], a[64:67], v[178:193]\n"
    "s_waitcnt vmcnt(25)\n"
    "v_mfma_f32_32x32x16_bf16 v[162:177], v[18:21], a[68:71], v[162:177]\n"
    "v_mfma_f32_32x32x16_bf16 v[178:193], v[22:25], a[68:71], v[178:193]\n"
    "s_waitcnt vmcnt(24)\n"
    "v_mfma_f32_32x32x16_bf16 v[162:177], v[26:29], a[72:75], v[162:177]\n"
    "v_mfma_f32_32x32x16_bf16 v[178:193], v[30:33], a[72:75], v[178:193]\n"
    "s_waitcnt vmcnt(23)\n"
    "v_mfma_f32_32x32x16_bf16 v[162:177], v[34:37], a[76:79], v[162:177]\n"
    "v_mfma_f32_32x32x16_bf16 v[178:193], v[38:41], a[76:79], v[178:193]\n"
    "s_waitcnt vmcnt(22)\n"
    "v_mfma_f32_32x32x16_bf16 v[162:177], v[42:45], a[80:83], v[162:177]\n"
    "v_mfma_f32_32x32x16_bf16 v[178:193], v[46:49], a[80:83], v[178:193]\n"
    "s_waitcnt vmcnt(21)\n"
    "v_mfma_f32_32x32x16_bf16 v[162:177], v[50:53], a[84:87], v[162:177]\n"
    "v_mfma_f32_32x32x16_bf16 v[178:193], v[54:57], a[84:87], v[178:193]\n"
    "s_waitcnt vmcnt(20)\n"
    "v_mfma_f32_32x32x16_bf16 v[162:177], v[58:61], a[88:91], v[162:177]\n"
    "v_mfma_f32_32x32x16_bf16 v[178:193], v[62:65], a[88:91], v[178:193]\n"
    "s_waitcnt vmcnt(19)\n"
    "v_mfma_f32_32x32x16_bf16 v[162:177], v[66:69], a[92:95], v[162:177]\n"
    "v_mfma_f32_32x32x16_bf16 v[178:193], v[70:73], a[92:95], v[178:193]\n"
    "s_waitcnt vmcnt(18)\n"
    "v_mfma_f32_32x32x16_bf16 v[162:177], v[74:77], a[96:99], v[162:177]\n"
    "v_mfma_f32_32x32x16_bf16 v[178:193], v[78:81], a[96:99], v[178:193]\n"
    "s_waitcnt vmcnt(17)\n"
    "v_mfma_f32_32x32x16_bf16 v[162:177], v[82:85], a[100:103], v[162:177]\n"
    "v_mfma_f32_32x32x16_bf16 v[178:193], v[86:89], a[100:103], v[178:193]\n"
    "s_waitcnt vmcnt(16)\n"
    "v_mfma_f32_32x32x16_bf16 v[162:177], v[90:93], a[104:107], v[162:177]\n"
    "v_mfma_f32_32x32x16_bf16 v[178:193], v[94:97], a[104:107], v[178:193]\n"
    "s_waitcnt vmcnt(15)\n"
    "v_mfma_f32_32x32x16_bf16 v[162:177], v[98:101], a[108:111], v[162:177]\n"
    "v_mfma_f32_32x32x16_bf16 v[178:193], a[224:227], a[108:111], v[178:193]\n"
    "s_waitcnt vmcnt(14)\n"
    "v_mfma_f32_32x32x16_bf16 v[162:177], a[228:231], a[112:115], v[162:177]\n"
    "v_mfma_f32_32x32x16_bf16 v[178:193], a[232:235], a[112:115], v[178:193]\n"
    "s_waitcnt vmcnt(13)\n"
    "v_mfma_f32_32x32x16_bf16 v[162:177], a[236:239], a[116:119], v[162:177]\n"
    "v_mfma_f32_32x32x16_bf16 v[178:193], a[240:243], a[116:119], v[178:193]\n"
    "s_waitcnt vmcnt(12)\n"
    "v_mfma_f32_32x32x16_bf16 v[162:177], a[244:247], a[120:123], v[162:177]\n"
    "v_mfma_f32_32x32x16_bf16 v[178:193], a[248:251], a[120:123], v[178:193]\n"
    "s_waitcnt vmcnt(11)\n"
    "v_mfma_f32_32x32x16_bf16 v[162:177], a[252:255], a[124:127], v[162:177]\n"
    "v_mfma_f32_32x32x16_bf16 v[178:193], a[0:3], a[124:127], v[178:193]\n"
    "s_waitcnt vmcnt(10)\n"
    "v_mfma_f32_32x32x16_bf16 v[162:177], a[4:7], a[128:131], v[162:177]\n"
    "v_mfma_f32_32x32x16_bf16 v[178:193], a[8:11], a[128:131], v[178:193]\n"
    "s_waitcnt vmcnt(9)\n"
    "v_mfma_f32_32x32x16_bf16 v[162:177], a[12:15], a[132:135], v[162:177]\n"
    "v_mfma_f32_32x32x16_bf16 v[178:193], a[16:19], a[132:135], v[178:193]\n"
    "s_waitcnt vmcnt(8)\n"
    "v_mfma_f32_32x32x16_bf16 v[162:177], a[20:23], a[136:139], v[162:177]\n"
    "v_mfma_f32_32x32x16_bf16 v[178:193], a[24:27], a[136:139], v[178:193]\n"
    "s_waitcnt vmcnt(7)\n"
    "v_mfma_f32_32x32x16_bf16 v[162:177], a[172:175], a[140:143], v[162:177]\n"
    "v_mfma_f32_32x32x16_bf16 v[178:193], a[176:179], a[140:143], v[178:193]\n"
    "s_waitcnt vmcnt(6)\n"
    "v_mfma_f32_32x32x16_bf16 v[162:177], a[180:183], a[144:147], v[162:177]\n"
    "v_mfma_f32_32x32x16_bf16 v[178:193], a[184:187], a[144:147], v[178:193]\n"
    "s_waitcnt vmcnt(5)\n"
    "v_mfma_f32_32x32x16_bf16 v[162:177], a[188:191], a[148:151], v[162:177]\n"
    "v_mfma_f32_32x32x16_bf16 v[178:193], a[192:195], a[148:151], v[178:193]\n"
    "s_waitcnt vmcnt(4)\n"
    "v_mfma_f32_32x32x16_bf16 v[162:177], a[196:199], a[152:155], v[162:177]\n"
    "v_mfma_f32_32x32x16_bf16 v[178:193], a[200:203], a[152:155], v[178:193]\n"
    "s_waitcnt vmcnt(3)\n"
    "v_mfma_f32_32x32x16_bf16 v[162:177], a[28:31], a[156:159], v[162:177]\n"
    "v_mfma_f32_32x32x16_bf16 v[178:193], a[32:35], a[156:159], v[178:193]\n"
    "s_waitcnt vmcnt(2)\n"
    "v_mfma_f32_32x32x16_bf16 v[162:177], a[36:39], a[160:163], v[162:177]\n"
    "v_mfma_f32_32x32x16_bf16 v[178:193], a[40:43], a[160:163], v[178:193]\n"
    "s_waitcnt vmcnt(1)\n"
    "v_mfma_f32_32x32x16_bf16 v[162:177], a[44:47], a[164:167], v[162:177]\n"
    "v_mfma_f32_32x32x16_bf16 v[178:193], a[48:51], a[164:167], v[178:193]\n"
    "s_waitcnt vmcnt(0)\n"
    "v_mfma_f32_32x32x16_bf16 v[162:177], a[52:55], a[168:171], v[162:177]\n"
    "v_mfma_f32_32x32x16_bf16 v[178:193], a[56:59], a[168:171], v[178:193]\n"
    "s_nop 10\n"
    "ds_write_b128 v1, v[162:165] offset:32768\n"
    "ds_write_b128 v1, v[166:169] offset:33792\n"
    "ds_write_b128 v1, v[170:173] offset:34816\n"
    "ds_write_b128 v1, v[174:177] offset:35840\n"
    "s_nop 11\n"
    "s_branch Lg2c_%=\n"
    "Lslowx_late_%=:\n"
    "s_mov_b32 s61, 0\n"
    "s_branch Lback_late_%=\n"
    "Lslow_sprs_%=:\n"
    "s_waitcnt vmcnt(0) lgkmcnt(0)\n"
    "s_nop 11\n"
    "Lslowl_sprs_%=:\n"
    "s_sub_u32 s61, s61, 1\n"
    "s_cbranch_scc1 Lslowx_sprs_%=\n"
    "s_nop 0\n"
    "s_mov_b32 s64, 32768\n"
    "buffer_load_dwordx4 v[92:95], v230, s[52:55], s64 offen sc1\n"
    "s_mov_b32 s64, 36864\n"
    "buffer_load_dwordx4 v[96:99], v230, s[52:55], s64 offen sc1\n"
    "s_mov_b32 s64, 40960\n"
    "buffer_load_dwordx4 v[100:103], v230, s[52:55], s64 offen sc1\n"
    "s_mov_b32 s64, 45056\n"
    "buffer_load_dwordx4 v[104:107], v230, s[52:55], s64 offen sc1\n"
    "s_mov_b32 s64, 49152\n"
    "buffer_load_dwordx4 v[108:111], v230, s[52:55], s64 offen sc1\n"
    "s_mov_b32 s64, 53248\n"
    "buffer_load_dwordx4 v[112:115], v230, s[52:55], s64 offen sc1\n"
    "s_mov_b32 s64, 57344\n"
    "buffer_load_dwordx4 v[116:119], v230, s[52:55], s64 offen sc1\n"
    "s_mov_b32 s64, 61440\n"
    "buffer_load_dwordx4 v[120:123], v230, s[52:55], s64 offen sc1\n"
    "s_waitcnt vmcnt(6)\n"
    "v_min3_u32 v124, v93, v95, v97\n"
    "v_max3_u32 v125, v93, v95, v97\n"
    "s_waitcnt vmcnt(5)\n"
    "v_min3_u32 v124, v124, v99, v101\n"
    "v_max3_u32 v125, v125, v99, v101\n"
    "s_waitcnt vmcnt(4)\n"
    "v_min3_u32 v124, v124, v103, v105\n"
    "v_max3_u32 v125, v125, v103, v105\n"
    "s_waitcnt vmcnt(3)\n"
    "v_min3_u32 v124, v124, v107, v109\n"
    "v_max3_u32 v125, v125, v107, v109\n"
    "s_waitcnt vmcnt(2)\n"
    "v_min3_u32 v124, v124, v111, v113\n"
    "v_max3_u32 v125, v125, v111, v113\n"
    "s_waitcnt vmcnt(1)\n"
    "v_min3_u32 v124, v124, v115, v117\n"
    "v_max3_u32 v125, v125, v115, v117\n"
    "s_waitcnt vmcnt(0)\n"
    "v_min3_u32 v124, v124, v119, v121\n"
    "v_max3_u32 v125, v125, v119, v121\n"
    "v_min_u32 v124, v124, v123\n"
    "v_max_u32 v125, v125, v123\n"
    "v_xor_b32 v124, s60, v124\n"
    "v_xor_b32 v125, s60, v125\n"
    "v_or_b32 v124, v124, v125\n"
    "v_cmp_ne_u32 vcc, 0, v124\n"
    "s_cbranch_vccnz Lslowl_sprs_%=\n"
    "s_branch Lslowy_sprs_%=\n"
    "Lslowx_sprs_%=:\n"
    "s_mov_b32 s61, 0\n"
    "Lslowy_sprs_%=:\n"
    "s_waitcnt vmcnt(0) lgkmcnt(0)\n"
    "s_nop 11\n"
    "s_branch Lback_sprs_%=\n"
    "Lslow_sptile0_%=:\n"
    "s_waitcnt vmcnt(0) lgkmcnt(0)\n"
    "s_nop 11\n"
    "Lslowl_sptile0_%=:\n"
    "s_sub_u32 s61, s61, 1\n"
    "s_cbranch_scc1 Lslowx_sptile0_%=\n"
    "s_sleep 4\n"
    "buffer_load_dwordx4 v[76:79], v74, s[52:55], s69 offen offset:0 sc1\n"
    "buffer_load_dwordx4 v[80:83], v74, s[52:55], s69 offen offset:1024 sc1\n"
    "buffer_load_dwordx4 v[84:87], v74, s[52:55], s69 offen offset:2048 sc1\n"
    "buffer_load_dwordx4 v[88:91], v74, s[52:55], s69 offen offset:3072 sc1\n"
    "buffer_load_dwordx4 v[92:95], v140, s[52:55], s69 offen offset:0 sc1\n"
    "buffer_load_dwordx4 v[96:99], v140, s[52:55], s69 offen offset:1024 sc1\n"
    "buffer_load_dwordx4 v[100:103], v140, s[52:55], s69 offen offset:2048 sc1\n"
    "s_waitcnt vmcnt(6)\n"
    "v_max3_u32 v132, v76, v77, v78\n"
    "s_waitcnt vmcnt(5)\n"
    "v_max3_u32 v132, v132, v79, v80\n"
    "v_max3_u32 v132, v132, v81, v82\n"
    "s_waitcnt vmcnt(4)\n"
    "v_max3_u32 v132, v132, v83, v84\n"
    "v_max3_u32 v132, v132, v85, v86\n"
    "s_waitcnt vmcnt(3)\n"
    "v_max3_u32 v132, v132, v87, v88\n"
    "v_max3_u32 v132, v132, v89, v90\n"
    "s_waitcnt vmcnt(2)\n"
    "v_max3_u32 v132, v132, v91, v92\n"
    "v_max3_u32 v132, v132, v93, v94\n"
    "s_waitcnt vmcnt(1)\n"
    "v_max3_u32 v132, v132, v95, v96\n"
    "v_max3_u32 v132, v132, v97, v98\n"
    "s_waitcnt vmcnt(0)\n"
    "v_max3_u32 v132, v132, v99, v100\n"
    "v_max3_u32 v132, v132, v101, v102\n"
    "v_max_u32 v132, v132, v103\n"
    "v_cmp_eq_u32 vcc, -1, v132\n"
    "s_cbranch_vccnz Lslowl_sptile0_%=\n"
    "s_branch Lslowy_sptile0_%=\n"
    "Lslowx_sptile0_%=:\n"
    "s_mov_b32 s61, 0\n"
    "Lslowy_sptile0_%=:\n"
    "s_waitcnt vmcnt(0) lgkmcnt(0)\n"
    "s_nop 11\n"
    "s_branch Lback_sptile0_%=\n"
    "Lslow_sptile1_%=:\n"
    "s_waitcnt vmcnt(0) lgkmcnt(0)\n"
    "s_nop 11\n"
    "Lslowl_sptile1_%=:\n"
    "s_sub_u32 s61, s61, 1\n"
    "s_cbranch_scc1 Lslowx_sptile1_%=\n"
    "s_sleep 4\n"
    "buffer_load_dwordx4 v[76:79], v74, s[52:55], s69 offen offset:0 sc1\n"
    "buffer_load_dwordx4 v[80:83], v74, s[52:55], s69 offen offset:1024 sc1\n"
    "buffer_load_dwordx4 v[84:87], v74, s[52:55], s69 offen offset:2048 sc1\n"
    "buffer_load_dwordx4 v[88:91], v74, s[52:55], s69 offen offset:3072 sc1\n"
    "buffer_load_dwordx4 v[92:95], v140, s[52:55], s69 offen offset:0 sc1\n"
    "buffer_load_dwordx4 v[96:99], v140, s[52:55], s69 offen offset:1024 sc1\n"
    "buffer_load_dwordx4 v[100:103], v140, s[52:55], s69 offen offset:2048 sc1\n"
    "s_waitcnt vmcnt(6)\n"
    "v_max3_u32 v132, v76, v77, v78\n"
    "s_waitcnt vmcnt(5)\n"
    "v_max3_u32 v132, v132, v79, v80\n"
    "v_max3_u32 v132, v132, v81, v82\n"
    "s_waitcnt vmcnt(4)\n"
    "v_max3_u32 v132, v132, v83, v84\n"
    "v_max3_u32 v132, v132, v85, v86\n"
    "s_waitcnt vmcnt(3)\n"
    "v_max3_u32 v132, v132, v87, v88\n"
    "v_max3_u32 v132, v132, v89, v90\n"
    "s_waitcnt vmcnt(2)\n"
    "v_max3_u32 v132, v132, v91, v92\n"
    "v_max3_u32 v132, v132, v93, v94\n"
    "s_waitcnt vmcnt(1)\n"
    "v_max3_u32 v132, v132, v95, v96\n"
    "v_max3_u32 v132, v132, v97, v98\n"
    "s_waitcnt vmcnt(0)\n"
    "v_max3_u32 v132, v132, v99, v100\n"
    "v_max3_u32 v132, v132, v101, v102\n"
    "v_max_u32 v132, v132, v103\n"
    "v_cmp_eq_u32 vcc, -1, v132\n"
    "s_cbranch_vccnz Lslowl_sptile1_%=\n"
    "s_branch Lslowy_sptile1_%=\n"
    "Lslowx_sptile1_%=:\n"
    "s_mov_b32 s61, 0\n"
    "Lslowy_sptile1_%=:\n"
    "s_waitcnt vmcnt(0) lgkmcnt(0)\n"
    "s_nop 11\n"
    "s_branch Lback_sptile1_%=\n"
    "Lend_%=:\n"
    :
    : "s"(__builtin_amdgcn_kernarg_segment_ptr()), "s"(blockIdx.x), "v"(threadIdx.x), "s"((uint32_t)(uintptr_t)lds)
    : "v1", "v2", "v3", "v4", "v5", "v6", "v7", "v8", "v9", "v10", "v11", "v12", "v13", "v14", "v15", "v16", "v17", "v18", "v19", "v20", "v21", "v22", "v23", "v24", "v25", "v26", "v27", "v28", "v29", "v30", "v31", "v32", "v33", "v34", "v35", "v36", "v37", "v38", "v39", "v40", "v41", "v42", "v43", "v44", "v45", "v46", "v47", "v48", "v49", "v50", "v51", "v52", "v53", "v54", "v55", "v56", "v57", "v58", "v59", "v60", "v61", "v62", "v63", "v64", "v65", "v66", "v67", "v68", "v69", "v70", "v71", "v72", "v73", "v74", "v75", "v76", "v77", "v78", "v79", "v80", "v81", "v82", "v83", "v84", "v85", "v86", "v87", "v88", "v89", "v90", "v91", "v92", "v93", "v94", "v95", "v96", "v97", "v98", "v99", "v100", "v101", "v102", "v103", "v104", "v105", "v106", "v107", "v108", "v109", "v110", "v111", "v112", "v113", "v114", "v115", "v116", "v117", "v118", "v119", "v120", "v121", "v122", "v123", "v124", "v125", "v126", "v127", "v128", "v129", "v130", "v131", "v132", "v133", "v134", "v135", "v136", "v137", "v138", "v139", "v140", "v141", "v142", "v143", "v144", "v145", "v146", "v147", "v148", "v149", "v150", "v151", "v152", "v153", "v154", "v155", "v156", "v157", "v158", "v159", "v160", "v161", "v162", "v163", "v164", "v165", "v166", "v167", "v168", "v169", "v170", "v171", "v172", "v173", "v174", "v175", "v176", "v177", "v178", "v179", "v180", "v181", "v182", "v183", "v184", "v185", "v186", "v187", "v188", "v189", "v190", "v191", "v192", "v193", "v194", "v195", "v196", "v197", "v198", "v199", "v200", "v201", "v202", "v203", "v204", "v205", "v206", "v207", "v208", "v209", "v210", "v211", "v212", "v213", "v214", "v215", "v216", "v217", "v218", "v219", "v220", "v221", "v222", "v223", "v224", "v225", "v226", "v227", "v228", "v229", "v230", "v231", "v232", "v233", "v234", "v235", "v236", "v237", "v238", "v239", "v240", "v241", "v242", "v243", "v244", "v245", "v246", "v247", "v248", "v249", "v250", "v251", "v252", "v253", "v254", "v255", "a0", "a1", "a2", "a3", "a4", "a5", "a6", "a7", "a8", "a9", "a10", "a11", "a12", "a13", "a14", "a15", "a16", "a17", "a18", "a19", "a20", "a21", "a22", "a23", "a24", "a25", "a26", "a27", "a28", "a29", "a30", "a31", "a32", "a33", "a34", "a35", "a36", "a37", "a38", "a39", "a40", "a41", "a42", "a43", "a44", "a45", "a46", "a47", "a48", "a49", "a50", "a51", "a52", "a53", "a54", "a55", "a56", "a57", "a58", "a59", "a60", "a61", "a62", "a63", "a64", "a65", "a66", "a67", "a68", "a69", "a70", "a71", "a72", "a73", "a74", "a75", "a76", "a77", "a78", "a79", "a80", "a81", "a82", "a83", "a84", "a85", "a86", "a87", "a88", "a89", "a90", "a91", "a92", "a93", "a94", "a95", "a96", "a97", "a98", "a99", "a100", "a101", "a102", "a103", "a104", "a105", "a106", "a107", "a108", "a109", "a110", "a111", "a112", "a113", "a114", "a115", "a116", "a117", "a118", "a119", "a120", "a121", "a122", "a123", "a124", "a125", "a126", "a127", "a128", "a129", "a130", "a131", "a132", "a133", "a134", "a135", "a136", "a137", "a138", "a139", "a140", "a141", "a142", "a143", "a144", "a145", "a146", "a147", "a148", "a149", "a150", "a151", "a152", "a153", "a154", "a155", "a156", "a157", "a158", "a159", "a160", "a161", "a162", "a163", "a164", "a165", "a166", "a167", "a168", "a169", "a170", "a171", "a172", "a173", "a174", "a175", "a176", "a177", "a178", "a179", "a180", "a181", "a182", "a183", "a184", "a185", "a186", "a187", "a188", "a189", "a190", "a191", "a192", "a193", "a194", "a195", "a196", "a197", "a198", "a199", "a200", "a201", "a202", "a203", "a204", "a205", "a206", "a207", "a208", "a209", "a210", "a211", "a212", "a213", "a214", "a215", "a216", "a217", "a218", "a219", "a220", "a221", "a222", "a223", "a224", "a225", "a226", "a227", "a228", "a229", "a230", "a231", "a232", "a233", "a234", "a235", "a236", "a237", "a238", "a239", "a240", "a241", "a242", "a243", "a244", "a245", "a246", "a247", "a248", "a249", "a250", "a251", "a252", "a253", "a254", "a255", "s8", "s9", "s10", "s11", "s12", "s13", "s14", "s15", "s16", "s17", "s18", "s19", "s20", "s21", "s22", "s23", "s24", "s25", "s26", "s27", "s28", "s29", "s30", "s31", "s33", "s34", "s35", "s36", "s37", "s38", "s39", "s40", "s41", "s42", "s43", "s44", "s45", "s46", "s47", "s48", "s49", "s50", "s51", "s52", "s53", "s54", "s55", "s56", "s57", "s58", "s59", "s60", "s61", "s62", "s63", "s64", "s65", "s66", "s67", "s68", "s69", "s70", "s71", "s72", "s73", "s74", "s75", "s76", "s77", "s78", "s79", "s80", "s81", "s82", "s83", "s84", "s85", "s86", "s87", "s88", "s89", "s90", "s91", "s92", "s93", "s94", "s95", "s96", "s97", "s98", "s99", "vcc", "scc", "memory");
}

extern "C" void kernel_launch(void* const* d_in, const int* in_sizes, int n_in, void* d_out, int out_size, void* d_ws, size_t ws_size,
                              hipStream_t stream) {
  Args a;
  const int src[10] = {0, 1, 2, 3, 4, 5, 6, 8, 9, 10};
  for (int i = 0; i < 10; i++) { a.s[i].p = d_in[src[i]]; a.s[i].bytes = (uint32_t)in_sizes[src[i]] * 2u; a.s[i].flags = 0x20000u; }
  a.s[10].p = d_out; a.s[10].bytes = (uint32_t)out_size * 2u; a.s[10].flags = 0x20000u;
  a.s[11].p = d_ws; a.s[11].bytes = 17920000u; a.s[11].flags = 0x20000u;
  fused_block<<<256, 256, 0, stream>>>(a);
}
```
